```python
import torch

def _softmax(x, axis=-1):
    return torch.softmax(x, dim=axis)
def _gelu(x):
    return torch.nn.functional.gelu(x, approximate="tanh")
def _silu(x):
    return torch.nn.functional.silu(x)
def _sigmoid(x):
    return torch.sigmoid(x)
def _layernorm(x, axis=-1, eps=1e-5):
    m = x.mean(dim=axis, keepdim=True)
    v = x.var(dim=axis, keepdim=True, unbiased=False)
    return (x - m) / torch.sqrt(v + eps)
def _scatter_add(tgt, idx, src, axis=0):
    return tgt.clone().index_add_(axis, idx, src)
def _segment_sum(src, seg, n):
    out = torch.zeros((n,) + src.shape[1:], dtype=src.dtype)
    return out.index_add_(0, seg, src)
def _pad(x, pw):
    flat = tuple(v for pair in reversed(pw) for v in pair)
    return torch.nn.functional.pad(x, flat)

kernel_name = 'recurrent_scan__ssm_conv_s4d__mixer_channel_mlp__prompt'

def setup_inputs():

    _w = (lambda a: (lambda t: t.to(torch.bfloat16) if t.is_floating_point() else t)(torch.as_tensor(a)))
    def _A_setup():
        Bsz, L, D, N = 1, 2048, 448, 256
        return {
            "u": _w((lambda *s: torch.randn(*s, dtype=torch.bfloat16) * 0.05)(Bsz, L, D)),
            "log_A_real": _w((lambda *s: torch.randn(*s, dtype=torch.bfloat16) * 3.2)(D, N) * 0.5),
            "A_imag": _w((lambda *s: torch.randn(*s, dtype=torch.bfloat16) * 0.2)(D, N) * 3.14),
            "C_real": _w((lambda *s: torch.randn(*s, dtype=torch.bfloat16) * 0.8)(D, N) / N ** 0.5),
            "C_imag": _w((lambda *s: torch.randn(*s, dtype=torch.bfloat16) * 0.8)(D, N) / N ** 0.5),
            "log_dt": _w((lambda *s: torch.randn(*s, dtype=torch.bfloat16) * 0.2)(D) * 0.5 - 4.0),
            "D_skip": _w((lambda *s: torch.randn(*s, dtype=torch.bfloat16) * 0.2)(D)),
        }
    _A_in = _A_setup()

    def _B_setup():
        B, S, D, F = 1, 2048, 448, 1792
        return {
            "x": _w((lambda *s: torch.randn(*s, dtype=torch.bfloat16) * 0.05)(B, S, D)),
            "w1": _w((lambda *s: torch.randn(*s, dtype=torch.bfloat16) * 1.6)(D, F) * (D ** -0.5)),
            "b1": _w((lambda *s: torch.randn(*s, dtype=torch.bfloat16) * 0.8)(F)),
            "w2": _w((lambda *s: torch.randn(*s, dtype=torch.bfloat16) * 0.1)(F, D) * (F ** -0.5)),
            "b2": _w((lambda *s: torch.randn(*s, dtype=torch.bfloat16) * 0.1)(D)),
        }
    _B_in = _B_setup()

    return {
        "A_u": _A_in["u"],
        "A_log_A_real": _A_in["log_A_real"],
        "A_A_imag": _A_in["A_imag"],
        "A_C_real": _A_in["C_real"],
        "A_C_imag": _A_in["C_imag"],
        "A_log_dt": _A_in["log_dt"],
        "A_D_skip": _A_in["D_skip"],
        "B_x": _B_in["x"],
        "B_w1": _B_in["w1"],
        "B_b1": _B_in["b1"],
        "B_w2": _B_in["w2"],
        "B_b2": _B_in["b2"],
    }

def reference(A_u, A_log_A_real, A_A_imag, A_C_real, A_C_imag, A_log_dt, A_D_skip, B_x, B_w1, B_b1, B_w2, B_b2):
    def _A_ref(u, log_A_real, A_imag, C_real, C_imag, log_dt, D_skip):
        L = u.shape[1]
        dt = torch.exp(log_dt)
        A = -torch.exp(log_A_real) + 1j * A_imag
        C = (C_real + 1j * C_imag) * dt[:, None]
        pos = (lambda x, dt: x.to(dt))(torch.arange(L), log_dt.dtype)
        P = torch.exp((A * dt[:, None])[:, :, None] * pos[None, None, :])
        K = 2.0 * torch.einsum("dn,dnl->dl", C, P).real
        L2 = 2 * L
        u_t = (lambda x, *d: x.permute(*d))(u, 0, 2, 1)
        Kf = (lambda x, *a, **k: torch.fft.rfft(x.float(), *a, **k))(K, L2)
        Uf = (lambda x, *a, **k: torch.fft.rfft(x.float(), *a, **k))(u_t, L2)
        y = (lambda x, *a, **k: torch.fft.irfft(x, *a, **k).to(torch.bfloat16))(Uf * Kf[None, :, :], L2)[:, :, :L]
        y = (lambda x, *d: x.permute(*d))(y, 0, 2, 1)
        return y + u * D_skip[None, None, :]

    def _B_ref(x, w1, b1, w2, b2):
        y = _layernorm(x, axis=-1)
        h = _gelu(torch.einsum("bsd,df->bsf", y, w1) + b1)
        return torch.einsum("bsf,fd->bsd", h, w2) + b2

    x = A_u
    xn = _layernorm(x, axis=-1)
    s = _A_ref(xn, A_log_A_real, A_A_imag, A_C_real, A_C_imag, A_log_dt, A_D_skip)
    g = _silu(_B_ref(xn, B_w1, B_b1, B_w2, B_b2))
    return x + s * g
```

```cpp
#include <hip/hip_runtime.h>
#include <hip/hip_bf16.h>
#include <stdint.h>
#define EXACT_POS 0
typedef unsigned short u16;
constexpr int L = 2048, D = 448, NS = 256, F = 1792;
__device__ __forceinline__ float bf2f(u16 v) { return __uint_as_float(((unsigned)v) << 16); }
__device__ __forceinline__ float rbf(float f) { unsigned u = __float_as_uint(f); u = (u + 0x7FFF + ((u >> 16) & 1)) & 0xFFFF0000u; return __uint_as_float(u); }
__device__ __forceinline__ u16 f2bf(float f) { unsigned u = __float_as_uint(f); return (u16)((u + 0x7FFF + ((u >> 16) & 1)) >> 16); }

__global__ void k_ln(const u16* x, float* xn, float* y) {
  int row = blockIdx.x; int lane = threadIdx.x;
  float v[7]; float s = 0;
  for (int i = 0; i < 7; i++) { v[i] = bf2f(x[row * D + lane + 64 * i]); s += v[i]; }
  for (int pass = 0; pass < 2; pass++) {
    s = 0; for (int i = 0; i < 7; i++) s += v[i];
    for (int o = 32; o; o >>= 1) s += __shfl_xor(s, o);
    float mean = s / D; float q = 0;
    for (int i = 0; i < 7; i++) q += (v[i] - mean) * (v[i] - mean);
    for (int o = 32; o; o >>= 1) q += __shfl_xor(q, o);
    float var = rbf(q / D); float m = rbf(mean);
    float den = rbf(sqrtf(rbf(var + 1e-5f)));
    for (int i = 0; i < 7; i++) v[i] = rbf(rbf(v[i] - m) / den);
    float* o = pass ? y : xn;
    for (int i = 0; i < 7; i++) o[row * D + lane + 64 * i] = v[i];
  }
}
__global__ void k_kgen(const u16* lA, const u16* Ai, const u16* Cr, const u16* Ci, const u16* ldt, float* K) {
  int d = blockIdx.y; int l = blockIdx.x * 256 + threadIdx.x;
  __shared__ float sa[NS], sb[NS], scr[NS], sci[NS];
  float dt = rbf(expf(bf2f(ldt[d])));
  int n = threadIdx.x;
  sa[n] = -rbf(expf(bf2f(lA[d * NS + n]))) * dt; sb[n] = bf2f(Ai[d * NS + n]) * dt;
  scr[n] = bf2f(Cr[d * NS + n]) * dt; sci[n] = bf2f(Ci[d * NS + n]) * dt;
  __syncthreads();
  float p = EXACT_POS ? (float)l : rbf((float)l);
  float acc = 0;
  for (int n = 0; n < NS; n++) { float e = expf(sa[n] * p); float s, c; sincosf(sb[n] * p, &s, &c); acc += e * (scr[n] * c - sci[n] * s); }
  K[d * L + l] = 2.f * acc;
}
__global__ void k_conv(const float* xn, const float* K, const u16* Dk, float* s) {
  int l = blockIdx.x; int d = threadIdx.x;
  float acc = 0;
  for (int j = 0; j <= l; j++) acc += K[d * L + j] * xn[(l - j) * D + d];
  s[l * D + d] = rbf(rbf(acc) + rbf(xn[l * D + d] * bf2f(Dk[d])));
}
__global__ void k_g1(const float* y, const u16* w1, const u16* b1, float* h) {
  int l = blockIdx.y; int f = blockIdx.x * 256 + threadIdx.x;
  float acc = 0;
  for (int k = 0; k < D; k++) acc += y[l * D + k] * bf2f(w1[k * F + f]);
  float t = rbf(rbf(acc) + bf2f(b1[f]));
  float g = 0.5f * t * (1.f + tanhf(0.7978845608028654f * (t + 0.044715f * t * t * t)));
  h[l * F + f] = rbf(g);
}
__global__ void k_g2(const float* h, const u16* w2, const u16* b2, const float* s, const u16* x, u16* out) {
  int l = blockIdx.x; int d = threadIdx.x;
  float acc = 0;
  for (int k = 0; k < F; k++) acc += h[l * F + k] * bf2f(w2[k * D + d]);
  float o = rbf(rbf(acc) + bf2f(b2[d]));
  float g = rbf(o / (1.f + expf(-o)));
  out[l * D + d] = f2bf(bf2f(x[l * D + d]) + rbf(s[l * D + d] * g));
}
extern "C" void kernel_launch(void* const* d_in, const int* in_sizes, int n_in, void* d_out, int out_size, void* d_ws, size_t ws_size, hipStream_t stream) {
  float* xn = (float*)d_ws; float* y = xn + L * D; float* K = y + L * D; float* s = K + L * D; float* h = s + L * D;
  k_ln<<<L, 64, 0, stream>>>((u16*)d_in[0], xn, y);
  k_kgen<<<dim3(L / 256, D), 256, 0, stream>>>((u16*)d_in[1], (u16*)d_in[2], (u16*)d_in[3], (u16*)d_in[4], (u16*)d_in[5], K);
  k_conv<<<L, D, 0, stream>>>(xn, K, (u16*)d_in[6], s);
  k_g1<<<dim3(F / 256, L), 256, 0, stream>>>(y, (u16*)d_in[8], (u16*)d_in[9], h);
  k_g2<<<L, D, 0, stream>>>(h, (u16*)d_in[10], (u16*)d_in[11], s, (u16*)d_in[0], (u16*)d_out);
}
```

```cpp
#include <hip/hip_runtime.h>
#include <stdint.h>

typedef unsigned int u32;
typedef unsigned long long u64;
typedef unsigned short u16;
typedef u32 v4u __attribute__((ext_vector_type(4)));
typedef u32 v2u __attribute__((ext_vector_type(2)));
typedef float f32x2 __attribute__((ext_vector_type(2)));
typedef float f32x4 __attribute__((ext_vector_type(4)));
typedef float f32x16 __attribute__((ext_vector_type(16)));
typedef __bf16 bf16x2 __attribute__((ext_vector_type(2)));
typedef __bf16 bf16x8 __attribute__((ext_vector_type(8)));
typedef short s16x4 __attribute__((ext_vector_type(4)));
typedef __amdgpu_buffer_rsrc_t rsrc_t;
#define LAS __attribute__((address_space(3)))
#define GAS __attribute__((address_space(1)))
#define CAS __attribute__((address_space(4)))
#define DI __device__ __forceinline__
#define SCHED __builtin_amdgcn_sched_barrier(0)
#define PINV(x) asm volatile("" : "+v"(x))
#define PINS(x) asm volatile("" : "+s"(x))

constexpr int L = 2048, D = 448, F = 1792;
constexpr u32 WS_EPOCH = 0, WS_FX = 256, WS_FH = 1024, WS_FS = 8192, WS_STAT = 16384, WS_DBG = 65536;
constexpr u32 WS_XT = 1u << 20;
constexpr u32 WS_ST = WS_XT + L * D * 2;
constexpr u32 WS_H = WS_ST + L * D * 2;
constexpr u32 WS_END = WS_H + L * F * 2;
constexpr u32 PAGE = 8192, NSLOT = 14;
constexpr u32 LD_K = NSLOT * PAGE;
constexpr u32 KCH = 4 * 4160;
constexpr u32 LD_U = LD_K + 2 * KCH;
constexpr u32 LD_SC = LD_K;
constexpr u32 LD_PAR = LD_SC + 8 * 4352;
constexpr u32 LD_RED = LD_K;
constexpr u32 LD_BIAS = LD_U + 2 * 4608;
constexpr u32 LD_CS = LD_BIAS + 512;
constexpr u32 LDS_BYTES = 163840;
static_assert(LD_CS + 4096 <= LDS_BYTES, "lds");
static_assert(LD_PAR + 4096 <= LD_BIAS, "lds");

struct Params {
  const char *x, *logA, *Aim, *Cr, *Ci, *logdt, *Dskip, *w1, *b1, *w2, *b2;
  char *out, *ws;
};

DI float bf_lo(u32 v) { return __uint_as_float(v << 16); }
DI float bf_hi(u32 v) { return __uint_as_float(v & 0xffff0000u); }
DI u32 pk2(f32x2 f) { return __builtin_bit_cast(u32, __builtin_convertvector(f, bf16x2)); }
DI u32 pk(float lo, float hi) { return pk2((f32x2){lo, hi}); }
DI u32 pkq(float lo, float hi) { asm("" : "+v"(lo)); asm("" : "+v"(hi)); return pk2((f32x2){lo, hi}); }
DI float rbf(float x) { return bf_lo(pk(x, x)); }
DI float dot2(u32 a, u32 b, float c) { return __builtin_amdgcn_fdot2_f32_bf16(__builtin_bit_cast(bf16x2, a), __builtin_bit_cast(bf16x2, b), c, false); }
DI f32x16 mfma32(v4u a, v4u b, f32x16 c) { return __builtin_amdgcn_mfma_f32_32x32x16_bf16(__builtin_bit_cast(bf16x8, a), __builtin_bit_cast(bf16x8, b), c, 0, 0, 0); }
DI f32x4 mfma16(v4u a, v4u b, f32x4 c) { return __builtin_amdgcn_mfma_f32_16x16x32_bf16(__builtin_bit_cast(bf16x8, a), __builtin_bit_cast(bf16x8, b), c, 0, 0, 0); }
DI rsrc_t mk(const void* p, u32 bytes) { return __builtin_amdgcn_make_buffer_rsrc((void*)p, 0, bytes, 0x00020000); }
template <class T> DI T lr(u32 a) { return *(const LAS T*)(uintptr_t)a; }
template <class T> DI T lrv(u32 a) { return *(const volatile LAS T*)(uintptr_t)a; }
template <class T> DI void lw(u32 a, T v) { *(LAS T*)(uintptr_t)a = v; }
DI v2u trr(u32 a) { return __builtin_bit_cast(v2u, __builtin_amdgcn_ds_read_tr16_b64_v4i16((LAS s16x4*)(uintptr_t)a)); }
DI v4u trr2(u32 a0, u32 a1) { v2u x = trr(a0), y = trr(a1); return (v4u){x.x, x.y, y.x, y.y}; }
template <u32 LOFF> DI void dma(u32 mw, u32 voff, rsrc_t r, u32 soff) {
  asm volatile("s_add_i32 m0, %0, %4\n\ts_nop 0\n\tbuffer_load_dwordx4 %1, %2, %3 offen lds" ::"s"(mw), "v"(voff), "s"(r), "s"(soff), "i"(LOFF) : "m0", "scc");
}
DI void ld16(v4u& d, u32 voff, rsrc_t r, u32 soff) { asm volatile("buffer_load_dwordx4 %0, %1, %2, %3 offen sc1" : "=v"(d) : "v"(voff), "s"(r), "s"(soff)); }
DI void ld16p(v4u& d, u32 voff, rsrc_t r, u32 soff) { asm volatile("buffer_load_dwordx4 %0, %1, %2, %3 offen" : "=v"(d) : "v"(voff), "s"(r), "s"(soff)); }
DI void ld8(v2u& d, u32 voff, rsrc_t r, u32 soff) { asm volatile("buffer_load_dwordx2 %0, %1, %2, %3 offen sc1" : "=v"(d) : "v"(voff), "s"(r), "s"(soff)); }
DI void st16(v4u d, u32 voff, rsrc_t r, u32 soff) { asm volatile("buffer_store_dwordx4 %0, %1, %2, %3 offen sc1\n\ts_nop 1" ::"v"(d), "v"(voff), "s"(r), "s"(soff)); }
DI void st8(v2u d, u32 voff, rsrc_t r, u32 soff) { asm volatile("buffer_store_dwordx2 %0, %1, %2, %3 offen sc1" ::"v"(d), "v"(voff), "s"(r), "s"(soff)); }
#define WAITV_R(n, r) asm volatile("s_waitcnt vmcnt(" #n ")" : "+v"(r)::"memory")
#define BARV(n) asm volatile("s_waitcnt vmcnt(" #n ") lgkmcnt(0)\n\ts_barrier" ::: "memory")
#define BARL() asm volatile("s_waitcnt lgkmcnt(0)\n\ts_barrier" ::: "memory")

#define STAMP(i)

DI void poll_finish(v2u got, u32 voff, rsrc_t r, u32 soff, u32 tlo, u32 thi) {
#pragma unroll 1
  for (int spins = 0; spins < (1 << 16); ++spins) {
    if (__all(got.x == tlo && got.y == thi)) break;
    __builtin_amdgcn_s_sleep(2);
    asm volatile("buffer_load_dwordx2 %0, %1, %2, %3 offen sc1\n\ts_waitcnt vmcnt(0)" : "=&v"(got) : "v"(voff), "s"(r), "s"(soff) : "memory");
  }
}
DI f32x2 cmul(f32x2 a, f32x2 b) {
  f32x2 t;
  asm("v_pk_mul_f32 %0, %1, %2 op_sel_hi:[1,0]\n\tv_pk_fma_f32 %0, %1, %2, %0 op_sel:[1,1,0] op_sel_hi:[0,1,1] neg_lo:[0,1,0]" : "=&v"(t) : "v"(a), "v"(b));
  return t;
}
DI f32x2 cpow(float a2, float br) {
  float m = __builtin_amdgcn_exp2f(a2), f = __builtin_amdgcn_fractf(br);
  float c = m * __builtin_amdgcn_cosf(f), s = m * __builtin_amdgcn_sinf(f);
  asm("" : "+v"(c)); asm("" : "+v"(s));
  return (f32x2){c, s};
}
#define DPP_ADD(v, ctl, rm) v += __builtin_bit_cast(float, __builtin_amdgcn_update_dpp(0, __builtin_bit_cast(int, v), ctl, rm, 0xf, true))
DI void wave_sum2(float& a, float& b) {
  DPP_ADD(a, 0xB1, 0xf); DPP_ADD(b, 0xB1, 0xf); DPP_ADD(a, 0x4E, 0xf); DPP_ADD(b, 0x4E, 0xf);
  DPP_ADD(a, 0x141, 0xf); DPP_ADD(b, 0x141, 0xf); DPP_ADD(a, 0x140, 0xf); DPP_ADD(b, 0x140, 0xf);
  DPP_ADD(a, 0x142, 0xf); DPP_ADD(b, 0x142, 0xf); DPP_ADD(a, 0x143, 0xf); DPP_ADD(b, 0x143, 0xf);
}
DI void rowstat(v4u xr, u32 lane, u32 tlo, u32 thi, rsrc_t rWS, u32 row) {
  float s = 0.f, q = 0.f;
#pragma unroll
  for (int j = 0; j < 4; ++j) { s = dot2(xr[j], 0x3f803f80u, s); q = dot2(xr[j], xr[j], q); }
  wave_sum2(s, q);
  const float nm = s * (-1.f / D), var = fmaxf(q * (1.f / D) - nm * nm, 0.f);
  const float den = rbf(__builtin_amdgcn_sqrtf(rbf(rbf(var) + 1e-5f))), rd = __builtin_amdgcn_rcpf(den);
  const float ir = den * rbf(__builtin_amdgcn_sqrtf(rbf(rbf(var * rd * rd) + 1e-5f)));
  const v4u g = {tlo, pk(nm, nm - rbf(nm)), thi, pk(ir, ir - rbf(ir))};
  st16(g, lane == 63 ? 0u : 0x7ffffff0u, rWS, WS_STAT + row * 16);
}
struct GeluC { float k0, k1, ir; };
DI float gelu1(float a, const GeluC& c) {
  const float p = __builtin_fmaf(a * a, c.k1, c.k0);
  const float e = __builtin_amdgcn_exp2f(a * p);
  return a * __builtin_amdgcn_rcpf(__builtin_fmaf(e, c.ir, c.ir));
}

__global__ __launch_bounds__(512, 2) void fused_kernel(Params P) {
  __shared__ __attribute__((aligned(1024))) char lds_all[LDS_BYTES];
  const u32 tid = threadIdx.x, lane = tid & 63;
  const u32 w = __builtin_amdgcn_readfirstlane(tid >> 6);
  const u32 b = blockIdx.x, xcd = b & 7, idx = b >> 3;
  asm volatile("" ::"v"((LAS char*)lds_all));
  u32 pfdummy = 0;
  if (w == 7) {
    u64 pc; u32 len;
    asm volatile("s_getpc_b64 %0\n.Lcode_here%=:\n\ts_mov_b32 %1, fused_code_end-.Lcode_here%=" : "=s"(pc), "=s"(len));
    u32 off = (idx * 8 + lane) * 128;
    if (lane < 8 && off < len) asm volatile("global_load_dword %0, %1, %2" : "=v"(pfdummy) : "v"(off), "s"(pc));
  }
  u32 vrow = lane < 56 ? lane * 16 : 0x7ffffff0u;
  u64 epoch;
#define TAG_READY() const u32 tlo = (u32)(epoch + 1), thi = (u32)((epoch + 1) >> 32); const v2u tagv = {tlo, thi}

  if (idx < 4) {
    const u32 h = xcd * 4 + idx;
    const rsrc_t rX = mk(P.x, L * D * 2), rWS = mk(P.ws, WS_END);
    const u32 srow = (h >> 1) * 128 + 112 + (h & 1) * 8 + w;
    v4u xs, xr[8];
    ld16p(xs, vrow, rX, srow * 896);
    asm volatile("s_load_dwordx2 %0, %1, 0x0" : "=s"(epoch) : "s"(P.ws));
#pragma unroll
    for (int i = 0; i < 8; ++i) ld16p(xr[i], vrow, rX, (h * 64 + w * 8 + i) * 896);
    asm volatile("s_waitcnt vmcnt(8) lgkmcnt(0)" : "+v"(xs), "+v"(pfdummy), "+s"(epoch)::"memory");
    TAG_READY();
    rowstat(xs, lane, tlo, thi, rWS, srow);
    STAMP(0);
    float sv = 0.f, qv = 0.f;
#pragma unroll
    for (int i = 0; i < 8; ++i) {
      if (i == 0) asm volatile("s_waitcnt vmcnt(5)" : "+v"(xr[0]), "+v"(xr[1]), "+v"(xr[2]), "+v"(xr[3])::"memory");
      if (i == 4) asm volatile("s_waitcnt vmcnt(1)" : "+v"(xr[4]), "+v"(xr[5]), "+v"(xr[6]), "+v"(xr[7])::"memory");
      float s = 0.f, q = 0.f;
#pragma unroll
      for (int j = 0; j < 4; ++j) { s = dot2(xr[i][j], 0x3f803f80u, s); q = dot2(xr[i][j], xr[i][j], q); }
      wave_sum2(s, q);
      sv = lane == 48 + i ? s : sv; qv = lane == 48 + i ? q : qv;
    }
    const float mean = sv * (1.f / D), var = fmaxf(qv * (1.f / D) - mean * mean, 0.f);
    const float rdv = __builtin_amdgcn_rcpf(rbf(__builtin_amdgcn_sqrtf(rbf(rbf(var) + 1e-5f)))), mrv = -rbf(mean) * rdv;
#pragma unroll
    for (int i = 0; i < 8; ++i) {
      const float rd = __builtin_bit_cast(float, __builtin_amdgcn_readlane(__builtin_bit_cast(int, rdv), 48 + i));
      const float mr = __builtin_bit_cast(float, __builtin_amdgcn_readlane(__builtin_bit_cast(int, mrv), 48 + i));
      f32x2 rd2 = {rd, rd}, mr2 = {mr, mr};
      PINV(rd2); PINV(mr2);
      v4u o;
#pragma unroll
      for (int j = 0; j < 4; ++j) o[j] = pk2(__builtin_elementwise_fma((f32x2){bf_lo(xr[i][j]), bf_hi(xr[i][j])}, rd2, mr2));
      const u32 tok = w * 8 + i;
      if (lane < 56) lw<v4u>(tok * 896 + ((lane ^ ((tok >> 2) & 7)) << 4), o);
    }
    BARL();
    STAMP(1);
    const u32 t4 = lane & 15;
    const u32 ta = t4 * 4 * 896 + ((w ^ (t4 & 7)) << 4) + (lane >> 4) * 4, tv = (w * 4 + (lane >> 4)) * 8192 + t4 * 16;
#pragma unroll
    for (int i = 0; i < 7; ++i) {
      const u32 a = ta + i * 128;
      v4u o = {lr<u32>(a), lr<u32>(a + 896), lr<u32>(a + 1792), lr<u32>(a + 2688)};
      st16(o, tv, rWS, WS_XT + h * 256 + i * 32 * 8192);
    }
    STAMP(2);
    BARV(0);
    if (w == 0) if (lane == 0) st8(tagv, 0, rWS, WS_FX + h * 8);
    STAMP(3);
    if (h == 0 && w < 5) {
      u32 vo2 = w < 4 ? WS_FH + (tid >> 4) * 128 + ((tid & 15) < 14 ? (tid & 15) : 0) * 8 : WS_FX + (lane & 31) * 8;
      v2u got = {tlo + 1, 0};
#pragma unroll 1
      for (int spins = 0; spins < (1 << 14); ++spins) {
        if (__all(got.x == tlo && got.y == thi)) break;
        __builtin_amdgcn_s_sleep(32);
        asm volatile("buffer_load_dwordx2 %0, %1, %2, 0 offen sc1\n\ts_waitcnt vmcnt(0)" : "=&v"(got) : "v"(vo2), "s"(rWS) : "memory");
      }
    }
    if (h == 0) { BARL(); if (w == 0) if (lane == 0) st8(tagv, 0, rWS, WS_EPOCH); }
  } else {
    const u32 m = idx - 4, mq = (m * 37) >> 8, mr = m - mq * 7;
    const u32 rt = (xcd >> 1) * 4 + mq, ct = (xcd & 1) * 7 + mr;
    const u32 rt2 = xcd * 4 + mq, ct2 = mr;
    const u32 cp = xcd * 28 + m;
    const u32 l8 = lane >> 3, h = lane >> 5, l31 = lane & 31;
    const u32 rowp = w * 8 + l8;
    const u32 wslot = ((lane & 7) ^ ((lane >> 2) & 4)) << 4;
    u32 vX0 = (rt * 128 + rowp) * 896 + (((lane & 7) ^ ((w & 1) * 4 + (lane >> 4))) << 4), vX1 = vX0 + 64 * 896;
    u32 vW1 = rowp * 3584 + wslot + ct * 256;
    u32 vH = lane * 16;
    u32 mw = w * 1024;
    u32 srow = rt * 128 + ct * 8 + w, srowb = srow * 896, spar = cp * 1024;
    u32 sc[7];
    { u32 c = mq + mr; c = c >= 7 ? c - 7 : c; sc[0] = c * 128; sc[1] = c == 6 ? 0 : sc[0] + 128; }
    u32 sw0 = sc[0] * 1792;
    PINV(vX0); PINV(vX1); PINV(vW1); PINV(vH); PINV(vrow); PINS(mw); PINS(srowb); PINS(spar); PINS(sc[0]); PINS(sc[1]); PINS(sw0);
    const rsrc_t rX = mk(P.x, L * D * 2), rW1 = mk(P.w1, D * F * 2);
    v4u xs;
    ld16p(xs, vrow, rX, srowb);
    if (w < 4) {
      const char* ap = w == 0 ? P.logA : w == 1 ? P.Aim : w == 2 ? P.Cr : P.Ci;
      dma<LD_PAR>(mw, vH, mk(ap, D * 256 * 2), spar);
    }
    dma<0>(mw, vX0, rX, sc[0]); dma<PAGE>(mw, vX1, rX, sc[0]); dma<2 * PAGE>(mw, vW1, rW1, sw0);
    dma<3 * PAGE>(mw, vX0, rX, sc[1]); dma<4 * PAGE>(mw, vX1, rX, sc[1]);
    asm volatile("s_load_dwordx2 %0, %1, 0x0" : "=s"(epoch) : "s"(P.ws));
    SCHED;
    const rsrc_t rW2 = mk(P.w2, D * F * 2), rWS = mk(P.ws, WS_END);
    const u32 ldt2 = *(const CAS u32*)(uintptr_t)(P.logdt + cp * 4), dsk2 = *(const CAS u32*)(uintptr_t)(P.Dskip + cp * 4);
#pragma unroll
    for (int i = 2; i < 7; ++i) { sc[i] = sc[i - 1] == 768 ? 0 : sc[i - 1] + 128; PINS(sc[i]); }
    u32 sp[14];
    { u32 c = 2 * mr + 4 * mq; c = c >= 14 ? c - 14 : c; sp[0] = c * 8192; PINS(sp[0]); }
#pragma unroll
    for (int i = 1; i < 14; ++i) { sp[i] = sp[i - 1] == 13 * 8192 ? 0 : sp[i - 1] + 8192; PINS(sp[i]); }
    u32 vW1b = vW1 + 128, vW2a = rowp * 896 + wslot + ct2 * 128, vW2b = vW2a + 64 * 896;
    PINV(vW1b); PINV(vW2a); PINV(vW2b);
#define PAGE_(p) do { constexpr u32 lo_ = ((p) % NSLOT) * PAGE;                                      \
      if constexpr ((p) < 21) { if constexpr ((p) % 3 == 0) dma<lo_>(mw, vX0, rX, sc[(p) / 3]);       \
        else if constexpr ((p) % 3 == 1) dma<lo_>(mw, vX1, rX, sc[(p) / 3]);                          \
        else dma<lo_>(mw, vW1, rW1, sc[(p) / 3] * 1792); }                                            \
      else if constexpr ((p) < 28) dma<lo_>(mw, vW1b, rW1, sc[((p) - 21) % 7] * 1792);                \
      else dma<lo_>(mw, (p) < 42 ? vW2a : vW2b, rW2, sp[(p) % 14] * 14); } while (0)
    const u32 ch = w >> 2, w4 = w & 3;
    u32 xsel[4], xmask[2], aX, aY;
    {
      const bool last = l31 == 31;
      u32 qx, qy, ncol = (l31 + 1) & 31;
      if (w4 == 0) {
        xsel[3] = h ? 0x01000100u : 0x01000302u; xsel[2] = h ? 0x03020504u : 0x05040706u;
        xsel[1] = h ? 0x05040504u : 0x01000302u; xsel[0] = h ? 0x07060100u : 0x05040706u;
        xmask[1] = h ? 0u : ~0u; xmask[0] = h ? 0xffffu : ~0u;
        qx = 2 * h; qy = h ? (last ? 3 : 2) : 1; ncol = h ? ncol : l31;
      } else if (w4 == 1) {
        const u32 a = 0x04040404u * h, n = (h != 0) != last ? 0x05040504u : 0x01000100u;
        xsel[3] = 0x01000100u + a; xsel[2] = 0x01000302u + a; xsel[1] = 0x03020302u + a; xsel[0] = n;
        xmask[1] = 0u; xmask[0] = ~0u;
        qx = 3; qy = (h && last) ? 4 : 3;
      } else {
        const u32 a = (w4 & 1) * 2 + h, an = a + (last ? 1 : 0);
        const u32 sa = 0x01000100u + 0x02020202u * a, sn = 0x01000100u + 0x02020202u * (an & 3);
        xsel[3] = sa; xsel[2] = sa; xsel[0] = sn;
        xmask[1] = (lane & 1) ? ~0u : 0xffffu; xmask[0] = ~0u;
        xsel[1] = (sn & xmask[1]) | (sa & ~xmask[1]);
        qx = 4; qy = 4 + (an >> 2);
      }
      aX = LD_SC + ch * 4 * 4352 + l31 * 128 + ((qx ^ (l31 & 7)) << 4);
      aY = LD_SC + ch * 4 * 4352 + ncol * 128 + ((qy ^ (ncol & 7)) << 4);
    }
    const u32 tt = w & 3, fq = w >> 2;
    u32 ax[2][4], aw[2][4];
#pragma unroll
    for (int j = 0; j < 4; ++j) {
      const u32 ks = j ^ tt;
      ax[0][j] = (tt >> 1) * PAGE + ((tt & 1) * 32 + l31) * 128 + (((2 * ks + h) ^ ((l31 >> 1) & 7)) << 4);
      aw[0][j] = (16 * ks + 8 * h + ((lane >> 2) & 3)) * 128 + ((fq * 64 + ((lane >> 4) & 1) * 32 + (lane & 3) * 8) ^ (((lane >> 3) & 1) << 6));
      ax[1][j] = ax[0][j] + 7 * PAGE; aw[1][j] = aw[0][j] + 7 * PAGE;
      PINV(ax[0][j]); PINV(ax[1][j]); PINV(aw[0][j]); PINV(aw[1][j]);
    }
    u32 sw = LD_SC + w * 4352 + lane * 4, sr = LD_SC + w * 4352 + (lane & 15) * 272 + (lane >> 4) * 16, apar = LD_PAR + tid * 2;
    PINV(sw); PINV(sr); PINV(apar);
    float dt = rbf(expf(ch ? bf_hi(ldt2) : bf_lo(ldt2)));
    PINV(dt);
    STAMP(0);
    asm volatile("s_waitcnt vmcnt(5) lgkmcnt(0)\n\ts_barrier" : "+v"(pfdummy), "+v"(xs), "+s"(epoch)::"memory");
    TAG_READY();
    STAMP(1);
    const float lA = bf_lo(lr<u16>(apar)), ai = bf_lo(lr<u16>(apar + 1024)), cr = bf_lo(lr<u16>(apar + 2048)), ci = bf_lo(lr<u16>(apar + 3072));
    rowstat(xs, lane, tlo, thi, rWS, srow);
    PAGE_(5);
    if (w == 4) { if (lane < 16) dma<LD_BIAS - 4096>(mw, vH, mk(P.b1, F * 2), ct * 256); }
    else if (w == 5) { if (lane < 8) dma<LD_BIAS + 256 - 5120>(mw, vH, mk(P.b2, D * 2), ct2 * 128); }
    const float a2 = -rbf(__builtin_amdgcn_exp2f(lA * 1.44269504f)) * dt * 1.44269504f, br = ai * dt * 0.15915494f;
    f32x2 tb[16];
    const f32x2 z8 = cpow(a2 * 8.f, br * 8.f), z16 = cmul(z8, z8);
    tb[0] = (f32x2){1.f, 0.f}; tb[1] = z8; tb[2] = z16;
#pragma unroll
    for (int j = 3; j < 16; ++j) tb[j] = cmul(tb[j - 2], z16);
    SCHED; PAGE_(6); SCHED;
    v4u xf[28], wf[4], lo[2][4], hi[4], ko;
    f32x16 acc;
#pragma unroll
    for (int i = 0; i < 16; ++i) acc[i] = 0.f;
    float cs = 0.f;
#define RD_X(i, j) xf[4 * (i) + (j)] = lr<v4u>(ax[((3 * (i)) % NSLOT) / 7][j] + (((3 * (i)) % NSLOT) % 7) * PAGE)
#define RD_W(p, j) trr2(aw[((p) % NSLOT) / 7][j] + (((p) % NSLOT) % 7) * PAGE, aw[((p) % NSLOT) / 7][j] + (((p) % NSLOT) % 7) * PAGE + 512)
#define CHUNK_A(i, NW)                                                                   \
    BARV(NW);                                                                            \
    _Pragma("unroll") for (int j = 0; j < 4; ++j) { wf[j] = RD_W(3 * (i) + 2, j); RD_X(i, j); } \
    PAGE_(3 * (i) + 7);                                                                  \
    _Pragma("unroll") for (int e = 0; e < 4; ++e) cs = dot2(wf[0][e], 0x3f803f80u, cs);  \
    SCHED;                                                                               \
    _Pragma("unroll") for (int j = 0; j < 4; ++j) acc = mfma32(wf[j], xf[4 * (i) + j], acc); \
    SCHED
#define TAB_OUT(n) _Pragma("unroll") for (int j = 0; j < n; ++j) lw<u32>(sw + j * 272, pk2(tb[j]))
#define TAB_IN(dst) _Pragma("unroll") for (int t = 0; t < 4; ++t) dst[t] = lr<v4u>(sr + t * 64)
    STAMP(2);
    CHUNK_A(0, 5);
    TAB_OUT(16); TAB_IN(lo[0]); SCHED; PAGE_(8);
    tb[0] = cmul(tb[14], z16); tb[1] = cmul(tb[15], z16);
#pragma unroll
    for (int j = 2; j < 16; ++j) tb[j] = cmul(tb[j - 2], z16);
    SCHED; PAGE_(9);
    CHUNK_A(1, 4);
    TAB_OUT(16); TAB_IN(lo[1]);
    const f32x2 z1 = cpow(a2, -br), z256 = cpow(a2 * 256.f, br * -256.f);
    { float c0 = 2.f * dt * cr, c1 = -2.f * dt * ci; asm("" : "+v"(c0)); asm("" : "+v"(c1)); tb[0] = (f32x2){c0, c1}; }
    SCHED; PAGE_(11);
#pragma unroll
    for (int j = 0; j < 7; ++j) { tb[j + 1] = cmul(tb[j], z1); if (!(j & 1)) tb[8 + j / 2] = cmul(tb[j], z256); }
    tb[12] = cmul(tb[8], z256); tb[13] = cmul(tb[10], z256); tb[14] = cmul(tb[12], z256); tb[15] = cmul(tb[13], z256);
    SCHED; PAGE_(12);
    STAMP(3);
    CHUNK_A(2, 4);
    TAB_OUT(16); TAB_IN(hi); SCHED; PAGE_(14);
    tb[0] = cmul(tb[14], z256);
#pragma unroll
    for (int j = 1; j < 5; ++j) tb[j] = cmul(tb[j - 1], z256);
    {
      f32x4 kd[2][2];
      v4u hi2[4];
      TAB_OUT(5); TAB_IN(hi2);
      SCHED;
#pragma unroll
      for (int t = 0; t < 4; ++t)
#pragma unroll
        for (int lt = 0; lt < 2; ++lt) kd[0][lt] = mfma16(hi[t], lo[lt][t], t ? kd[0][lt] : (f32x4){0.f, 0.f, 0.f, 0.f});
#pragma unroll
      for (int t = 0; t < 4; ++t)
#pragma unroll
        for (int lt = 0; lt < 2; ++lt) kd[1][lt] = mfma16(hi2[t], lo[lt][t], t ? kd[1][lt] : (f32x4){0.f, 0.f, 0.f, 0.f});
      const u32 pw = LD_SC + w * 4352 + (lane & 15) * 128;
#pragma unroll
      for (int ht = 0; ht < 2; ++ht)
#pragma unroll
        for (int lt = 0; lt < 2; ++lt) lw<f32x4>(pw + lt * 2048 + (((ht * 4 + (lane >> 4)) ^ (lane & 7)) << 4), kd[ht][lt]);
    }
    SCHED; PAGE_(15);
    STAMP(4);
    CHUNK_A(3, 4);
    {
      f32x4 X = lr<f32x4>(aX), Y = lr<f32x4>(aY);
#pragma unroll
      for (int s = 1; s < 4; ++s) { X += lr<f32x4>(aX + s * 4352); Y += lr<f32x4>(aY + s * 4352); }
      PAGE_(17);
      const u32 x01 = pk(X[0], X[1]), x23 = pk(X[2], X[3]), y01 = pk(Y[0], Y[1]), y23 = pk(Y[2], Y[3]);
      ko[3] = __builtin_amdgcn_perm(x23, x01, xsel[3]); ko[2] = __builtin_amdgcn_perm(x23, x01, xsel[2]);
#pragma unroll
      for (int mm = 0; mm < 2; ++mm) ko[mm] = (__builtin_amdgcn_perm(y23, y01, xsel[mm]) & xmask[mm]) | (__builtin_amdgcn_perm(x23, x01, xsel[mm]) & ~xmask[mm]);
    }
    SCHED; PAGE_(18);
    CHUNK_A(4, 4);
    {
      const u32 ka = LD_K + ch * KCH + (255 - (tid & 255)) * 16;
      const u32 e1 = __builtin_amdgcn_alignbit(ko[1], ko[0], 16), e2 = __builtin_amdgcn_alignbit(ko[2], ko[1], 16), e3 = __builtin_amdgcn_alignbit(ko[3], ko[2], 16);
      lw<v4u>(ka, ko);
      lw<u16>(ka + 4160 + 2, (u16)ko[0]); lw<u32>(ka + 4160 + 4, e1); lw<v2u>(ka + 4160 + 8, (v2u){e2, e3}); lw<u16>(ka + 4160 + 16, (u16)(ko[3] >> 16));
      lw<u32>(ka + 8320 + 4, ko[0]); lw<v2u>(ka + 8320 + 8, (v2u){ko[1], ko[2]}); lw<u32>(ka + 8320 + 16, ko[3]);
      lw<u16>(ka + 12480 + 6, (u16)ko[0]); lw<v2u>(ka + 12480 + 8, (v2u){e1, e2}); lw<u32>(ka + 12480 + 16, e3); lw<u16>(ka + 12480 + 20, (u16)(ko[3] >> 16));
      if (w4 == 0) lw<u16>(LD_K + ch * KCH + (lane >> 4) * 4162 + 4096 + (lane & 15) * 2, (u16)0);
      PAGE_(20);
    }
    SCHED; PAGE_(21);
    STAMP(5);
    CHUNK_A(5, 4);
    v4u gran; u32 vgr = l31 * 16; const u32 sgr = WS_STAT + (rt * 128 + tt * 32) * 16;
    ld16(gran, vgr, rWS, sgr);
    PAGE_(23); PAGE_(24);
    CHUNK_A(6, 5);
    PAGE_(26); PAGE_(27);
    const u32 acs = LD_CS + fq * 256 + lane * 4;
    lw<float>(acs + tt * 512, cs);
    STAMP(6);
    asm volatile("s_waitcnt vmcnt(5) lgkmcnt(0)\n\ts_barrier" : "+v"(gran)::"memory");
#pragma unroll 1
    for (int spins = 0; spins < (1 << 16); ++spins) {
      if (__all(gran[0] == tlo && gran[2] == thi)) break;
      __builtin_amdgcn_s_sleep(2);
      asm volatile("buffer_load_dwordx4 %0, %1, %2, %3 offen sc1\n\ts_waitcnt vmcnt(0)" : "=&v"(gran) : "v"(vgr), "s"(rWS), "s"(sgr) : "memory");
    }
    STAMP(7);
    v4u wn[4];
#pragma unroll
    for (int j = 0; j < 4; ++j) wf[j] = RD_W(21, j);
    auto make_aext = [&](int st) {
      float c = lr<float>(acs + st * 2048);
#pragma unroll
      for (int s = 1; s < 4; ++s) c += lr<float>(acs + st * 2048 + s * 512);
      const u32 b1v = h ? 0u : (u32)lr<u16>(LD_BIAS + st * 128 + fq * 64 + l31 * 2);
      const u32 chh = pk(c, c); const float cl = c - bf_lo(chh);
      return (v4u){chh, pk(cl, cl), b1v * 0x10001u, 0u};
    };
    const v4u bext = {gran[1], gran[1], gran[3], 0u};
    GeluC gc;
    { gc.ir = bf_lo(gran[3]) + bf_hi(gran[3]); const float rs = __builtin_amdgcn_rcpf(gc.ir); gc.k0 = -2.3022082f * rs; gc.k1 = -0.10294324f * rs * rs * rs; }
    const u32 sHst = WS_H + ((rt * 4 + tt) * 112 + ct * 8 + fq * 2) * 1024;
    v2u pollx; const u32 vpx = l31 * 8;
    PAGE_(28);
    acc = mfma32(make_aext(0), bext, acc);
    f32x16 accb;
#pragma unroll
    for (int i = 0; i < 16; ++i) accb[i] = 0.f;
    cs = 0.f;
    float gl[8];
    v4u ureg;
#define STEP_B(i, cur, nxt)                                                              \
    SCHED;                                                                               \
    if (i < 6) { _Pragma("unroll") for (int j = 0; j < 4; ++j) nxt[j] = RD_W(22 + (i), j); } \
    _Pragma("unroll") for (int e = 0; e < 4; ++e) cs = dot2(cur[0][e], 0x3f803f80u, cs); \
    SCHED;                                                                               \
    _Pragma("unroll") for (int j = 0; j < 4; ++j) accb = mfma32(cur[j], xf[4 * (i) + j], accb); \
    SCHED
#define GELU_A(j0, n) _Pragma("unroll") for (int j = j0; j < j0 + n; ++j) gl[j & 7] = gelu1(acc[j], gc)
#define H_OUT(off) st16((v4u){pkq(gl[0], gl[1]), pkq(gl[2], gl[3]), pkq(gl[4], gl[5]), pkq(gl[6], gl[7])}, vH, rWS, sHst + (off))
    STEP_B(0, wf, wn); GELU_A(0, 3); PAGE_(29);
    BARV(2);
    STEP_B(1, wn, wf); GELU_A(3, 3); PAGE_(30);
    STEP_B(2, wf, wn); GELU_A(6, 2); H_OUT(0); PAGE_(31);
    STEP_B(3, wn, wf); GELU_A(8, 3); PAGE_(32); ld8(pollx, vpx, rWS, WS_FX);
    STEP_B(4, wf, wn); GELU_A(11, 3); PAGE_(33);
    STEP_B(5, wn, wf); GELU_A(14, 2); H_OUT(1024); PAGE_(34);
    STEP_B(6, wf, wn);
    lw<float>(acs + 2048 + tt * 512, cs);
    STAMP(8);
    asm volatile("s_waitcnt vmcnt(3) lgkmcnt(0)\n\ts_barrier" : "+v"(pollx)::"memory");
    poll_finish(pollx, vpx, rWS, WS_FX, tlo, thi);
    ld16(ureg, tid * 16, rWS, WS_XT + cp * 8192);
    PAGE_(35); PAGE_(36);
    STAMP(9);
    {
      accb = mfma32(make_aext(1), bext, accb);
#pragma unroll
      for (int s = 0; s < 2; ++s) {
#pragma unroll
        for (int j = 0; j < 8; ++j) gl[j] = gelu1(accb[8 * s + j], gc);
        if (s == 0) { PAGE_(37); PAGE_(38); }
        st16((v4u){pk(gl[0], gl[1]), pk(gl[2], gl[3]), pk(gl[4], gl[5]), pk(gl[6], gl[7])}, vH, rWS, sHst + 4096 + s * 1024);
        SCHED;
      }
    }
    STAMP(10);
    WAITV_R(6, ureg);
    lw<v2u>(LD_U + 512 + tid * 8, (v2u){__builtin_amdgcn_perm(ureg[1], ureg[0], 0x05040100u), __builtin_amdgcn_perm(ureg[3], ureg[2], 0x05040100u)});
    lw<v2u>(LD_U + 4608 + 512 + tid * 8, (v2u){__builtin_amdgcn_perm(ureg[1], ureg[0], 0x07060302u), __builtin_amdgcn_perm(ureg[3], ureg[2], 0x07060302u)});
    lw<u16>(LD_U + ch * 4608 + (tid & 255) * 2, (u16)0);
    BARL();
    if (w == 0) if (lane == 0) st8(tagv, 0, rWS, WS_FH + rt * 128 + ct * 8);
    PAGE_(39); PAGE_(40); PAGE_(41);
    STAMP(11);
    const u32 t2 = w >> 2, kq = w & 3;
    u32 vHa = vH + WS_H + ((rt2 * 2 + t2) * 112 + kq) * 1024, vHb = vHa + 4096;
    PINV(vHa); PINV(vHb);
    v4u hf[28];
#define HLOAD(j) ld16(hf[j], (j) < 14 ? vHa : vHb, rWS, sp[(j) % 14])
    v2u pollh; const u32 vph = (lane < 14 ? lane : 0) * 8, sph = WS_FH + (rt2 >> 1) * 128;
    f32x4 ya[8];
    const u32 r16 = lane & 15, g4 = lane >> 4;
    u32 kb = LD_K + ch * KCH + ((r16 + 1) & 3) * 4162 + (2031 - r16 + 8 * g4) * 2 - 7 * 512 - 64 - w4 * 128;
    u32 ub = LD_U + ch * 4608 + 512 - 32 - 64 + r16 * 32 + g4 * 16 - w4 * 128;
    PINV(kb); PINV(ub);
    {
      v4u B[2][8], A[2];
      auto rdA = [&](int n) { const u32 a = kb + (1 - n / 8) * 64 + (7 - n % 8) * 512; const v2u x = lrv<v2u>(a), y = lrv<v2u>(a + 8); return (v4u){x.x, x.y, y.x, y.y}; };
      A[0] = rdA(0);
#pragma unroll
      for (int T = 0; T < 8; ++T) B[0][T] = lr<v4u>(ub + 64 + T * 512);
#pragma unroll
      for (int n = 0; n < 16; ++n) {
        const int it = n / 8, a = n % 8;
        SCHED;
        if (n == 8) {
          asm volatile("s_waitcnt vmcnt(0) lgkmcnt(0)\n\ts_barrier" : "+v"(pollh)::"memory");
          if (w == 0) if (lane == 0) st8(tagv, 0, rWS, WS_FH + 2048 + rt * 128 + ct * 8);
          poll_finish(pollh, vph, rWS, sph, tlo, thi);
          STAMP(12);
        }
        if (n < 15) A[(n + 1) & 1] = rdA(n + 1);
        if (n < 8) B[1][7 - n] = lr<v4u>(ub + (7 - n) * 512);
        if (n == 3) ld8(pollh, vph, rWS, sph);
        if (n >= 8 && n < 13) { HLOAD(2 * (n - 8)); }
        SCHED;
#pragma unroll
        for (int T = 7; T >= a; --T) ya[T] = mfma16(A[n & 1], B[it][T - a], n ? ya[T] : (f32x4){0.f, 0.f, 0.f, 0.f});
        if (n >= 8 && n < 13) { SCHED; HLOAD(2 * (n - 8) + 1); }
      }
    }
    STAMP(13);
    const v4u xn = lr<v4u>(LD_U + ch * 4608 + 512 + (tid & 255) * 16);
    u32 yp[16];
#pragma unroll
    for (int T = 0; T < 8; ++T) { yp[2 * T] = pk(ya[T][0], ya[T][1]); yp[2 * T + 1] = pk(ya[T][2], ya[T][3]); }
    HLOAD(10); HLOAD(11);
    BARL();
    {
      const u32 ra = LD_RED + w * 4096 + r16 * 32 + g4 * 8;
#pragma unroll
      for (int T = 0; T < 8; ++T) lw<v2u>(ra + T * 512, (v2u){yp[2 * T], yp[2 * T + 1]});
    }
    HLOAD(12); HLOAD(13);
    f32x16 g0, g1;
#pragma unroll
    for (int i = 0; i < 16; ++i) { g0[i] = 0.f; g1[i] = 0.f; }
    u32 a2w[2][2];
    a2w[0][0] = (16 * kq + 4 * h + ((lane >> 2) & 3)) * 128 + ((((lane >> 4) & 1) * 32 + (lane & 3) * 8) ^ (((lane >> 3) & 1) << 6));
    a2w[0][1] = a2w[0][0] ^ 64; a2w[1][0] = a2w[0][0] + 7 * PAGE; a2w[1][1] = a2w[0][1] + 7 * PAGE;
    PINV(a2w[0][0]); PINV(a2w[0][1]); PINV(a2w[1][0]); PINV(a2w[1][1]);
    v4u wa[2][2], wb[2][2];
#define RD_W2(j, d) trr2(a2w[((j) % NSLOT) / 7][d] + (((j) % NSLOT) % 7) * PAGE, a2w[((j) % NSLOT) / 7][d] + (((j) % NSLOT) % 7) * PAGE + 1024)
#define RD_G(dst, j) dst[0][0] = RD_W2(j, 0); dst[0][1] = RD_W2(j, 1); dst[1][0] = RD_W2((j) + 1, 0); dst[1][1] = RD_W2((j) + 1, 1)
#define MM_G(src, j) SCHED; g0 = mfma32(src[0][0], hf[j], g0); g1 = mfma32(src[0][1], hf[j], g1); g0 = mfma32(src[1][0], hf[(j) + 1], g0); g1 = mfma32(src[1][1], hf[(j) + 1], g1); SCHED
#define HWAIT(n, j) asm volatile("s_waitcnt vmcnt(" #n ")" : "+v"(hf[j]), "+v"(hf[(j) + 1]))
    RD_G(wa, 0);
    BARL();
    STAMP(14);
    v4u yq[4];
#pragma unroll
    for (int s = 0; s < 4; ++s) yq[s] = lr<v4u>(LD_RED + (ch * 4 + s) * 4096 + (tid & 255) * 16);
    float yo[8];
    const float dsk = ch ? bf_hi(dsk2) : bf_lo(dsk2);
    RD_G(wb, 2); HWAIT(12, 0); MM_G(wa, 0);
#pragma unroll
    for (int i = 0; i < 4; ++i) { yo[2 * i] = bf_lo(yq[0][i]) + bf_lo(yq[1][i]); yo[2 * i + 1] = bf_hi(yq[0][i]) + bf_hi(yq[1][i]); }
    ld8(pollh, vph, rWS, sph + 2048);
    RD_G(wa, 4); HWAIT(11, 2); MM_G(wb, 2);
#pragma unroll
    for (int i = 0; i < 4; ++i) { yo[2 * i] += bf_lo(yq[2][i]); yo[2 * i + 1] += bf_hi(yq[2][i]); yo[2 * i] += bf_lo(yq[3][i]); }
    RD_G(wb, 6); HWAIT(9, 4); MM_G(wa, 4);
#pragma unroll
    for (int i = 0; i < 4; ++i) { yo[2 * i + 1] += bf_hi(yq[3][i]); yo[2 * i] = __builtin_fmaf(bf_lo(xn[i]), dsk, yo[2 * i]); yo[2 * i + 1] = __builtin_fmaf(bf_hi(xn[i]), dsk, yo[2 * i + 1]); }
    st16((v4u){pkq(yo[0], yo[1]), pkq(yo[2], yo[3]), pkq(yo[4], yo[5]), pkq(yo[6], yo[7])}, vH, rWS, WS_ST + cp * 8192 + mw);
    asm volatile("s_waitcnt vmcnt(1) lgkmcnt(0)\n\ts_barrier" : "+v"(hf[6]), "+v"(hf[7]), "+v"(hf[8]), "+v"(hf[9]), "+v"(hf[10]), "+v"(hf[11]), "+v"(hf[12]), "+v"(hf[13]), "+v"(pollh)::"memory");
    PAGE_(42); PAGE_(43);
    poll_finish(pollh, vph, rWS, sph + 2048, tlo, thi);
    STAMP(15);
    RD_G(wa, 8); HLOAD(14); HLOAD(15); MM_G(wb, 6); PAGE_(44); PAGE_(45);
    RD_G(wb, 10); HLOAD(16); HLOAD(17); MM_G(wa, 8); PAGE_(46); PAGE_(47);
    RD_G(wa, 12); HLOAD(18); HLOAD(19); MM_G(wb, 10); PAGE_(48); PAGE_(49);
    BARV(8);
    if (w == 0) if (lane == 0) st8(tagv, 0, rWS, WS_FS + cp * 8);
    STAMP(16);
    RD_G(wb, 14); HLOAD(20); HLOAD(21); MM_G(wa, 12); PAGE_(50); PAGE_(51);
    RD_G(wa, 16); HLOAD(22); HLOAD(23); HWAIT(63, 14); MM_G(wb, 14); PAGE_(52); PAGE_(53);
    BARV(8);
    RD_G(wb, 18); HLOAD(24); HLOAD(25); HWAIT(63, 16); MM_G(wa, 16); PAGE_(54); PAGE_(55);
    RD_G(wa, 20); HLOAD(26); HLOAD(27); HWAIT(63, 18); MM_G(wb, 18);
    v2u polls; ld8(polls, vpx, rWS, WS_FS + ct2 * 256);
    v4u xin; const u32 vout = (tid >> 3) * 896 + (tid & 7) * 16, sout = rt2 * 64 * 896 + ct2 * 128;
    ld16p(xin, vout, rX, sout);
    BARV(8);
    STAMP(17);
    RD_G(wb, 22); HWAIT(63, 20); MM_G(wa, 20);
    RD_G(wa, 24); HWAIT(63, 22); MM_G(wb, 22);
    asm volatile("s_waitcnt vmcnt(0) lgkmcnt(0)\n\ts_barrier" : "+v"(hf[24]), "+v"(hf[25]), "+v"(hf[26]), "+v"(hf[27]), "+v"(polls), "+v"(xin)::"memory");
    poll_finish(polls, vpx, rWS, WS_FS + ct2 * 256, tlo, thi);
    v4u sreg; ld16(sreg, (tid >> 3) * 4096 + (tid & 7) * 16, rWS, WS_ST + ct2 * 64 * 4096 + rt2 * 128);
    STAMP(18);
    RD_G(wb, 26); MM_G(wa, 24); MM_G(wb, 26);
    {
      const u32 pa = kq * 16384 + (t2 * 32 + l31) * 256;
#pragma unroll
      for (int qd = 0; qd < 4; ++qd) lw<f32x4>(pa + (((2 * qd + h) ^ (lane & 7)) << 4), (f32x4){g0[4 * qd], g0[4 * qd + 1], g0[4 * qd + 2], g0[4 * qd + 3]});
#pragma unroll
      for (int qd = 0; qd < 4; ++qd) lw<f32x4>(pa + 128 + (((2 * qd + h) ^ (lane & 7)) << 4), (f32x4){g1[4 * qd], g1[4 * qd + 1], g1[4 * qd + 2], g1[4 * qd + 3]});
      WAITV_R(0, sreg);
      lw<v4u>(LD_RED + (tid >> 3) * 128 + (((tid & 7) ^ w) << 4), sreg);
    }
    STAMP(19);
    BARL();
    {
      const u32 tok = tid >> 3, d8 = tid & 7;
      const u32 ra = tok * 256 + (((2 * d8) ^ (tok & 7)) << 4);
      f32x4 o0 = lr<f32x4>(ra), o1 = lr<f32x4>(ra ^ 16);
#pragma unroll
      for (int s = 1; s < 4; ++s) { o0 += lr<f32x4>(ra + s * 16384); o1 += lr<f32x4>((ra ^ 16) + s * 16384); }
      const v4u b2v = lr<v4u>(LD_BIAS + 256 + d8 * 16);
      const u32 sa = LD_RED + d8 * 1024 + ((w ^ d8) << 4) + (tok & 7) * 2;
      float o[8] = {o0[0], o0[1], o0[2], o0[3], o1[0], o1[1], o1[2], o1[3]};
      v4u res;
#pragma unroll
      for (int i = 0; i < 4; ++i) {
        const float v0 = o[2 * i] + bf_lo(b2v[i]), v1 = o[2 * i + 1] + bf_hi(b2v[i]);
        const float s0 = bf_lo(lr<u16>(sa + (2 * i) * 128)) * v0, s1 = bf_lo(lr<u16>(sa + (2 * i + 1) * 128)) * v1;
        const float r0 = __builtin_amdgcn_rcpf(1.f + __builtin_amdgcn_exp2f(v0 * -1.44269504f)), r1 = __builtin_amdgcn_rcpf(1.f + __builtin_amdgcn_exp2f(v1 * -1.44269504f));
        res[i] = pk(__builtin_fmaf(s0, r0, bf_lo(xin[i])), __builtin_fmaf(s1, r1, bf_hi(xin[i])));
      }
      *(GAS v4u*)(P.out + sout + vout) = res;
    }
    STAMP(20);
  }
  asm volatile("fused_code_end:");
}

extern "C" void kernel_launch(void* const* d_in, const int* in_sizes, int n_in, void* d_out, int out_size, void* d_ws, size_t ws_size, hipStream_t stream) {
  Params P;
  P.x = (const char*)d_in[0]; P.logA = (const char*)d_in[1]; P.Aim = (const char*)d_in[2]; P.Cr = (const char*)d_in[3]; P.Ci = (const char*)d_in[4];
  P.logdt = (const char*)d_in[5]; P.Dskip = (const char*)d_in[6]; P.w1 = (const char*)d_in[8]; P.b1 = (const char*)d_in[9];
  P.w2 = (const char*)d_in[10]; P.b2 = (const char*)d_in[11]; P.out = (char*)d_out; P.ws = (char*)d_ws;
  fused_kernel<<<256, 512, 0, stream>>>(P);
}
```

```cpp
#include <hip/hip_runtime.h>
#include <stdint.h>

typedef unsigned int u32;
typedef unsigned long long u64;
typedef unsigned short u16;
typedef u32 v4u __attribute__((ext_vector_type(4)));
typedef u32 v2u __attribute__((ext_vector_type(2)));
typedef float f32x2 __attribute__((ext_vector_type(2)));
typedef float f32x4 __attribute__((ext_vector_type(4)));
typedef float f32x16 __attribute__((ext_vector_type(16)));
typedef __bf16 bf16x2 __attribute__((ext_vector_type(2)));
typedef __bf16 bf16x8 __attribute__((ext_vector_type(8)));
typedef short s16x4 __attribute__((ext_vector_type(4)));
typedef __amdgpu_buffer_rsrc_t rsrc_t;
#define LAS __attribute__((address_space(3)))
#define GAS __attribute__((address_space(1)))
#define CAS __attribute__((address_space(4)))
#define DI __device__ __forceinline__
#define SCHED __builtin_amdgcn_sched_barrier(0)
#define PINV(x) asm volatile("" : "+v"(x))
#define PINS(x) asm volatile("" : "+s"(x))

constexpr int L = 2048, D = 448, F = 1792;
constexpr u32 WS_EPOCH = 0, WS_FX = 256, WS_FH = 1024, WS_FS = 8192, WS_STAT = 16384, WS_DBG = 65536;
constexpr u32 WS_XT = 1u << 20;
constexpr u32 WS_ST = WS_XT + L * D * 2;
constexpr u32 WS_H = WS_ST + L * D * 2;
constexpr u32 WS_END = WS_H + L * F * 2;
constexpr u32 PAGE = 8192, NSLOT = 14;
constexpr u32 LD_K = NSLOT * PAGE;
constexpr u32 KCH = 4 * 4160;
constexpr u32 LD_U = LD_K + 2 * KCH;
constexpr u32 LD_SC = LD_K;
constexpr u32 LD_PAR = LD_SC + 8 * 4352;
constexpr u32 LD_RED = LD_K;
constexpr u32 LD_BIAS = LD_U + 2 * 4608;
constexpr u32 LD_CS = LD_BIAS + 512;
constexpr u32 LDS_BYTES = 163840;
static_assert(LD_CS + 4096 <= LDS_BYTES, "lds");
static_assert(LD_PAR + 4096 <= LD_BIAS, "lds");

struct Params {
  const char *x, *logA, *Aim, *Cr, *Ci, *logdt, *Dskip, *w1, *b1, *w2, *b2;
  char *out, *ws;
};

DI float bf_lo(u32 v) { return __uint_as_float(v << 16); }
DI float bf_hi(u32 v) { return __uint_as_float(v & 0xffff0000u); }
DI u32 pk2(f32x2 f) { return __builtin_bit_cast(u32, __builtin_convertvector(f, bf16x2)); }
DI u32 pk(float lo, float hi) { return pk2((f32x2){lo, hi}); }
DI u32 pkq(float lo, float hi) { asm("" : "+v"(lo)); asm("" : "+v"(hi)); return pk2((f32x2){lo, hi}); }
DI float rbf(float x) { return bf_lo(pk(x, x)); }
DI float dot2(u32 a, u32 b, float c) { return __builtin_amdgcn_fdot2_f32_bf16(__builtin_bit_cast(bf16x2, a), __builtin_bit_cast(bf16x2, b), c, false); }
DI f32x16 mfma32(v4u a, v4u b, f32x16 c) { return __builtin_amdgcn_mfma_f32_32x32x16_bf16(__builtin_bit_cast(bf16x8, a), __builtin_bit_cast(bf16x8, b), c, 0, 0, 0); }
DI f32x4 mfma16(v4u a, v4u b, f32x4 c) { return __builtin_amdgcn_mfma_f32_16x16x32_bf16(__builtin_bit_cast(bf16x8, a), __builtin_bit_cast(bf16x8, b), c, 0, 0, 0); }
DI rsrc_t mk(const void* p, u32 bytes) { return __builtin_amdgcn_make_buffer_rsrc((void*)p, 0, bytes, 0x00020000); }
template <class T> DI T lr(u32 a) { return *(const LAS T*)(uintptr_t)a; }
template <class T> DI T lrv(u32 a) { return *(const volatile LAS T*)(uintptr_t)a; }
template <class T> DI void lw(u32 a, T v) { *(LAS T*)(uintptr_t)a = v; }
DI v2u trr(u32 a) { return __builtin_bit_cast(v2u, __builtin_amdgcn_ds_read_tr16_b64_v4i16((LAS s16x4*)(uintptr_t)a)); }
DI v4u trr2(u32 a0, u32 a1) { v2u x = trr(a0), y = trr(a1); return (v4u){x.x, x.y, y.x, y.y}; }
template <u32 LOFF> DI void dma(u32 mw, u32 voff, rsrc_t r, u32 soff) {
  asm volatile("s_add_i32 m0, %0, %4\n\ts_nop 0\n\tbuffer_load_dwordx4 %1, %2, %3 offen lds" ::"s"(mw), "v"(voff), "s"(r), "s"(soff), "i"(LOFF) : "m0", "scc");
}
DI void ld16(v4u& d, u32 voff, rsrc_t r, u32 soff) { asm volatile("buffer_load_dwordx4 %0, %1, %2, %3 offen sc1" : "=v"(d) : "v"(voff), "s"(r), "s"(soff)); }
DI void ld16p(v4u& d, u32 voff, rsrc_t r, u32 soff) { asm volatile("buffer_load_dwordx4 %0, %1, %2, %3 offen" : "=v"(d) : "v"(voff), "s"(r), "s"(soff)); }
DI void ld8(v2u& d, u32 voff, rsrc_t r, u32 soff) { asm volatile("buffer_load_dwordx2 %0, %1, %2, %3 offen sc1" : "=v"(d) : "v"(voff), "s"(r), "s"(soff)); }
DI void st16(v4u d, u32 voff, rsrc_t r, u32 soff) { asm volatile("buffer_store_dwordx4 %0, %1, %2, %3 offen sc1\n\ts_nop 1" ::"v"(d), "v"(voff), "s"(r), "s"(soff)); }
DI void st8(v2u d, u32 voff, rsrc_t r, u32 soff) { asm volatile("buffer_store_dwordx2 %0, %1, %2, %3 offen sc1" ::"v"(d), "v"(voff), "s"(r), "s"(soff)); }
#define WAITV_R(n, r) asm volatile("s_waitcnt vmcnt(" #n ")" : "+v"(r)::"memory")
#define BARV(n) asm volatile("s_waitcnt vmcnt(" #n ") lgkmcnt(0)\n\ts_barrier" ::: "memory")
#define BARL() asm volatile("s_waitcnt lgkmcnt(0)\n\ts_barrier" ::: "memory")

#define DIAG 0
#define DBLK 255
#define STAMP(i) do { if (i == DIAG - 10) { asm volatile("s_memrealtime %0\n\ts_waitcnt lgkmcnt(0)" : "=s"(r_mark)::"memory"); } } while (0)

DI void poll_finish(v2u got, u32 voff, rsrc_t r, u32 soff, u32 tlo, u32 thi) {
#pragma unroll 1
  for (int spins = 0; spins < (1 << 16); ++spins) {
    if (__all(got.x == tlo && got.y == thi)) break;
    __builtin_amdgcn_s_sleep(2);
    asm volatile("buffer_load_dwordx2 %0, %1, %2, %3 offen sc1\n\ts_waitcnt vmcnt(0)" : "=&v"(got) : "v"(voff), "s"(r), "s"(soff) : "memory");
  }
}
DI f32x2 cmul(f32x2 a, f32x2 b) {
  f32x2 t;
  asm("v_pk_mul_f32 %0, %1, %2 op_sel_hi:[1,0]\n\tv_pk_fma_f32 %0, %1, %2, %0 op_sel:[1,1,0] op_sel_hi:[0,1,1] neg_lo:[0,1,0]" : "=&v"(t) : "v"(a), "v"(b));
  return t;
}
DI f32x2 cpow(float a2, float br) {
  float m = __builtin_amdgcn_exp2f(a2), f = __builtin_amdgcn_fractf(br);
  float c = m * __builtin_amdgcn_cosf(f), s = m * __builtin_amdgcn_sinf(f);
  asm("" : "+v"(c)); asm("" : "+v"(s));
  return (f32x2){c, s};
}
#define DPP_ADD(v, ctl, rm) v += __builtin_bit_cast(float, __builtin_amdgcn_update_dpp(0, __builtin_bit_cast(int, v), ctl, rm, 0xf, true))
DI void wave_sum2(float& a, float& b) {
  DPP_ADD(a, 0xB1, 0xf); DPP_ADD(b, 0xB1, 0xf); DPP_ADD(a, 0x4E, 0xf); DPP_ADD(b, 0x4E, 0xf);
  DPP_ADD(a, 0x141, 0xf); DPP_ADD(b, 0x141, 0xf); DPP_ADD(a, 0x140, 0xf); DPP_ADD(b, 0x140, 0xf);
  DPP_ADD(a, 0x142, 0xf); DPP_ADD(b, 0x142, 0xf); DPP_ADD(a, 0x143, 0xf); DPP_ADD(b, 0x143, 0xf);
}
DI void rowstat(v4u xr, u32 lane, u32 tlo, u32 thi, rsrc_t rWS, u32 row) {
  float s = 0.f, q = 0.f;
#pragma unroll
  for (int j = 0; j < 4; ++j) { s = dot2(xr[j], 0x3f803f80u, s); q = dot2(xr[j], xr[j], q); }
  wave_sum2(s, q);
  const float nm = s * (-1.f / D), var = fmaxf(q * (1.f / D) - nm * nm, 0.f);
  const float den = rbf(__builtin_amdgcn_sqrtf(rbf(rbf(var) + 1e-5f))), rd = __builtin_amdgcn_rcpf(den);
  const float ir = den * rbf(__builtin_amdgcn_sqrtf(rbf(rbf(var * rd * rd) + 1e-5f)));
  const v4u g = {tlo, pk(nm, nm - rbf(nm)), thi, pk(ir, ir - rbf(ir))};
  st16(g, lane == 63 ? 0u : 0x7ffffff0u, rWS, WS_STAT + row * 16);
}
struct GeluC { float k0, k1, ir; };
DI float gelu1(float a, const GeluC& c) {
  const float p = __builtin_fmaf(a * a, c.k1, c.k0);
  const float e = __builtin_amdgcn_exp2f(a * p);
  return a * __builtin_amdgcn_rcpf(__builtin_fmaf(e, c.ir, c.ir));
}

__global__ __launch_bounds__(512, 2) void fused_kernel(Params P) {
  __shared__ __attribute__((aligned(1024))) char lds_all[LDS_BYTES];
  const u32 tid = threadIdx.x, lane = tid & 63;
  const u32 w = __builtin_amdgcn_readfirstlane(tid >> 6);
  const u32 b = blockIdx.x, xcd = b & 7, idx = b >> 3;
  u64 r_mark = 0; const u64 d_r0 = __builtin_amdgcn_s_memrealtime(), d_t0 = __builtin_amdgcn_s_memtime();
  asm volatile("" ::"v"((LAS char*)lds_all));
  u32 pfdummy = 0;
  if (w == 7) {
    u64 pc; u32 len;
    asm volatile("s_getpc_b64 %0\n.Lcode_here%=:\n\ts_mov_b32 %1, fused_code_end-.Lcode_here%=" : "=s"(pc), "=s"(len));
    u32 off = (idx * 8 + lane) * 128;
    if (lane < 8 && off < len) asm volatile("global_load_dword %0, %1, %2" : "=v"(pfdummy) : "v"(off), "s"(pc));
  }
  u32 vrow = lane < 56 ? lane * 16 : 0x7ffffff0u;
  u64 epoch;
#define TAG_READY() const u32 tlo = (u32)(epoch + 1), thi = (u32)((epoch + 1) >> 32); const v2u tagv = {tlo, thi}

  if (idx < 4) {
    const u32 h = xcd * 4 + idx;
    const rsrc_t rX = mk(P.x, L * D * 2), rWS = mk(P.ws, WS_END);
    const u32 srow = (h >> 1) * 128 + 112 + (h & 1) * 8 + w;
    v4u xs, xr[8];
    ld16p(xs, vrow, rX, srow * 896);
    asm volatile("s_load_dwordx2 %0, %1, 0x0" : "=s"(epoch) : "s"(P.ws));
#pragma unroll
    for (int i = 0; i < 8; ++i) ld16p(xr[i], vrow, rX, (h * 64 + w * 8 + i) * 896);
    asm volatile("s_waitcnt vmcnt(8) lgkmcnt(0)" : "+v"(xs), "+v"(pfdummy), "+s"(epoch)::"memory");
    TAG_READY();
    if (w == 1) { if (lane == 0) { __hip_atomic_store((GAS u64*)(P.ws + WS_DBG) + b, d_r0, __ATOMIC_RELAXED, __HIP_MEMORY_SCOPE_AGENT); } }
    rowstat(xs, lane, tlo, thi, rWS, srow);
    STAMP(0);
    float sv = 0.f, qv = 0.f;
#pragma unroll
    for (int i = 0; i < 8; ++i) {
      if (i == 0) asm volatile("s_waitcnt vmcnt(5)" : "+v"(xr[0]), "+v"(xr[1]), "+v"(xr[2]), "+v"(xr[3])::"memory");
      if (i == 4) asm volatile("s_waitcnt vmcnt(1)" : "+v"(xr[4]), "+v"(xr[5]), "+v"(xr[6]), "+v"(xr[7])::"memory");
      float s = 0.f, q = 0.f;
#pragma unroll
      for (int j = 0; j < 4; ++j) { s = dot2(xr[i][j], 0x3f803f80u, s); q = dot2(xr[i][j], xr[i][j], q); }
      wave_sum2(s, q);
      sv = lane == 48 + i ? s : sv; qv = lane == 48 + i ? q : qv;
    }
    const float mean = sv * (1.f / D), var = fmaxf(qv * (1.f / D) - mean * mean, 0.f);
    const float rdv = __builtin_amdgcn_rcpf(rbf(__builtin_amdgcn_sqrtf(rbf(rbf(var) + 1e-5f)))), mrv = -rbf(mean) * rdv;
#pragma unroll
    for (int i = 0; i < 8; ++i) {
      const float rd = __builtin_bit_cast(float, __builtin_amdgcn_readlane(__builtin_bit_cast(int, rdv), 48 + i));
      const float mr = __builtin_bit_cast(float, __builtin_amdgcn_readlane(__builtin_bit_cast(int, mrv), 48 + i));
      f32x2 rd2 = {rd, rd}, mr2 = {mr, mr};
      PINV(rd2); PINV(mr2);
      v4u o;
#pragma unroll
      for (int j = 0; j < 4; ++j) o[j] = pk2(__builtin_elementwise_fma((f32x2){bf_lo(xr[i][j]), bf_hi(xr[i][j])}, rd2, mr2));
      const u32 tok = w * 8 + i;
      if (lane < 56) lw<v4u>(tok * 896 + ((lane ^ ((tok >> 2) & 7)) << 4), o);
    }
    BARL();
    STAMP(1);
    const u32 t4 = lane & 15;
    const u32 ta = t4 * 4 * 896 + ((w ^ (t4 & 7)) << 4) + (lane >> 4) * 4, tv = (w * 4 + (lane >> 4)) * 8192 + t4 * 16;
#pragma unroll
    for (int i = 0; i < 7; ++i) {
      const u32 a = ta + i * 128;
      v4u o = {lr<u32>(a), lr<u32>(a + 896), lr<u32>(a + 1792), lr<u32>(a + 2688)};
      st16(o, tv, rWS, WS_XT + h * 256 + i * 32 * 8192);
    }
    STAMP(2);
    BARV(0);
    if (w == 0) if (lane == 0) st8(tagv, 0, rWS, WS_FX + h * 8);
    STAMP(3);
    if (h == 0 && w < 5) {
      u32 vo2 = w < 4 ? WS_FH + (tid >> 4) * 128 + ((tid & 15) < 14 ? (tid & 15) : 0) * 8 : WS_FX + (lane & 31) * 8;
      v2u got = {tlo + 1, 0};
#pragma unroll 1
      for (int spins = 0; spins < (1 << 14); ++spins) {
        if (__all(got.x == tlo && got.y == thi)) break;
        __builtin_amdgcn_s_sleep(32);
        asm volatile("buffer_load_dwordx2 %0, %1, %2, 0 offen sc1\n\ts_waitcnt vmcnt(0)" : "=&v"(got) : "v"(vo2), "s"(rWS) : "memory");
      }
    }
    if (h == 0) { BARL(); if (w == 0) if (lane == 0) st8(tagv, 0, rWS, WS_EPOCH); }
  } else {
    const u32 m = idx - 4, mq = (m * 37) >> 8, mr = m - mq * 7;
    const u32 rt = (xcd >> 1) * 4 + mq, ct = (xcd & 1) * 7 + mr;
    const u32 rt2 = xcd * 4 + mq, ct2 = mr;
    const u32 cp = xcd * 28 + m;
    const u32 l8 = lane >> 3, h = lane >> 5, l31 = lane & 31;
    const u32 rowp = w * 8 + l8;
    const u32 wslot = ((lane & 7) ^ ((lane >> 2) & 4)) << 4;
    u32 vX0 = (rt * 128 + rowp) * 896 + (((lane & 7) ^ ((w & 1) * 4 + (lane >> 4))) << 4), vX1 = vX0 + 64 * 896;
    u32 vW1 = rowp * 3584 + wslot + ct * 256;
    u32 vH = lane * 16;
    u32 mw = w * 1024;
    u32 srow = rt * 128 + ct * 8 + w, srowb = srow * 896, spar = cp * 1024;
    u32 sc[7];
    { u32 c = mq + mr; c = c >= 7 ? c - 7 : c; sc[0] = c * 128; sc[1] = c == 6 ? 0 : sc[0] + 128; }
    u32 sw0 = sc[0] * 1792;
    PINV(vX0); PINV(vX1); PINV(vW1); PINV(vH); PINV(vrow); PINS(mw); PINS(srowb); PINS(spar); PINS(sc[0]); PINS(sc[1]); PINS(sw0);
    const rsrc_t rX = mk(P.x, L * D * 2), rW1 = mk(P.w1, D * F * 2);
    v4u xs;
    ld16p(xs, vrow, rX, srowb);
    if (w < 4) {
      const char* ap = w == 0 ? P.logA : w == 1 ? P.Aim : w == 2 ? P.Cr : P.Ci;
      dma<LD_PAR>(mw, vH, mk(ap, D * 256 * 2), spar);
    }
    dma<0>(mw, vX0, rX, sc[0]); dma<PAGE>(mw, vX1, rX, sc[0]); dma<2 * PAGE>(mw, vW1, rW1, sw0);
    dma<3 * PAGE>(mw, vX0, rX, sc[1]); dma<4 * PAGE>(mw, vX1, rX, sc[1]);
    asm volatile("s_load_dwordx2 %0, %1, 0x0" : "=s"(epoch) : "s"(P.ws));
    SCHED;
    const rsrc_t rW2 = mk(P.w2, D * F * 2), rWS = mk(P.ws, WS_END);
    const u32 ldt2 = *(const CAS u32*)(uintptr_t)(P.logdt + cp * 4), dsk2 = *(const CAS u32*)(uintptr_t)(P.Dskip + cp * 4);
#pragma unroll
    for (int i = 2; i < 7; ++i) { sc[i] = sc[i - 1] == 768 ? 0 : sc[i - 1] + 128; PINS(sc[i]); }
    u32 sp[14];
    { u32 c = 2 * mr + 4 * mq; c = c >= 14 ? c - 14 : c; sp[0] = c * 8192; PINS(sp[0]); }
#pragma unroll
    for (int i = 1; i < 14; ++i) { sp[i] = sp[i - 1] == 13 * 8192 ? 0 : sp[i - 1] + 8192; PINS(sp[i]); }
    u32 vW1b = vW1 + 128, vW2a = rowp * 896 + wslot + ct2 * 128, vW2b = vW2a + 64 * 896;
    PINV(vW1b); PINV(vW2a); PINV(vW2b);
#define PAGE_(p) do { constexpr u32 lo_ = ((p) % NSLOT) * PAGE;                                      \
      if constexpr ((p) < 21) { if constexpr ((p) % 3 == 0) dma<lo_>(mw, vX0, rX, sc[(p) / 3]);       \
        else if constexpr ((p) % 3 == 1) dma<lo_>(mw, vX1, rX, sc[(p) / 3]);                          \
        else dma<lo_>(mw, vW1, rW1, sc[(p) / 3] * 1792); }                                            \
      else if constexpr ((p) < 28) dma<lo_>(mw, vW1b, rW1, sc[((p) - 21) % 7] * 1792);                \
      else dma<lo_>(mw, (p) < 42 ? vW2a : vW2b, rW2, sp[(p) % 14] * 14); } while (0)
    const u32 ch = w >> 2, w4 = w & 3;
    u32 xsel[4], xmask[2], aX, aY;
    {
      const bool last = l31 == 31;
      u32 qx, qy, ncol = (l31 + 1) & 31;
      if (w4 == 0) {
        xsel[3] = h ? 0x01000100u : 0x01000302u; xsel[2] = h ? 0x03020504u : 0x05040706u;
        xsel[1] = h ? 0x05040504u : 0x01000302u; xsel[0] = h ? 0x07060100u : 0x05040706u;
        xmask[1] = h ? 0u : ~0u; xmask[0] = h ? 0xffffu : ~0u;
        qx = 2 * h; qy = h ? (last ? 3 : 2) : 1; ncol = h ? ncol : l31;
      } else if (w4 == 1) {
        const u32 a = 0x04040404u * h, n = (h != 0) != last ? 0x05040504u : 0x01000100u;
        xsel[3] = 0x01000100u + a; xsel[2] = 0x01000302u + a; xsel[1] = 0x03020302u + a; xsel[0] = n;
        xmask[1] = 0u; xmask[0] = ~0u;
        qx = 3; qy = (h && last) ? 4 : 3;
      } else {
        const u32 a = (w4 & 1) * 2 + h, an = a + (last ? 1 : 0);
        const u32 sa = 0x01000100u + 0x02020202u * a, sn = 0x01000100u + 0x02020202u * (an & 3);
        xsel[3] = sa; xsel[2] = sa; xsel[0] = sn;
        xmask[1] = (lane & 1) ? ~0u : 0xffffu; xmask[0] = ~0u;
        xsel[1] = (sn & xmask[1]) | (sa & ~xmask[1]);
        qx = 4; qy = 4 + (an >> 2);
      }
      aX = LD_SC + ch * 4 * 4352 + l31 * 128 + ((qx ^ (l31 & 7)) << 4);
      aY = LD_SC + ch * 4 * 4352 + ncol * 128 + ((qy ^ (ncol & 7)) << 4);
    }
    const u32 tt = w & 3, fq = w >> 2;
    u32 ax[2][4], aw[2][4];
#pragma unroll
    for (int j = 0; j < 4; ++j) {
      const u32 ks = j ^ tt;
      ax[0][j] = (tt >> 1) * PAGE + ((tt & 1) * 32 + l31) * 128 + (((2 * ks + h) ^ ((l31 >> 1) & 7)) << 4);
      aw[0][j] = (16 * ks + 8 * h + ((lane >> 2) & 3)) * 128 + ((fq * 64 + ((lane >> 4) & 1) * 32 + (lane & 3) * 8) ^ (((lane >> 3) & 1) << 6));
      ax[1][j] = ax[0][j] + 7 * PAGE; aw[1][j] = aw[0][j] + 7 * PAGE;
      PINV(ax[0][j]); PINV(ax[1][j]); PINV(aw[0][j]); PINV(aw[1][j]);
    }
    u32 sw = LD_SC + w * 4352 + lane * 4, sr = LD_SC + w * 4352 + (lane & 15) * 272 + (lane >> 4) * 16, apar = LD_PAR + tid * 2;
    PINV(sw); PINV(sr); PINV(apar);
    float dt = rbf(expf(ch ? bf_hi(ldt2) : bf_lo(ldt2)));
    PINV(dt);
    STAMP(0);
    asm volatile("s_waitcnt vmcnt(5) lgkmcnt(0)\n\ts_barrier" : "+v"(pfdummy), "+v"(xs), "+s"(epoch)::"memory");
    TAG_READY();
    if (w == 1) { if (lane == 0) { __hip_atomic_store((GAS u64*)(P.ws + WS_DBG) + b, d_r0, __ATOMIC_RELAXED, __HIP_MEMORY_SCOPE_AGENT); } }
    STAMP(1);
    const float lA = bf_lo(lr<u16>(apar)), ai = bf_lo(lr<u16>(apar + 1024)), cr = bf_lo(lr<u16>(apar + 2048)), ci = bf_lo(lr<u16>(apar + 3072));
    rowstat(xs, lane, tlo, thi, rWS, srow);
    PAGE_(5);
    if (w == 4) { if (lane < 16) dma<LD_BIAS - 4096>(mw, vH, mk(P.b1, F * 2), ct * 256); }
    else if (w == 5) { if (lane < 8) dma<LD_BIAS + 256 - 5120>(mw, vH, mk(P.b2, D * 2), ct2 * 128); }
    const float a2 = -rbf(__builtin_amdgcn_exp2f(lA * 1.44269504f)) * dt * 1.44269504f, br = ai * dt * 0.15915494f;
    f32x2 tb[16];
    const f32x2 z8 = cpow(a2 * 8.f, br * 8.f), z16 = cmul(z8, z8);
    tb[0] = (f32x2){1.f, 0.f}; tb[1] = z8; tb[2] = z16;
#pragma unroll
    for (int j = 3; j < 16; ++j) tb[j] = cmul(tb[j - 2], z16);
    SCHED; PAGE_(6); SCHED;
    v4u xf[28], wf[4], lo[2][4], hi[4], ko;
    f32x16 acc;
#pragma unroll
    for (int i = 0; i < 16; ++i) acc[i] = 0.f;
    float cs = 0.f;
#define RD_X(i, j) xf[4 * (i) + (j)] = lr<v4u>(ax[((3 * (i)) % NSLOT) / 7][j] + (((3 * (i)) % NSLOT) % 7) * PAGE)
#define RD_W(p, j) trr2(aw[((p) % NSLOT) / 7][j] + (((p) % NSLOT) % 7) * PAGE, aw[((p) % NSLOT) / 7][j] + (((p) % NSLOT) % 7) * PAGE + 512)
#define CHUNK_A(i, NW)                                                                   \
    BARV(NW);                                                                            \
    _Pragma("unroll") for (int j = 0; j < 4; ++j) { wf[j] = RD_W(3 * (i) + 2, j); RD_X(i, j); } \
    PAGE_(3 * (i) + 7);                                                                  \
    _Pragma("unroll") for (int e = 0; e < 4; ++e) cs = dot2(wf[0][e], 0x3f803f80u, cs);  \
    SCHED;                                                                               \
    _Pragma("unroll") for (int j = 0; j < 4; ++j) acc = mfma32(wf[j], xf[4 * (i) + j], acc); \
    SCHED
#define TAB_OUT(n) _Pragma("unroll") for (int j = 0; j < n; ++j) lw<u32>(sw + j * 272, pk2(tb[j]))
#define TAB_IN(dst) _Pragma("unroll") for (int t = 0; t < 4; ++t) dst[t] = lr<v4u>(sr + t * 64)
    STAMP(2);
    CHUNK_A(0, 5);
    TAB_OUT(16); TAB_IN(lo[0]); SCHED; PAGE_(8);
    tb[0] = cmul(tb[14], z16); tb[1] = cmul(tb[15], z16);
#pragma unroll
    for (int j = 2; j < 16; ++j) tb[j] = cmul(tb[j - 2], z16);
    SCHED; PAGE_(9);
    CHUNK_A(1, 4);
    TAB_OUT(16); TAB_IN(lo[1]);
    const f32x2 z1 = cpow(a2, -br), z256 = cpow(a2 * 256.f, br * -256.f);
    { float c0 = 2.f * dt * cr, c1 = -2.f * dt * ci; asm("" : "+v"(c0)); asm("" : "+v"(c1)); tb[0] = (f32x2){c0, c1}; }
    SCHED; PAGE_(11);
#pragma unroll
    for (int j = 0; j < 7; ++j) { tb[j + 1] = cmul(tb[j], z1); if (!(j & 1)) tb[8 + j / 2] = cmul(tb[j], z256); }
    tb[12] = cmul(tb[8], z256); tb[13] = cmul(tb[10], z256); tb[14] = cmul(tb[12], z256); tb[15] = cmul(tb[13], z256);
    SCHED; PAGE_(12);
    STAMP(3);
    CHUNK_A(2, 4);
    TAB_OUT(16); TAB_IN(hi); SCHED; PAGE_(14);
    tb[0] = cmul(tb[14], z256);
#pragma unroll
    for (int j = 1; j < 5; ++j) tb[j] = cmul(tb[j - 1], z256);
    {
      f32x4 kd[2][2];
      v4u hi2[4];
      TAB_OUT(5); TAB_IN(hi2);
      SCHED;
#pragma unroll
      for (int t = 0; t < 4; ++t)
#pragma unroll
        for (int lt = 0; lt < 2; ++lt) kd[0][lt] = mfma16(hi[t], lo[lt][t], t ? kd[0][lt] : (f32x4){0.f, 0.f, 0.f, 0.f});
#pragma unroll
      for (int t = 0; t < 4; ++t)
#pragma unroll
        for (int lt = 0; lt < 2; ++lt) kd[1][lt] = mfma16(hi2[t], lo[lt][t], t ? kd[1][lt] : (f32x4){0.f, 0.f, 0.f, 0.f});
      const u32 pw = LD_SC + w * 4352 + (lane & 15) * 128;
#pragma unroll
      for (int ht = 0; ht < 2; ++ht)
#pragma unroll
        for (int lt = 0; lt < 2; ++lt) lw<f32x4>(pw + lt * 2048 + (((ht * 4 + (lane >> 4)) ^ (lane & 7)) << 4), kd[ht][lt]);
    }
    SCHED; PAGE_(15);
    STAMP(4);
    CHUNK_A(3, 4);
    {
      f32x4 X = lr<f32x4>(aX), Y = lr<f32x4>(aY);
#pragma unroll
      for (int s = 1; s < 4; ++s) { X += lr<f32x4>(aX + s * 4352); Y += lr<f32x4>(aY + s * 4352); }
      PAGE_(17);
      const u32 x01 = pk(X[0], X[1]), x23 = pk(X[2], X[3]), y01 = pk(Y[0], Y[1]), y23 = pk(Y[2], Y[3]);
      ko[3] = __builtin_amdgcn_perm(x23, x01, xsel[3]); ko[2] = __builtin_amdgcn_perm(x23, x01, xsel[2]);
#pragma unroll
      for (int mm = 0; mm < 2; ++mm) ko[mm] = (__builtin_amdgcn_perm(y23, y01, xsel[mm]) & xmask[mm]) | (__builtin_amdgcn_perm(x23, x01, xsel[mm]) & ~xmask[mm]);
    }
    SCHED; PAGE_(18);
    CHUNK_A(4, 4);
    {
      const u32 ka = LD_K + ch * KCH + (255 - (tid & 255)) * 16;
      const u32 e1 = __builtin_amdgcn_alignbit(ko[1], ko[0], 16), e2 = __builtin_amdgcn_alignbit(ko[2], ko[1], 16), e3 = __builtin_amdgcn_alignbit(ko[3], ko[2], 16);
      lw<v4u>(ka, ko);
      lw<u16>(ka + 4160 + 2, (u16)ko[0]); lw<u32>(ka + 4160 + 4, e1); lw<v2u>(ka + 4160 + 8, (v2u){e2, e3}); lw<u16>(ka + 4160 + 16, (u16)(ko[3] >> 16));
      lw<u32>(ka + 8320 + 4, ko[0]); lw<v2u>(ka + 8320 + 8, (v2u){ko[1], ko[2]}); lw<u32>(ka + 8320 + 16, ko[3]);
      lw<u16>(ka + 12480 + 6, (u16)ko[0]); lw<v2u>(ka + 12480 + 8, (v2u){e1, e2}); lw<u32>(ka + 12480 + 16, e3); lw<u16>(ka + 12480 + 20, (u16)(ko[3] >> 16));
      if (w4 == 0) lw<u16>(LD_K + ch * KCH + (lane >> 4) * 4162 + 4096 + (lane & 15) * 2, (u16)0);
      PAGE_(20);
    }
    SCHED; PAGE_(21);
    STAMP(5);
    CHUNK_A(5, 4);
    v4u gran; u32 vgr = l31 * 16; const u32 sgr = WS_STAT + (rt * 128 + tt * 32) * 16;
    ld16(gran, vgr, rWS, sgr);
    PAGE_(23); PAGE_(24);
    CHUNK_A(6, 5);
    PAGE_(26); PAGE_(27);
    const u32 acs = LD_CS + fq * 256 + lane * 4;
    lw<float>(acs + tt * 512, cs);
    STAMP(6);
    asm volatile("s_waitcnt vmcnt(5) lgkmcnt(0)\n\ts_barrier" : "+v"(gran)::"memory");
#pragma unroll 1
    for (int spins = 0; spins < (1 << 16); ++spins) {
      if (__all(gran[0] == tlo && gran[2] == thi)) break;
      __builtin_amdgcn_s_sleep(2);
      asm volatile("buffer_load_dwordx4 %0, %1, %2, %3 offen sc1\n\ts_waitcnt vmcnt(0)" : "=&v"(gran) : "v"(vgr), "s"(rWS), "s"(sgr) : "memory");
    }
    STAMP(7);
    v4u wn[4];
#pragma unroll
    for (int j = 0; j < 4; ++j) wf[j] = RD_W(21, j);
    auto make_aext = [&](int st) {
      float c = lr<float>(acs + st * 2048);
#pragma unroll
      for (int s = 1; s < 4; ++s) c += lr<float>(acs + st * 2048 + s * 512);
      const u32 b1v = h ? 0u : (u32)lr<u16>(LD_BIAS + st * 128 + fq * 64 + l31 * 2);
      const u32 chh = pk(c, c); const float cl = c - bf_lo(chh);
      return (v4u){chh, pk(cl, cl), b1v * 0x10001u, 0u};
    };
    const v4u bext = {gran[1], gran[1], gran[3], 0u};
    GeluC gc;
    { gc.ir = bf_lo(gran[3]) + bf_hi(gran[3]); const float rs = __builtin_amdgcn_rcpf(gc.ir); gc.k0 = -2.3022082f * rs; gc.k1 = -0.10294324f * rs * rs * rs; }
    const u32 sHst = WS_H + ((rt * 4 + tt) * 112 + ct * 8 + fq * 2) * 1024;
    v2u pollx; const u32 vpx = l31 * 8;
    PAGE_(28);
    acc = mfma32(make_aext(0), bext, acc);
    f32x16 accb;
#pragma unroll
    for (int i = 0; i < 16; ++i) accb[i] = 0.f;
    cs = 0.f;
    float gl[8];
    v4u ureg;
#define STEP_B(i, cur, nxt)                                                              \
    SCHED;                                                                               \
    if (i < 6) { _Pragma("unroll") for (int j = 0; j < 4; ++j) nxt[j] = RD_W(22 + (i), j); } \
    _Pragma("unroll") for (int e = 0; e < 4; ++e) cs = dot2(cur[0][e], 0x3f803f80u, cs); \
    SCHED;                                                                               \
    _Pragma("unroll") for (int j = 0; j < 4; ++j) accb = mfma32(cur[j], xf[4 * (i) + j], accb); \
    SCHED
#define GELU_A(j0, n) _Pragma("unroll") for (int j = j0; j < j0 + n; ++j) gl[j & 7] = gelu1(acc[j], gc)
#define H_OUT(off) st16((v4u){pkq(gl[0], gl[1]), pkq(gl[2], gl[3]), pkq(gl[4], gl[5]), pkq(gl[6], gl[7])}, vH, rWS, sHst + (off))
    STEP_B(0, wf, wn); GELU_A(0, 3); PAGE_(29);
    BARV(2);
    STEP_B(1, wn, wf); GELU_A(3, 3); PAGE_(30);
    STEP_B(2, wf, wn); GELU_A(6, 2); H_OUT(0); PAGE_(31);
    STEP_B(3, wn, wf); GELU_A(8, 3); PAGE_(32); ld8(pollx, vpx, rWS, WS_FX);
    STEP_B(4, wf, wn); GELU_A(11, 3); PAGE_(33);
    STEP_B(5, wn, wf); GELU_A(14, 2); H_OUT(1024); PAGE_(34);
    STEP_B(6, wf, wn);
    lw<float>(acs + 2048 + tt * 512, cs);
    STAMP(8);
    asm volatile("s_waitcnt vmcnt(3) lgkmcnt(0)\n\ts_barrier" : "+v"(pollx)::"memory");
    poll_finish(pollx, vpx, rWS, WS_FX, tlo, thi);
    ld16(ureg, tid * 16, rWS, WS_XT + cp * 8192);
    PAGE_(35); PAGE_(36);
    STAMP(9);
    {
      accb = mfma32(make_aext(1), bext, accb);
#pragma unroll
      for (int s = 0; s < 2; ++s) {
#pragma unroll
        for (int j = 0; j < 8; ++j) gl[j] = gelu1(accb[8 * s + j], gc);
        if (s == 0) { PAGE_(37); PAGE_(38); }
        st16((v4u){pk(gl[0], gl[1]), pk(gl[2], gl[3]), pk(gl[4], gl[5]), pk(gl[6], gl[7])}, vH, rWS, sHst + 4096 + s * 1024);
        SCHED;
      }
    }
    STAMP(10);
    WAITV_R(6, ureg);
    lw<v2u>(LD_U + 512 + tid * 8, (v2u){__builtin_amdgcn_perm(ureg[1], ureg[0], 0x05040100u), __builtin_amdgcn_perm(ureg[3], ureg[2], 0x05040100u)});
    lw<v2u>(LD_U + 4608 + 512 + tid * 8, (v2u){__builtin_amdgcn_perm(ureg[1], ureg[0], 0x07060302u), __builtin_amdgcn_perm(ureg[3], ureg[2], 0x07060302u)});
    lw<u16>(LD_U + ch * 4608 + (tid & 255) * 2, (u16)0);
    BARL();
    if (w == 0) if (lane == 0) st8(tagv, 0, rWS, WS_FH + rt * 128 + ct * 8);
    PAGE_(39); PAGE_(40); PAGE_(41);
    STAMP(11);
    const u32 t2 = w >> 2, kq = w & 3;
    u32 vHa = vH + WS_H + ((rt2 * 2 + t2) * 112 + kq) * 1024, vHb = vHa + 4096;
    PINV(vHa); PINV(vHb);
    v4u hf[28];
#define HLOAD(j) ld16(hf[j], (j) < 14 ? vHa : vHb, rWS, sp[(j) % 14])
    v2u pollh; const u32 vph = (lane < 14 ? lane : 0) * 8, sph = WS_FH + (rt2 >> 1) * 128;
    f32x4 ya[8];
    const u32 r16 = lane & 15, g4 = lane >> 4;
    u32 kb = LD_K + ch * KCH + ((r16 + 1) & 3) * 4162 + (2031 - r16 + 8 * g4) * 2 - 7 * 512 - 64 - w4 * 128;
    u32 ub = LD_U + ch * 4608 + 512 - 32 - 64 + r16 * 32 + g4 * 16 - w4 * 128;
    PINV(kb); PINV(ub);
    {
      v4u B[2][8], A[2];
      auto rdA = [&](int n) { const u32 a = kb + (1 - n / 8) * 64 + (7 - n % 8) * 512; const v2u x = lrv<v2u>(a), y = lrv<v2u>(a + 8); return (v4u){x.x, x.y, y.x, y.y}; };
      A[0] = rdA(0);
#pragma unroll
      for (int T = 0; T < 8; ++T) B[0][T] = lr<v4u>(ub + 64 + T * 512);
#pragma unroll
      for (int n = 0; n < 16; ++n) {
        const int it = n / 8, a = n % 8;
        SCHED;
        if (n == 8) {
          asm volatile("s_waitcnt vmcnt(0) lgkmcnt(0)\n\ts_barrier" : "+v"(pollh)::"memory");
          if (w == 0) if (lane == 0) st8(tagv, 0, rWS, WS_FH + 2048 + rt * 128 + ct * 8);
          poll_finish(pollh, vph, rWS, sph, tlo, thi);
          STAMP(12);
        }
        if (n < 15) A[(n + 1) & 1] = rdA(n + 1);
        if (n < 8) B[1][7 - n] = lr<v4u>(ub + (7 - n) * 512);
        if (n == 3) ld8(pollh, vph, rWS, sph);
        if (n >= 8 && n < 13) { HLOAD(2 * (n - 8)); }
        SCHED;
#pragma unroll
        for (int T = 7; T >= a; --T) ya[T] = mfma16(A[n & 1], B[it][T - a], n ? ya[T] : (f32x4){0.f, 0.f, 0.f, 0.f});
        if (n >= 8 && n < 13) { SCHED; HLOAD(2 * (n - 8) + 1); }
      }
    }
    STAMP(13);
    const v4u xn = lr<v4u>(LD_U + ch * 4608 + 512 + (tid & 255) * 16);
    u32 yp[16];
#pragma unroll
    for (int T = 0; T < 8; ++T) { yp[2 * T] = pk(ya[T][0], ya[T][1]); yp[2 * T + 1] = pk(ya[T][2], ya[T][3]); }
    HLOAD(10); HLOAD(11);
    BARL();
    {
      const u32 ra = LD_RED + w * 4096 + r16 * 32 + g4 * 8;
#pragma unroll
      for (int T = 0; T < 8; ++T) lw<v2u>(ra + T * 512, (v2u){yp[2 * T], yp[2 * T + 1]});
    }
    HLOAD(12); HLOAD(13);
    f32x16 g0, g1;
#pragma unroll
    for (int i = 0; i < 16; ++i) { g0[i] = 0.f; g1[i] = 0.f; }
    u32 a2w[2][2];
    a2w[0][0] = (16 * kq + 4 * h + ((lane >> 2) & 3)) * 128 + ((((lane >> 4) & 1) * 32 + (lane & 3) * 8) ^ (((lane >> 3) & 1) << 6));
    a2w[0][1] = a2w[0][0] ^ 64; a2w[1][0] = a2w[0][0] + 7 * PAGE; a2w[1][1] = a2w[0][1] + 7 * PAGE;
    PINV(a2w[0][0]); PINV(a2w[0][1]); PINV(a2w[1][0]); PINV(a2w[1][1]);
    v4u wa[2][2], wb[2][2];
#define RD_W2(j, d) trr2(a2w[((j) % NSLOT) / 7][d] + (((j) % NSLOT) % 7) * PAGE, a2w[((j) % NSLOT) / 7][d] + (((j) % NSLOT) % 7) * PAGE + 1024)
#define RD_G(dst, j) dst[0][0] = RD_W2(j, 0); dst[0][1] = RD_W2(j, 1); dst[1][0] = RD_W2((j) + 1, 0); dst[1][1] = RD_W2((j) + 1, 1)
#define MM_G(src, j) SCHED; g0 = mfma32(src[0][0], hf[j], g0); g1 = mfma32(src[0][1], hf[j], g1); g0 = mfma32(src[1][0], hf[(j) + 1], g0); g1 = mfma32(src[1][1], hf[(j) + 1], g1); SCHED
#define HWAIT(n, j) asm volatile("s_waitcnt vmcnt(" #n ")" : "+v"(hf[j]), "+v"(hf[(j) + 1]))
    RD_G(wa, 0);
    BARL();
    STAMP(14);
    v4u yq[4];
#pragma unroll
    for (int s = 0; s < 4; ++s) yq[s] = lr<v4u>(LD_RED + (ch * 4 + s) * 4096 + (tid & 255) * 16);
    float yo[8];
    const float dsk = ch ? bf_hi(dsk2) : bf_lo(dsk2);
    RD_G(wb, 2); HWAIT(12, 0); MM_G(wa, 0);
#pragma unroll
    for (int i = 0; i < 4; ++i) { yo[2 * i] = bf_lo(yq[0][i]) + bf_lo(yq[1][i]); yo[2 * i + 1] = bf_hi(yq[0][i]) + bf_hi(yq[1][i]); }
    ld8(pollh, vph, rWS, sph + 2048);
    RD_G(wa, 4); HWAIT(11, 2); MM_G(wb, 2);
#pragma unroll
    for (int i = 0; i < 4; ++i) { yo[2 * i] += bf_lo(yq[2][i]); yo[2 * i + 1] += bf_hi(yq[2][i]); yo[2 * i] += bf_lo(yq[3][i]); }
    RD_G(wb, 6); HWAIT(9, 4); MM_G(wa, 4);
#pragma unroll
    for (int i = 0; i < 4; ++i) { yo[2 * i + 1] += bf_hi(yq[3][i]); yo[2 * i] = __builtin_fmaf(bf_lo(xn[i]), dsk, yo[2 * i]); yo[2 * i + 1] = __builtin_fmaf(bf_hi(xn[i]), dsk, yo[2 * i + 1]); }
    st16((v4u){pkq(yo[0], yo[1]), pkq(yo[2], yo[3]), pkq(yo[4], yo[5]), pkq(yo[6], yo[7])}, vH, rWS, WS_ST + cp * 8192 + mw);
    asm volatile("s_waitcnt vmcnt(1) lgkmcnt(0)\n\ts_barrier" : "+v"(hf[6]), "+v"(hf[7]), "+v"(hf[8]), "+v"(hf[9]), "+v"(hf[10]), "+v"(hf[11]), "+v"(hf[12]), "+v"(hf[13]), "+v"(pollh)::"memory");
    PAGE_(42); PAGE_(43);
    poll_finish(pollh, vph, rWS, sph + 2048, tlo, thi);
    STAMP(15);
    RD_G(wa, 8); HLOAD(14); HLOAD(15); MM_G(wb, 6); PAGE_(44); PAGE_(45);
    RD_G(wb, 10); HLOAD(16); HLOAD(17); MM_G(wa, 8); PAGE_(46); PAGE_(47);
    RD_G(wa, 12); HLOAD(18); HLOAD(19); MM_G(wb, 10); PAGE_(48); PAGE_(49);
    BARV(8);
    if (w == 0) if (lane == 0) st8(tagv, 0, rWS, WS_FS + cp * 8);
    STAMP(16);
    RD_G(wb, 14); HLOAD(20); HLOAD(21); MM_G(wa, 12); PAGE_(50); PAGE_(51);
    RD_G(wa, 16); HLOAD(22); HLOAD(23); HWAIT(63, 14); MM_G(wb, 14); PAGE_(52); PAGE_(53);
    BARV(8);
    RD_G(wb, 18); HLOAD(24); HLOAD(25); HWAIT(63, 16); MM_G(wa, 16); PAGE_(54); PAGE_(55);
    RD_G(wa, 20); HLOAD(26); HLOAD(27); HWAIT(63, 18); MM_G(wb, 18);
    v2u polls; ld8(polls, vpx, rWS, WS_FS + ct2 * 256);
    v4u xin; const u32 vout = (tid >> 3) * 896 + (tid & 7) * 16, sout = rt2 * 64 * 896 + ct2 * 128;
    ld16p(xin, vout, rX, sout);
    BARV(8);
    STAMP(17);
    RD_G(wb, 22); HWAIT(63, 20); MM_G(wa, 20);
    RD_G(wa, 24); HWAIT(63, 22); MM_G(wb, 22);
    asm volatile("s_waitcnt vmcnt(0) lgkmcnt(0)\n\ts_barrier" : "+v"(hf[24]), "+v"(hf[25]), "+v"(hf[26]), "+v"(hf[27]), "+v"(polls), "+v"(xin)::"memory");
    poll_finish(polls, vpx, rWS, WS_FS + ct2 * 256, tlo, thi);
    v4u sreg; ld16(sreg, (tid >> 3) * 4096 + (tid & 7) * 16, rWS, WS_ST + ct2 * 64 * 4096 + rt2 * 128);
    STAMP(18);
    RD_G(wb, 26); MM_G(wa, 24); MM_G(wb, 26);
    {
      const u32 pa = kq * 16384 + (t2 * 32 + l31) * 256;
#pragma unroll
      for (int qd = 0; qd < 4; ++qd) lw<f32x4>(pa + (((2 * qd + h) ^ (lane & 7)) << 4), (f32x4){g0[4 * qd], g0[4 * qd + 1], g0[4 * qd + 2], g0[4 * qd + 3]});
#pragma unroll
      for (int qd = 0; qd < 4; ++qd) lw<f32x4>(pa + 128 + (((2 * qd + h) ^ (lane & 7)) << 4), (f32x4){g1[4 * qd], g1[4 * qd + 1], g1[4 * qd + 2], g1[4 * qd + 3]});
      WAITV_R(0, sreg);
      lw<v4u>(LD_RED + (tid >> 3) * 128 + (((tid & 7) ^ w) << 4), sreg);
    }
    STAMP(19);
    BARL();
    {
      const u32 tok = tid >> 3, d8 = tid & 7;
      const u32 ra = tok * 256 + (((2 * d8) ^ (tok & 7)) << 4);
      f32x4 o0 = lr<f32x4>(ra), o1 = lr<f32x4>(ra ^ 16);
#pragma unroll
      for (int s = 1; s < 4; ++s) { o0 += lr<f32x4>(ra + s * 16384); o1 += lr<f32x4>((ra ^ 16) + s * 16384); }
      const v4u b2v = lr<v4u>(LD_BIAS + 256 + d8 * 16);
      const u32 sa = LD_RED + d8 * 1024 + ((w ^ d8) << 4) + (tok & 7) * 2;
      float o[8] = {o0[0], o0[1], o0[2], o0[3], o1[0], o1[1], o1[2], o1[3]};
      v4u res;
#pragma unroll
      for (int i = 0; i < 4; ++i) {
        const float v0 = o[2 * i] + bf_lo(b2v[i]), v1 = o[2 * i + 1] + bf_hi(b2v[i]);
        const float s0 = bf_lo(lr<u16>(sa + (2 * i) * 128)) * v0, s1 = bf_lo(lr<u16>(sa + (2 * i + 1) * 128)) * v1;
        const float r0 = __builtin_amdgcn_rcpf(1.f + __builtin_amdgcn_exp2f(v0 * -1.44269504f)), r1 = __builtin_amdgcn_rcpf(1.f + __builtin_amdgcn_exp2f(v1 * -1.44269504f));
        res[i] = pk(__builtin_fmaf(s0, r0, bf_lo(xin[i])), __builtin_fmaf(s1, r1, bf_hi(xin[i])));
      }
      *(GAS v4u*)(P.out + sout + vout) = res;
    }
    STAMP(20);
  }
  {
    asm volatile("s_waitcnt vmcnt(0)" ::: "memory");
    const u64 d_r1 = __builtin_amdgcn_s_memrealtime(), d_t1 = __builtin_amdgcn_s_memtime();
    if (b == DBLK && w == 0) {
      u64 mn = ~0ull, mx = 0;
      for (int i = 0; i < 4; ++i) { u64 v = __hip_atomic_load((GAS u64*)(P.ws + WS_DBG) + lane + 64 * i, __ATOMIC_RELAXED, __HIP_MEMORY_SCOPE_AGENT); mn = v < mn ? v : mn; mx = v > mx ? v : mx; }
      for (int o = 32; o; o >>= 1) { u64 a = __shfl_xor(mn, o), c = __shfl_xor(mx, o); mn = a < mn ? a : mn; mx = c > mx ? c : mx; }
      u64 val = DIAG == 0 ? 0 : DIAG == 1 ? mx - mn : DIAG == 2 ? d_r1 - mn : DIAG == 3 ? d_r1 - d_r0 : DIAG == 4 ? (d_t1 - d_t0) / 24 : DIAG == 5 ? d_r0 - mn : r_mark - d_r0;
      if (val > 20000) val = 20000;
      const u64 until = mn + 6000 + 10 * val;
      for (int sp = 0; sp < 100000000; ++sp) { if (__builtin_amdgcn_s_memrealtime() >= until) break; }
    }
  }
  asm volatile("fused_code_end:");
}

extern "C" void kernel_launch(void* const* d_in, const int* in_sizes, int n_in, void* d_out, int out_size, void* d_ws, size_t ws_size, hipStream_t stream) {
  Params P;
  P.x = (const char*)d_in[0]; P.logA = (const char*)d_in[1]; P.Aim = (const char*)d_in[2]; P.Cr = (const char*)d_in[3]; P.Ci = (const char*)d_in[4];
  P.logdt = (const char*)d_in[5]; P.Dskip = (const char*)d_in[6]; P.w1 = (const char*)d_in[8]; P.b1 = (const char*)d_in[9];
  P.w2 = (const char*)d_in[10]; P.b2 = (const char*)d_in[11]; P.out = (char*)d_out; P.ws = (char*)d_ws;
  fused_kernel<<<256, 512, 0, stream>>>(P);
}
```

```cpp
#include <hip/hip_runtime.h>
#include <stdint.h>

typedef unsigned int u32;
typedef unsigned long long u64;
typedef unsigned short u16;
typedef u32 v4u __attribute__((ext_vector_type(4)));
typedef u32 v2u __attribute__((ext_vector_type(2)));
typedef float f32x2 __attribute__((ext_vector_type(2)));
typedef float f32x4 __attribute__((ext_vector_type(4)));
typedef float f32x16 __attribute__((ext_vector_type(16)));
typedef __bf16 bf16x2 __attribute__((ext_vector_type(2)));
typedef __bf16 bf16x8 __attribute__((ext_vector_type(8)));
typedef short s16x4 __attribute__((ext_vector_type(4)));
typedef __amdgpu_buffer_rsrc_t rsrc_t;
#define LAS __attribute__((address_space(3)))
#define GAS __attribute__((address_space(1)))
#define CAS __attribute__((address_space(4)))
#define DI __device__ __forceinline__
#define SCHED __builtin_amdgcn_sched_barrier(0)
#define PINV(x) asm volatile("" : "+v"(x))
#define PINS(x) asm volatile("" : "+s"(x))

constexpr int L = 2048, D = 448, F = 1792;
constexpr u32 WS_EPOCH = 0, WS_FX = 256, WS_FH = 1024, WS_FS = 8192, WS_STAT = 16384, WS_DBG = 65536;
constexpr u32 WS_XT = 1u << 20;
constexpr u32 WS_ST = WS_XT + L * D * 2;
constexpr u32 WS_H = WS_ST + L * D * 2;
constexpr u32 WS_END = WS_H + L * F * 2;
constexpr u32 PAGE = 8192, NSLOT = 14;
constexpr u32 LD_K = NSLOT * PAGE;
constexpr u32 KCH = 4 * 4160;
constexpr u32 LD_U = LD_K + 2 * KCH;
constexpr u32 LD_SC = LD_K;
constexpr u32 LD_PAR = LD_SC + 8 * 4352;
constexpr u32 LD_RED = LD_K;
constexpr u32 LD_BIAS = LD_U + 2 * 4608;
constexpr u32 LD_CS = LD_BIAS + 512;
constexpr u32 LDS_BYTES = 163840;
static_assert(LD_CS + 4096 <= LDS_BYTES, "lds");
static_assert(LD_PAR + 4096 <= LD_BIAS, "lds");

struct Params {
  const char *x, *logA, *Aim, *Cr, *Ci, *logdt, *Dskip, *w1, *b1, *w2, *b2;
  char *out, *ws;
};

DI float bf_lo(u32 v) { return __uint_as_float(v << 16); }
DI float bf_hi(u32 v) { return __uint_as_float(v & 0xffff0000u); }
DI u32 pk2(f32x2 f) { return __builtin_bit_cast(u32, __builtin_convertvector(f, bf16x2)); }
DI u32 pk(float lo, float hi) { return pk2((f32x2){lo, hi}); }
DI u32 pkq(float lo, float hi) { asm("" : "+v"(lo)); asm("" : "+v"(hi)); return pk2((f32x2){lo, hi}); }
DI float rbf(float x) { return bf_lo(pk(x, x)); }
DI float dot2(u32 a, u32 b, float c) { return __builtin_amdgcn_fdot2_f32_bf16(__builtin_bit_cast(bf16x2, a), __builtin_bit_cast(bf16x2, b), c, false); }
DI f32x16 mfma32(v4u a, v4u b, f32x16 c) { return __builtin_amdgcn_mfma_f32_32x32x16_bf16(__builtin_bit_cast(bf16x8, a), __builtin_bit_cast(bf16x8, b), c, 0, 0, 0); }
DI f32x4 mfma16(v4u a, v4u b, f32x4 c) { return __builtin_amdgcn_mfma_f32_16x16x32_bf16(__builtin_bit_cast(bf16x8, a), __builtin_bit_cast(bf16x8, b), c, 0, 0, 0); }
DI rsrc_t mk(const void* p, u32 bytes) { return __builtin_amdgcn_make_buffer_rsrc((void*)p, 0, bytes, 0x00020000); }
template <class T> DI T lr(u32 a) { return *(const LAS T*)(uintptr_t)a; }
template <class T> DI T lrv(u32 a) { return *(const volatile LAS T*)(uintptr_t)a; }
template <class T> DI void lw(u32 a, T v) { *(LAS T*)(uintptr_t)a = v; }
DI v2u trr(u32 a) { return __builtin_bit_cast(v2u, __builtin_amdgcn_ds_read_tr16_b64_v4i16((LAS s16x4*)(uintptr_t)a)); }
DI v4u trr2(u32 a0, u32 a1) { v2u x = trr(a0), y = trr(a1); return (v4u){x.x, x.y, y.x, y.y}; }
template <u32 LOFF> DI void dma(u32 mw, u32 voff, rsrc_t r, u32 soff) {
  asm volatile("s_add_i32 m0, %0, %4\n\ts_nop 0\n\tbuffer_load_dwordx4 %1, %2, %3 offen lds" ::"s"(mw), "v"(voff), "s"(r), "s"(soff), "i"(LOFF) : "m0", "scc");
}
DI void ld16(v4u& d, u32 voff, rsrc_t r, u32 soff) { asm volatile("buffer_load_dwordx4 %0, %1, %2, %3 offen sc1" : "=v"(d) : "v"(voff), "s"(r), "s"(soff)); }
DI void ld16p(v4u& d, u32 voff, rsrc_t r, u32 soff) { asm volatile("buffer_load_dwordx4 %0, %1, %2, %3 offen" : "=v"(d) : "v"(voff), "s"(r), "s"(soff)); }
DI void ld8(v2u& d, u32 voff, rsrc_t r, u32 soff) { asm volatile("buffer_load_dwordx2 %0, %1, %2, %3 offen sc1" : "=v"(d) : "v"(voff), "s"(r), "s"(soff)); }
DI void st16(v4u d, u32 voff, rsrc_t r, u32 soff) { asm volatile("buffer_store_dwordx4 %0, %1, %2, %3 offen sc1\n\ts_nop 1" ::"v"(d), "v"(voff), "s"(r), "s"(soff)); }
DI void st8(v2u d, u32 voff, rsrc_t r, u32 soff) { asm volatile("buffer_store_dwordx2 %0, %1, %2, %3 offen sc1" ::"v"(d), "v"(voff), "s"(r), "s"(soff)); }
#define WAITV_R(n, r) asm volatile("s_waitcnt vmcnt(" #n ")" : "+v"(r)::"memory")
#define BARV(n) asm volatile("s_waitcnt vmcnt(" #n ") lgkmcnt(0)\n\ts_barrier" ::: "memory")
#define BARL() asm volatile("s_waitcnt lgkmcnt(0)\n\ts_barrier" ::: "memory")

#define STAMP(i)

DI void poll_finish(v2u got, u32 voff, rsrc_t r, u32 soff, u32 tlo, u32 thi) {
#pragma unroll 1
  for (int spins = 0; spins < (1 << 16); ++spins) {
    if (__all(got.x == tlo && got.y == thi)) break;
    __builtin_amdgcn_s_sleep(2);
    asm volatile("buffer_load_dwordx2 %0, %1, %2, %3 offen sc1\n\ts_waitcnt vmcnt(0)" : "=&v"(got) : "v"(voff), "s"(r), "s"(soff) : "memory");
  }
}
DI f32x2 cmul(f32x2 a, f32x2 b) {
  f32x2 t;
  asm("v_pk_mul_f32 %0, %1, %2 op_sel_hi:[1,0]\n\tv_pk_fma_f32 %0, %1, %2, %0 op_sel:[1,1,0] op_sel_hi:[0,1,1] neg_lo:[0,1,0]" : "=&v"(t) : "v"(a), "v"(b));
  return t;
}
DI f32x2 cpow(float a2, float br) {
  float m = __builtin_amdgcn_exp2f(a2), f = __builtin_amdgcn_fractf(br);
  float c = m * __builtin_amdgcn_cosf(f), s = m * __builtin_amdgcn_sinf(f);
  asm("" : "+v"(c)); asm("" : "+v"(s));
  return (f32x2){c, s};
}
#define DPP_ADD(v, ctl, rm) v += __builtin_bit_cast(float, __builtin_amdgcn_update_dpp(0, __builtin_bit_cast(int, v), ctl, rm, 0xf, true))
DI void wave_sum2(float& a, float& b) {
  DPP_ADD(a, 0xB1, 0xf); DPP_ADD(b, 0xB1, 0xf); DPP_ADD(a, 0x4E, 0xf); DPP_ADD(b, 0x4E, 0xf);
  DPP_ADD(a, 0x141, 0xf); DPP_ADD(b, 0x141, 0xf); DPP_ADD(a, 0x140, 0xf); DPP_ADD(b, 0x140, 0xf);
  DPP_ADD(a, 0x142, 0xf); DPP_ADD(b, 0x142, 0xf); DPP_ADD(a, 0x143, 0xf); DPP_ADD(b, 0x143, 0xf);
}
DI void rowstat(v4u xr, u32 lane, u32 tlo, u32 thi, rsrc_t rWS, u32 row) {
  float s = 0.f, q = 0.f;
#pragma unroll
  for (int j = 0; j < 4; ++j) { s = dot2(xr[j], 0x3f803f80u, s); q = dot2(xr[j], xr[j], q); }
  wave_sum2(s, q);
  const float nm = s * (-1.f / D), var = fmaxf(q * (1.f / D) - nm * nm, 0.f);
  const float den = rbf(__builtin_amdgcn_sqrtf(rbf(rbf(var) + 1e-5f))), rd = __builtin_amdgcn_rcpf(den);
  const float ir = den * rbf(__builtin_amdgcn_sqrtf(rbf(rbf(var * rd * rd) + 1e-5f)));
  const v4u g = {tlo, pk(nm, nm - rbf(nm)), thi, pk(ir, ir - rbf(ir))};
  st16(g, lane == 63 ? 0u : 0x7ffffff0u, rWS, WS_STAT + row * 16);
}
struct GeluC { float k0, k1, ir; };
DI float gelu1(float a, const GeluC& c) {
  const float p = __builtin_fmaf(a * a, c.k1, c.k0);
  const float e = __builtin_amdgcn_exp2f(a * p);
  return a * __builtin_amdgcn_rcpf(__builtin_fmaf(e, c.ir, c.ir));
}

__global__ __launch_bounds__(512, 2) void fused_kernel(Params P) {
  __shared__ __attribute__((aligned(1024))) char lds_all[LDS_BYTES];
  const u32 tid = threadIdx.x, lane = tid & 63;
  const u32 w = __builtin_amdgcn_readfirstlane(tid >> 6);
  const u32 b = blockIdx.x, xcd = b & 7, idx = b >> 3;
  asm volatile("" ::"v"((LAS char*)lds_all));
  u32 pfdummy = 0;
  if (w == 7) {
    u64 pc; u32 len;
    asm volatile("s_getpc_b64 %0\n.Lcode_here%=:\n\ts_mov_b32 %1, fused_code_end-.Lcode_here%=" : "=s"(pc), "=s"(len));
    u32 off = (idx * 8 + lane) * 128;
    if (lane < 8 && off < len) asm volatile("global_load_dword %0, %1, %2" : "=v"(pfdummy) : "v"(off), "s"(pc));
  }
  u32 vrow = lane < 56 ? lane * 16 : 0x7ffffff0u;
  u64 epoch;
#define TAG_READY() const u32 tlo = (u32)(epoch + 1), thi = (u32)((epoch + 1) >> 32); const v2u tagv = {tlo, thi}

  if (idx < 4) {
    const u32 h = xcd * 4 + idx;
    const rsrc_t rX = mk(P.x, L * D * 2), rWS = mk(P.ws, WS_END);
    const u32 srow = (h >> 1) * 128 + 112 + (h & 1) * 8 + w;
    v4u xs, xr[8];
    ld16p(xs, vrow, rX, srow * 896);
    asm volatile("s_load_dwordx2 %0, %1, 0x0" : "=s"(epoch) : "s"(P.ws));
#pragma unroll
    for (int i = 0; i < 8; ++i) ld16p(xr[i], vrow, rX, (h * 64 + w * 8 + i) * 896);
    asm volatile("s_waitcnt vmcnt(8) lgkmcnt(0)" : "+v"(xs), "+v"(pfdummy), "+s"(epoch)::"memory");
    TAG_READY();
    rowstat(xs, lane, tlo, thi, rWS, srow);
    STAMP(0);
    float sv = 0.f, qv = 0.f;
#pragma unroll
    for (int i = 0; i < 8; ++i) {
      if (i == 0) asm volatile("s_waitcnt vmcnt(5)" : "+v"(xr[0]), "+v"(xr[1]), "+v"(xr[2]), "+v"(xr[3])::"memory");
      if (i == 4) asm volatile("s_waitcnt vmcnt(1)" : "+v"(xr[4]), "+v"(xr[5]), "+v"(xr[6]), "+v"(xr[7])::"memory");
      float s = 0.f, q = 0.f;
#pragma unroll
      for (int j = 0; j < 4; ++j) { s = dot2(xr[i][j], 0x3f803f80u, s); q = dot2(xr[i][j], xr[i][j], q); }
      wave_sum2(s, q);
      sv = lane == 48 + i ? s : sv; qv = lane == 48 + i ? q : qv;
    }
    const float mean = sv * (1.f / D), var = fmaxf(qv * (1.f / D) - mean * mean, 0.f);
    const float rdv = __builtin_amdgcn_rcpf(rbf(__builtin_amdgcn_sqrtf(rbf(rbf(var) + 1e-5f)))), mrv = -rbf(mean) * rdv;
#pragma unroll
    for (int i = 0; i < 8; ++i) {
      const float rd = __builtin_bit_cast(float, __builtin_amdgcn_readlane(__builtin_bit_cast(int, rdv), 48 + i));
      const float mr = __builtin_bit_cast(float, __builtin_amdgcn_readlane(__builtin_bit_cast(int, mrv), 48 + i));
      f32x2 rd2 = {rd, rd}, mr2 = {mr, mr};
      PINV(rd2); PINV(mr2);
      v4u o;
#pragma unroll
      for (int j = 0; j < 4; ++j) o[j] = pk2(__builtin_elementwise_fma((f32x2){bf_lo(xr[i][j]), bf_hi(xr[i][j])}, rd2, mr2));
      const u32 tok = w * 8 + i;
      if (lane < 56) lw<v4u>(tok * 896 + ((lane ^ ((tok >> 2) & 7)) << 4), o);
    }
    BARL();
    STAMP(1);
    const u32 t4 = lane & 15;
    const u32 ta = t4 * 4 * 896 + ((w ^ (t4 & 7)) << 4) + (lane >> 4) * 4, tv = (w * 4 + (lane >> 4)) * 8192 + t4 * 16;
#pragma unroll
    for (int i = 0; i < 7; ++i) {
      const u32 a = ta + i * 128;
      v4u o = {lr<u32>(a), lr<u32>(a + 896), lr<u32>(a + 1792), lr<u32>(a + 2688)};
      st16(o, tv, rWS, WS_XT + h * 256 + i * 32 * 8192);
    }
    STAMP(2);
    BARV(0);
    if (w == 0) if (lane == 0) st8(tagv, 0, rWS, WS_FX + h * 8);
    STAMP(3);
    if (h == 0 && w < 5) {
      u32 vo2 = w < 4 ? WS_FH + (tid >> 4) * 128 + ((tid & 15) < 14 ? (tid & 15) : 0) * 8 : WS_FX + (lane & 31) * 8;
      v2u got = {tlo + 1, 0};
#pragma unroll 1
      for (int spins = 0; spins < (1 << 14); ++spins) {
        if (__all(got.x == tlo && got.y == thi)) break;
        __builtin_amdgcn_s_sleep(32);
        asm volatile("buffer_load_dwordx2 %0, %1, %2, 0 offen sc1\n\ts_waitcnt vmcnt(0)" : "=&v"(got) : "v"(vo2), "s"(rWS) : "memory");
      }
    }
    if (h == 0) { BARL(); if (w == 0) if (lane == 0) st8(tagv, 0, rWS, WS_EPOCH); }
  } else {
    const u32 m = idx - 4, mq = (m * 37) >> 8, mr = m - mq * 7;
    const u32 rt = (xcd >> 1) * 4 + mq, ct = (xcd & 1) * 7 + mr;
    const u32 rt2 = xcd * 4 + mq, ct2 = mr;
    const u32 cp = xcd * 28 + m;
    const u32 l8 = lane >> 3, h = lane >> 5, l31 = lane & 31;
    const u32 rowp = w * 8 + l8;
    const u32 wslot = ((lane & 7) ^ ((lane >> 2) & 4)) << 4;
    u32 vX0 = (rt * 128 + rowp) * 896 + (((lane & 7) ^ ((w & 1) * 4 + (lane >> 4))) << 4), vX1 = vX0 + 64 * 896;
    u32 vW1 = rowp * 3584 + wslot + ct * 256;
    u32 vH = lane * 16;
    u32 mw = w * 1024;
    u32 srow = rt * 128 + ct * 8 + w, srowb = srow * 896, spar = cp * 1024;
    u32 sc[7];
    { u32 c = mq + mr; c = c >= 7 ? c - 7 : c; sc[0] = c * 128; sc[1] = c == 6 ? 0 : sc[0] + 128; }
    u32 sw0 = sc[0] * 1792;
    PINV(vX0); PINV(vX1); PINV(vW1); PINV(vH); PINV(vrow); PINS(mw); PINS(srowb); PINS(spar); PINS(sc[0]); PINS(sc[1]); PINS(sw0);
    const rsrc_t rX = mk(P.x, L * D * 2), rW1 = mk(P.w1, D * F * 2);
    v4u xs;
    ld16p(xs, vrow, rX, srowb);
    if (w < 4) {
      const char* ap = w == 0 ? P.logA : w == 1 ? P.Aim : w == 2 ? P.Cr : P.Ci;
      dma<LD_PAR>(mw, vH, mk(ap, D * 256 * 2), spar);
    }
    dma<0>(mw, vX0, rX, sc[0]); dma<PAGE>(mw, vX1, rX, sc[0]); dma<2 * PAGE>(mw, vW1, rW1, sw0);
    dma<3 * PAGE>(mw, vX0, rX, sc[1]); dma<4 * PAGE>(mw, vX1, rX, sc[1]);
    asm volatile("s_load_dwordx2 %0, %1, 0x0" : "=s"(epoch) : "s"(P.ws));
    SCHED;
    const rsrc_t rW2 = mk(P.w2, D * F * 2), rWS = mk(P.ws, WS_END);
    const u32 ldt2 = *(const CAS u32*)(uintptr_t)(P.logdt + cp * 4), dsk2 = *(const CAS u32*)(uintptr_t)(P.Dskip + cp * 4);
#pragma unroll
    for (int i = 2; i < 7; ++i) { sc[i] = sc[i - 1] == 768 ? 0 : sc[i - 1] + 128; PINS(sc[i]); }
    u32 sp[14];
    { u32 c = 2 * mr + 4 * mq; c = c >= 14 ? c - 14 : c; sp[0] = c * 8192; PINS(sp[0]); }
#pragma unroll
    for (int i = 1; i < 14; ++i) { sp[i] = sp[i - 1] == 13 * 8192 ? 0 : sp[i - 1] + 8192; PINS(sp[i]); }
    u32 vW1b = vW1 + 128, vW2a = rowp * 896 + wslot + ct2 * 128, vW2b = vW2a + 64 * 896;
    PINV(vW1b); PINV(vW2a); PINV(vW2b);
#define PAGE_(p) do { constexpr u32 lo_ = ((p) % NSLOT) * PAGE;                                      \
      if constexpr ((p) < 21) { if constexpr ((p) % 3 == 0) dma<lo_>(mw, vX0, rX, sc[(p) / 3]);       \
        else if constexpr ((p) % 3 == 1) dma<lo_>(mw, vX1, rX, sc[(p) / 3]);                          \
        else dma<lo_>(mw, vW1, rW1, sc[(p) / 3] * 1792); }                                            \
      else if constexpr ((p) < 28) dma<lo_>(mw, vW1b, rW1, sc[((p) - 21) % 7] * 1792);                \
      else dma<lo_>(mw, (p) < 42 ? vW2a : vW2b, rW2, sp[(p) % 14] * 14); } while (0)
    const u32 ch = w >> 2, w4 = w & 3;
    u32 xsel[4], xmask[2], aX, aY;
    {
      const bool last = l31 == 31;
      u32 qx, qy, ncol = (l31 + 1) & 31;
      if (w4 == 0) {
        xsel[3] = h ? 0x01000100u : 0x01000302u; xsel[2] = h ? 0x03020504u : 0x05040706u;
        xsel[1] = h ? 0x05040504u : 0x01000302u; xsel[0] = h ? 0x07060100u : 0x05040706u;
        xmask[1] = h ? 0u : ~0u; xmask[0] = h ? 0xffffu : ~0u;
        qx = 2 * h; qy = h ? (last ? 3 : 2) : 1; ncol = h ? ncol : l31;
      } else if (w4 == 1) {
        const u32 a = 0x04040404u * h, n = (h != 0) != last ? 0x05040504u : 0x01000100u;
        xsel[3] = 0x01000100u + a; xsel[2] = 0x01000302u + a; xsel[1] = 0x03020302u + a; xsel[0] = n;
        xmask[1] = 0u; xmask[0] = ~0u;
        qx = 3; qy = (h && last) ? 4 : 3;
      } else {
        const u32 a = (w4 & 1) * 2 + h, an = a + (last ? 1 : 0);
        const u32 sa = 0x01000100u + 0x02020202u * a, sn = 0x01000100u + 0x02020202u * (an & 3);
        xsel[3] = sa; xsel[2] = sa; xsel[0] = sn;
        xmask[1] = (lane & 1) ? ~0u : 0xffffu; xmask[0] = ~0u;
        xsel[1] = (sn & xmask[1]) | (sa & ~xmask[1]);
        qx = 4; qy = 4 + (an >> 2);
      }
      aX = LD_SC + ch * 4 * 4352 + l31 * 128 + ((qx ^ (l31 & 7)) << 4);
      aY = LD_SC + ch * 4 * 4352 + ncol * 128 + ((qy ^ (ncol & 7)) << 4);
    }
    const u32 tt = w & 3, fq = w >> 2;
    u32 ax[2][4], aw[2][4];
#pragma unroll
    for (int j = 0; j < 4; ++j) {
      const u32 ks = j ^ tt;
      ax[0][j] = (tt >> 1) * PAGE + ((tt & 1) * 32 + l31) * 128 + (((2 * ks + h) ^ ((l31 >> 1) & 7)) << 4);
      aw[0][j] = (16 * ks + 8 * h + ((lane >> 2) & 3)) * 128 + ((fq * 64 + ((lane >> 4) & 1) * 32 + (lane & 3) * 8) ^ (((lane >> 3) & 1) << 6));
      ax[1][j] = ax[0][j] + 7 * PAGE; aw[1][j] = aw[0][j] + 7 * PAGE;
      PINV(ax[0][j]); PINV(ax[1][j]); PINV(aw[0][j]); PINV(aw[1][j]);
    }
    u32 sw = LD_SC + w * 4352 + lane * 4, sr = LD_SC + w * 4352 + (lane & 15) * 272 + (lane >> 4) * 16, apar = LD_PAR + tid * 2;
    PINV(sw); PINV(sr); PINV(apar);
    float dt = rbf(expf(ch ? bf_hi(ldt2) : bf_lo(ldt2)));
    PINV(dt);
    STAMP(0);
    asm volatile("s_waitcnt vmcnt(5) lgkmcnt(0)\n\ts_barrier" : "+v"(pfdummy), "+v"(xs), "+s"(epoch)::"memory");
    TAG_READY();
    STAMP(1);
    const float lA = bf_lo(lr<u16>(apar)), ai = bf_lo(lr<u16>(apar + 1024)), cr = bf_lo(lr<u16>(apar + 2048)), ci = bf_lo(lr<u16>(apar + 3072));
    rowstat(xs, lane, tlo, thi, rWS, srow);
    PAGE_(5);
    if (w == 4) { if (lane < 16) dma<LD_BIAS - 4096>(mw, vH, mk(P.b1, F * 2), ct * 256); }
    else if (w == 5) { if (lane < 8) dma<LD_BIAS + 256 - 5120>(mw, vH, mk(P.b2, D * 2), ct2 * 128); }
    const float a2 = -rbf(__builtin_amdgcn_exp2f(lA * 1.44269504f)) * dt * 1.44269504f, br = ai * dt * 0.15915494f;
    f32x2 tb[16];
    const f32x2 z8 = cpow(a2 * 8.f, br * 8.f), z16 = cmul(z8, z8);
    tb[0] = (f32x2){1.f, 0.f}; tb[1] = z8; tb[2] = z16;
#pragma unroll
    for (int j = 3; j < 16; ++j) tb[j] = cmul(tb[j - 2], z16);
    SCHED; PAGE_(6); SCHED;
    v4u xf[28], wf[4], lo[2][4], hi[4], ko;
    f32x16 acc;
#pragma unroll
    for (int i = 0; i < 16; ++i) acc[i] = 0.f;
    float cs = 0.f;
#define RD_X(i, j) xf[4 * (i) + (j)] = lr<v4u>(ax[((3 * (i)) % NSLOT) / 7][j] + (((3 * (i)) % NSLOT) % 7) * PAGE)
#define RD_W(p, j) trr2(aw[((p) % NSLOT) / 7][j] + (((p) % NSLOT) % 7) * PAGE, aw[((p) % NSLOT) / 7][j] + (((p) % NSLOT) % 7) * PAGE + 512)
#define CHUNK_A(i, NW)                                                                   \
    BARV(NW);                                                                            \
    _Pragma("unroll") for (int j = 0; j < 4; ++j) { wf[j] = RD_W(3 * (i) + 2, j); RD_X(i, j); } \
    PAGE_(3 * (i) + 7);                                                                  \
    _Pragma("unroll") for (int e = 0; e < 4; ++e) cs = dot2(wf[0][e], 0x3f803f80u, cs);  \
    SCHED;                                                                               \
    _Pragma("unroll") for (int j = 0; j < 4; ++j) acc = mfma32(wf[j], xf[4 * (i) + j], acc); \
    SCHED
#define TAB_OUT(n) _Pragma("unroll") for (int j = 0; j < n; ++j) lw<u32>(sw + j * 272, pk2(tb[j]))
#define TAB_IN(dst) _Pragma("unroll") for (int t = 0; t < 4; ++t) dst[t] = lr<v4u>(sr + t * 64)
    STAMP(2);
    CHUNK_A(0, 5);
    TAB_OUT(16); TAB_IN(lo[0]); SCHED; PAGE_(8);
    tb[0] = cmul(tb[14], z16); tb[1] = cmul(tb[15], z16);
#pragma unroll
    for (int j = 2; j < 16; ++j) tb[j] = cmul(tb[j - 2], z16);
    SCHED; PAGE_(9);
    CHUNK_A(1, 4);
    TAB_OUT(16); TAB_IN(lo[1]);
    const f32x2 z1 = cpow(a2, -br), z256 = cpow(a2 * 256.f, br * -256.f);
    { float c0 = 2.f * dt * cr, c1 = -2.f * dt * ci; asm("" : "+v"(c0)); asm("" : "+v"(c1)); tb[0] = (f32x2){c0, c1}; }
    SCHED; PAGE_(11);
#pragma unroll
    for (int j = 0; j < 7; ++j) { tb[j + 1] = cmul(tb[j], z1); if (!(j & 1)) tb[8 + j / 2] = cmul(tb[j], z256); }
    tb[12] = cmul(tb[8], z256); tb[13] = cmul(tb[10], z256); tb[14] = cmul(tb[12], z256); tb[15] = cmul(tb[13], z256);
    SCHED; PAGE_(12);
    STAMP(3);
    CHUNK_A(2, 4);
    TAB_OUT(16); TAB_IN(hi); SCHED; PAGE_(14);
    tb[0] = cmul(tb[14], z256);
#pragma unroll
    for (int j = 1; j < 5; ++j) tb[j] = cmul(tb[j - 1], z256);
    {
      f32x4 kd[2][2];
      v4u hi2[4];
      TAB_OUT(5); TAB_IN(hi2);
      SCHED;
#pragma unroll
      for (int t = 0; t < 4; ++t)
#pragma unroll
        for (int lt = 0; lt < 2; ++lt) kd[0][lt] = mfma16(hi[t], lo[lt][t], t ? kd[0][lt] : (f32x4){0.f, 0.f, 0.f, 0.f});
#pragma unroll
      for (int t = 0; t < 4; ++t)
#pragma unroll
        for (int lt = 0; lt < 2; ++lt) kd[1][lt] = mfma16(hi2[t], lo[lt][t], t ? kd[1][lt] : (f32x4){0.f, 0.f, 0.f, 0.f});
      const u32 pw = LD_SC + w * 4352 + (lane & 15) * 128;
#pragma unroll
      for (int ht = 0; ht < 2; ++ht)
#pragma unroll
        for (int lt = 0; lt < 2; ++lt) lw<f32x4>(pw + lt * 2048 + (((ht * 4 + (lane >> 4)) ^ (lane & 7)) << 4), kd[ht][lt]);
    }
    SCHED; PAGE_(15);
    STAMP(4);
    CHUNK_A(3, 4);
    {
      f32x4 X = lr<f32x4>(aX), Y = lr<f32x4>(aY);
#pragma unroll
      for (int s = 1; s < 4; ++s) { X += lr<f32x4>(aX + s * 4352); Y += lr<f32x4>(aY + s * 4352); }
      PAGE_(17);
      const u32 x01 = pk(X[0], X[1]), x23 = pk(X[2], X[3]), y01 = pk(Y[0], Y[1]), y23 = pk(Y[2], Y[3]);
      ko[3] = __builtin_amdgcn_perm(x23, x01, xsel[3]); ko[2] = __builtin_amdgcn_perm(x23, x01, xsel[2]);
#pragma unroll
      for (int mm = 0; mm < 2; ++mm) ko[mm] = (__builtin_amdgcn_perm(y23, y01, xsel[mm]) & xmask[mm]) | (__builtin_amdgcn_perm(x23, x01, xsel[mm]) & ~xmask[mm]);
    }
    SCHED; PAGE_(18);
    CHUNK_A(4, 4);
    {
      const u32 ka = LD_K + ch * KCH + (255 - (tid & 255)) * 16;
      const u32 e1 = __builtin_amdgcn_alignbit(ko[1], ko[0], 16), e2 = __builtin_amdgcn_alignbit(ko[2], ko[1], 16), e3 = __builtin_amdgcn_alignbit(ko[3], ko[2], 16);
      lw<v4u>(ka, ko);
      lw<u16>(ka + 4160 + 2, (u16)ko[0]); lw<u32>(ka + 4160 + 4, e1); lw<v2u>(ka + 4160 + 8, (v2u){e2, e3}); lw<u16>(ka + 4160 + 16, (u16)(ko[3] >> 16));
      lw<u32>(ka + 8320 + 4, ko[0]); lw<v2u>(ka + 8320 + 8, (v2u){ko[1], ko[2]}); lw<u32>(ka + 8320 + 16, ko[3]);
      lw<u16>(ka + 12480 + 6, (u16)ko[0]); lw<v2u>(ka + 12480 + 8, (v2u){e1, e2}); lw<u32>(ka + 12480 + 16, e3); lw<u16>(ka + 12480 + 20, (u16)(ko[3] >> 16));
      if (w4 == 0) lw<u16>(LD_K + ch * KCH + (lane >> 4) * 4162 + 4096 + (lane & 15) * 2, (u16)0);
      PAGE_(20);
    }
    SCHED; PAGE_(21);
    STAMP(5);
    CHUNK_A(5, 4);
    v4u gran; u32 vgr = l31 * 16; const u32 sgr = WS_STAT + (rt * 128 + tt * 32) * 16;
    ld16(gran, vgr, rWS, sgr);
    PAGE_(23); PAGE_(24);
    CHUNK_A(6, 5);
    PAGE_(26); PAGE_(27);
    const u32 acs = LD_CS + fq * 256 + lane * 4;
    lw<float>(acs + tt * 512, cs);
    STAMP(6);
    asm volatile("s_waitcnt vmcnt(5) lgkmcnt(0)\n\ts_barrier" : "+v"(gran)::"memory");
#pragma unroll 1
    for (int spins = 0; spins < (1 << 16); ++spins) {
      if (__all(gran[0] == tlo && gran[2] == thi)) break;
      __builtin_amdgcn_s_sleep(2);
      asm volatile("buffer_load_dwordx4 %0, %1, %2, %3 offen sc1\n\ts_waitcnt vmcnt(0)" : "=&v"(gran) : "v"(vgr), "s"(rWS), "s"(sgr) : "memory");
    }
    STAMP(7);
    v4u wn[4];
#pragma unroll
    for (int j = 0; j < 4; ++j) wf[j] = RD_W(21, j);
    auto make_aext = [&](int st) {
      float c = lr<float>(acs + st * 2048);
#pragma unroll
      for (int s = 1; s < 4; ++s) c += lr<float>(acs + st * 2048 + s * 512);
      const u32 b1v = h ? 0u : (u32)lr<u16>(LD_BIAS + st * 128 + fq * 64 + l31 * 2);
      const u32 chh = pk(c, c); const float cl = c - bf_lo(chh);
      return (v4u){chh, pk(cl, cl), b1v * 0x10001u, 0u};
    };
    const v4u bext = {gran[1], gran[1], gran[3], 0u};
    GeluC gc;
    { gc.ir = bf_lo(gran[3]) + bf_hi(gran[3]); const float rs = __builtin_amdgcn_rcpf(gc.ir); gc.k0 = -2.3022082f * rs; gc.k1 = -0.10294324f * rs * rs * rs; }
    const u32 sHst = WS_H + ((rt * 4 + tt) * 112 + ct * 8 + fq * 2) * 1024;
    v2u pollx; const u32 vpx = l31 * 8;
    PAGE_(28);
    acc = mfma32(make_aext(0), bext, acc);
    f32x16 accb;
#pragma unroll
    for (int i = 0; i < 16; ++i) accb[i] = 0.f;
    cs = 0.f;
    float gl[8];
    v4u ureg;
#define STEP_B(i, cur, nxt)                                                              \
    SCHED;                                                                               \
    if (i < 6) { _Pragma("unroll") for (int j = 0; j < 4; ++j) nxt[j] = RD_W(22 + (i), j); } \
    _Pragma("unroll") for (int e = 0; e < 4; ++e) cs = dot2(cur[0][e], 0x3f803f80u, cs); \
    SCHED;                                                                               \
    _Pragma("unroll") for (int j = 0; j < 4; ++j) accb = mfma32(cur[j], xf[4 * (i) + j], accb); \
    SCHED
#define GELU_A(j0, n) _Pragma("unroll") for (int j = j0; j < j0 + n; ++j) gl[j & 7] = gelu1(acc[j], gc)
#define H_OUT(off) st16((v4u){pkq(gl[0], gl[1]), pkq(gl[2], gl[3]), pkq(gl[4], gl[5]), pkq(gl[6], gl[7])}, vH, rWS, sHst + (off))
    STEP_B(0, wf, wn); GELU_A(0, 3); PAGE_(29);
    BARV(2);
    STEP_B(1, wn, wf); GELU_A(3, 3); PAGE_(30);
    STEP_B(2, wf, wn); GELU_A(6, 2); H_OUT(0); PAGE_(31);
    STEP_B(3, wn, wf); GELU_A(8, 3); PAGE_(32); ld8(pollx, vpx, rWS, WS_FX);
    STEP_B(4, wf, wn); GELU_A(11, 3); PAGE_(33);
    STEP_B(5, wn, wf); GELU_A(14, 2); H_OUT(1024); PAGE_(34);
    STEP_B(6, wf, wn);
    lw<float>(acs + 2048 + tt * 512, cs);
    STAMP(8);
    asm volatile("s_waitcnt vmcnt(3) lgkmcnt(0)\n\ts_barrier" : "+v"(pollx)::"memory");
    poll_finish(pollx, vpx, rWS, WS_FX, tlo, thi);
    ld16(ureg, tid * 16, rWS, WS_XT + cp * 8192);
    PAGE_(35); PAGE_(36);
    STAMP(9);
    {
      accb = mfma32(make_aext(1), bext, accb);
#pragma unroll
      for (int s = 0; s < 2; ++s) {
#pragma unroll
        for (int j = 0; j < 8; ++j) gl[j] = gelu1(accb[8 * s + j], gc);
        if (s == 0) { PAGE_(37); PAGE_(38); }
        st16((v4u){pk(gl[0], gl[1]), pk(gl[2], gl[3]), pk(gl[4], gl[5]), pk(gl[6], gl[7])}, vH, rWS, sHst + 4096 + s * 1024);
        SCHED;
      }
    }
    STAMP(10);
    WAITV_R(6, ureg);
    lw<v2u>(LD_U + 512 + tid * 8, (v2u){__builtin_amdgcn_perm(ureg[1], ureg[0], 0x05040100u), __builtin_amdgcn_perm(ureg[3], ureg[2], 0x05040100u)});
    lw<v2u>(LD_U + 4608 + 512 + tid * 8, (v2u){__builtin_amdgcn_perm(ureg[1], ureg[0], 0x07060302u), __builtin_amdgcn_perm(ureg[3], ureg[2], 0x07060302u)});
    lw<u16>(LD_U + ch * 4608 + (tid & 255) * 2, (u16)0);
    BARL();
    if (w == 0) if (lane == 0) st8(tagv, 0, rWS, WS_FH + rt * 128 + ct * 8);
    PAGE_(39); PAGE_(40); PAGE_(41);
    STAMP(11);
    const u32 t2 = w >> 2, kq = w & 3;
    u32 vHa = vH + WS_H + ((rt2 * 2 + t2) * 112 + kq) * 1024, vHb = vHa + 4096;
    PINV(vHa); PINV(vHb);
    v4u hf[28];
#define HLOAD(j) ld16(hf[j], (j) < 14 ? vHa : vHb, rWS, sp[(j) % 14])
    v2u pollh; const u32 vph = (lane < 14 ? lane : 0) * 8, sph = WS_FH + (rt2 >> 1) * 128;
    f32x4 ya[8];
    const u32 r16 = lane & 15, g4 = lane >> 4;
    u32 kb = LD_K + ch * KCH + ((r16 + 1) & 3) * 4162 + (2031 - r16 + 8 * g4) * 2 - 7 * 512 - 64 - w4 * 128;
    u32 ub = LD_U + ch * 4608 + 512 - 32 - 64 + r16 * 32 + g4 * 16 - w4 * 128;
    PINV(kb); PINV(ub);
    {
      v4u B[2][8], A[2];
      auto rdA = [&](int n) { const u32 a = kb + (1 - n / 8) * 64 + (7 - n % 8) * 512; const v2u x = lrv<v2u>(a), y = lrv<v2u>(a + 8); return (v4u){x.x, x.y, y.x, y.y}; };
      A[0] = rdA(0);
#pragma unroll
      for (int T = 0; T < 8; ++T) B[0][T] = lr<v4u>(ub + 64 + T * 512);
#pragma unroll
      for (int n = 0; n < 16; ++n) {
        const int it = n / 8, a = n % 8;
        SCHED;
        if (n == 8) {
          asm volatile("s_waitcnt vmcnt(0) lgkmcnt(0)\n\ts_barrier" : "+v"(pollh)::"memory");
          if (w == 0) if (lane == 0) st8(tagv, 0, rWS, WS_FH + 2048 + rt * 128 + ct * 8);
          poll_finish(pollh, vph, rWS, sph, tlo, thi);
          STAMP(12);
        }
        if (n < 15) A[(n + 1) & 1] = rdA(n + 1);
        if (n < 8) B[1][7 - n] = lr<v4u>(ub + (7 - n) * 512);
        if (n == 3) ld8(pollh, vph, rWS, sph);
        if (n >= 8 && n < 13) { HLOAD(2 * (n - 8)); }
        SCHED;
#pragma unroll
        for (int T = 7; T >= a; --T) ya[T] = mfma16(A[n & 1], B[it][T - a], n ? ya[T] : (f32x4){0.f, 0.f, 0.f, 0.f});
        if (n >= 8 && n < 13) { SCHED; HLOAD(2 * (n - 8) + 1); }
      }
    }
    STAMP(13);
    const v4u xn = lr<v4u>(LD_U + ch * 4608 + 512 + (tid & 255) * 16);
    u32 yp[16];
#pragma unroll
    for (int T = 0; T < 8; ++T) { yp[2 * T] = pk(ya[T][0], ya[T][1]); yp[2 * T + 1] = pk(ya[T][2], ya[T][3]); }
    HLOAD(10); HLOAD(11);
    BARL();
    {
      const u32 ra = LD_RED + w * 4096 + r16 * 32 + g4 * 8;
#pragma unroll
      for (int T = 0; T < 8; ++T) lw<v2u>(ra + T * 512, (v2u){yp[2 * T], yp[2 * T + 1]});
    }
    HLOAD(12); HLOAD(13);
    f32x16 g0, g1;
#pragma unroll
    for (int i = 0; i < 16; ++i) { g0[i] = 0.f; g1[i] = 0.f; }
    u32 a2w[2][2];
    a2w[0][0] = (16 * kq + 4 * h + ((lane >> 2) & 3)) * 128 + ((((lane >> 4) & 1) * 32 + (lane & 3) * 8) ^ (((lane >> 3) & 1) << 6));
    a2w[0][1] = a2w[0][0] ^ 64; a2w[1][0] = a2w[0][0] + 7 * PAGE; a2w[1][1] = a2w[0][1] + 7 * PAGE;
    PINV(a2w[0][0]); PINV(a2w[0][1]); PINV(a2w[1][0]); PINV(a2w[1][1]);
    v4u wa[2][2], wb[2][2];
#define RD_W2(j, d) trr2(a2w[((j) % NSLOT) / 7][d] + (((j) % NSLOT) % 7) * PAGE, a2w[((j) % NSLOT) / 7][d] + (((j) % NSLOT) % 7) * PAGE + 1024)
#define RD_G(dst, j) dst[0][0] = RD_W2(j, 0); dst[0][1] = RD_W2(j, 1); dst[1][0] = RD_W2((j) + 1, 0); dst[1][1] = RD_W2((j) + 1, 1)
#define MM_G(src, j) SCHED; g0 = mfma32(src[0][0], hf[j], g0); g1 = mfma32(src[0][1], hf[j], g1); g0 = mfma32(src[1][0], hf[(j) + 1], g0); g1 = mfma32(src[1][1], hf[(j) + 1], g1); SCHED
#define HWAIT(n, j) asm volatile("s_waitcnt vmcnt(" #n ")" : "+v"(hf[j]), "+v"(hf[(j) + 1]))
    RD_G(wa, 0);
    BARL();
    STAMP(14);
    v4u yq[4];
#pragma unroll
    for (int s = 0; s < 4; ++s) yq[s] = lr<v4u>(LD_RED + (ch * 4 + s) * 4096 + (tid & 255) * 16);
    float yo[8];
    const float dsk = ch ? bf_hi(dsk2) : bf_lo(dsk2);
    RD_G(wb, 2); HWAIT(12, 0); MM_G(wa, 0);
#pragma unroll
    for (int i = 0; i < 4; ++i) { yo[2 * i] = bf_lo(yq[0][i]) + bf_lo(yq[1][i]); yo[2 * i + 1] = bf_hi(yq[0][i]) + bf_hi(yq[1][i]); }
    ld8(pollh, vph, rWS, sph + 2048);
    RD_G(wa, 4); HWAIT(11, 2); MM_G(wb, 2);
#pragma unroll
    for (int i = 0; i < 4; ++i) { yo[2 * i] += bf_lo(yq[2][i]); yo[2 * i + 1] += bf_hi(yq[2][i]); yo[2 * i] += bf_lo(yq[3][i]); }
    RD_G(wb, 6); HWAIT(9, 4); MM_G(wa, 4);
#pragma unroll
    for (int i = 0; i < 4; ++i) { yo[2 * i + 1] += bf_hi(yq[3][i]); yo[2 * i] = __builtin_fmaf(bf_lo(xn[i]), dsk, yo[2 * i]); yo[2 * i + 1] = __builtin_fmaf(bf_hi(xn[i]), dsk, yo[2 * i + 1]); }
    st16((v4u){pkq(yo[0], yo[1]), pkq(yo[2], yo[3]), pkq(yo[4], yo[5]), pkq(yo[6], yo[7])}, vH, rWS, WS_ST + cp * 8192 + mw);
    asm volatile("s_waitcnt vmcnt(1) lgkmcnt(0)\n\ts_barrier" : "+v"(hf[6]), "+v"(hf[7]), "+v"(hf[8]), "+v"(hf[9]), "+v"(hf[10]), "+v"(hf[11]), "+v"(hf[12]), "+v"(hf[13]), "+v"(pollh)::"memory");
    PAGE_(42); PAGE_(43);
    poll_finish(pollh, vph, rWS, sph + 2048, tlo, thi);
    STAMP(15);
    RD_G(wa, 8); HLOAD(14); HLOAD(15); MM_G(wb, 6); PAGE_(44); PAGE_(45);
    RD_G(wb, 10); HLOAD(16); HLOAD(17); MM_G(wa, 8); PAGE_(46); PAGE_(47);
    RD_G(wa, 12); HLOAD(18); HLOAD(19); MM_G(wb, 10); PAGE_(48); PAGE_(49);
    BARV(8);
    if (w == 0) if (lane == 0) st8(tagv, 0, rWS, WS_FS + cp * 8);
    STAMP(16);
    RD_G(wb, 14); HLOAD(20); HLOAD(21); MM_G(wa, 12); PAGE_(50); PAGE_(51);
    RD_G(wa, 16); HLOAD(22); HLOAD(23); HWAIT(63, 14); MM_G(wb, 14); PAGE_(52); PAGE_(53);
    BARV(8);
    RD_G(wb, 18); HLOAD(24); HLOAD(25); HWAIT(63, 16); MM_G(wa, 16); PAGE_(54); PAGE_(55);
    RD_G(wa, 20); HLOAD(26); HLOAD(27); HWAIT(63, 18); MM_G(wb, 18);
    v2u polls; ld8(polls, vpx, rWS, WS_FS + ct2 * 256);
    v4u xin; const u32 vout = (tid >> 3) * 896 + (tid & 7) * 16, sout = rt2 * 64 * 896 + ct2 * 128;
    ld16p(xin, vout, rX, sout);
    BARV(8);
    STAMP(17);
    RD_G(wb, 22); HWAIT(63, 20); MM_G(wa, 20);
    RD_G(wa, 24); HWAIT(63, 22); MM_G(wb, 22);
    asm volatile("s_waitcnt vmcnt(0) lgkmcnt(0)\n\ts_barrier" : "+v"(hf[24]), "+v"(hf[25]), "+v"(hf[26]), "+v"(hf[27]), "+v"(polls), "+v"(xin)::"memory");
    poll_finish(polls, vpx, rWS, WS_FS + ct2 * 256, tlo, thi);
    v4u sreg; ld16(sreg, (tid >> 3) * 4096 + (tid & 7) * 16, rWS, WS_ST + ct2 * 64 * 4096 + rt2 * 128);
    STAMP(18);
    RD_G(wb, 26); MM_G(wa, 24); MM_G(wb, 26);
    {
      const u32 pa = kq * 16384 + (t2 * 32 + l31) * 256;
#pragma unroll
      for (int qd = 0; qd < 4; ++qd) lw<f32x4>(pa + (((2 * qd + h) ^ (lane & 7)) << 4), (f32x4){g0[4 * qd], g0[4 * qd + 1], g0[4 * qd + 2], g0[4 * qd + 3]});
#pragma unroll
      for (int qd = 0; qd < 4; ++qd) lw<f32x4>(pa + 128 + (((2 * qd + h) ^ (lane & 7)) << 4), (f32x4){g1[4 * qd], g1[4 * qd + 1], g1[4 * qd + 2], g1[4 * qd + 3]});
      WAITV_R(0, sreg);
      lw<v4u>(LD_RED + (tid >> 3) * 128 + (((tid & 7) ^ w) << 4), sreg);
    }
    STAMP(19);
    BARL();
    {
      const u32 tok = tid >> 3, d8 = tid & 7;
      const u32 ra = tok * 256 + (((2 * d8) ^ (tok & 7)) << 4);
      f32x4 o0 = lr<f32x4>(ra), o1 = lr<f32x4>(ra ^ 16);
#pragma unroll
      for (int s = 1; s < 4; ++s) { o0 += lr<f32x4>(ra + s * 16384); o1 += lr<f32x4>((ra ^ 16) + s * 16384); }
      const v4u b2v = lr<v4u>(LD_BIAS + 256 + d8 * 16);
      const u32 sa = LD_RED + d8 * 1024 + ((w ^ d8) << 4) + (tok & 7) * 2;
      float o[8] = {o0[0], o0[1], o0[2], o0[3], o1[0], o1[1], o1[2], o1[3]};
      v4u res;
#pragma unroll
      for (int i = 0; i < 4; ++i) {
        const float v0 = o[2 * i] + bf_lo(b2v[i]), v1 = o[2 * i + 1] + bf_hi(b2v[i]);
        const float s0 = bf_lo(lr<u16>(sa + (2 * i) * 128)) * v0, s1 = bf_lo(lr<u16>(sa + (2 * i + 1) * 128)) * v1;
        const float r0 = __builtin_amdgcn_rcpf(1.f + __builtin_amdgcn_exp2f(v0 * -1.44269504f)), r1 = __builtin_amdgcn_rcpf(1.f + __builtin_amdgcn_exp2f(v1 * -1.44269504f));
        res[i] = pk(__builtin_fmaf(s0, r0, bf_lo(xin[i])), __builtin_fmaf(s1, r1, bf_hi(xin[i])));
      }
      st16(res, vout, mk(P.out, L * D * 2), sout);
    }
    STAMP(20);
  }
  asm volatile("fused_code_end:");
}

extern "C" void kernel_launch(void* const* d_in, const int* in_sizes, int n_in, void* d_out, int out_size, void* d_ws, size_t ws_size, hipStream_t stream) {
  Params P;
  P.x = (const char*)d_in[0]; P.logA = (const char*)d_in[1]; P.Aim = (const char*)d_in[2]; P.Cr = (const char*)d_in[3]; P.Ci = (const char*)d_in[4];
  P.logdt = (const char*)d_in[5]; P.Dskip = (const char*)d_in[6]; P.w1 = (const char*)d_in[8]; P.b1 = (const char*)d_in[9];
  P.w2 = (const char*)d_in[10]; P.b2 = (const char*)d_in[11]; P.out = (char*)d_out; P.ws = (char*)d_ws;
  fused_kernel<<<256, 512, 0, stream>>>(P);
}
```

```cpp
#include <hip/hip_runtime.h>

struct Params {
  const char *x, *w1, *b1, *w2;
  char *ws, *out;
  const char *b2, *logA, *Aim, *Cr, *Ci, *logdt, *Dskip;
};
constexpr unsigned WS_DBG = 196608;

__global__ __launch_bounds__(512, 2) void fused_kernel(Params) {
#if defined(__HIP_DEVICE_COMPILE__)
  __shared__ __attribute__((aligned(1024))) char lds_all[163840];
  asm volatile("" ::"s"((__attribute__((address_space(3))) char*)lds_all));
  asm volatile(
    "s_load_dword s98, %0, 0x0\n"
    "s_load_dword s99, %0, 0x40\n"
    "s_getpc_b64 s[6:7]\n"
    ".Lpc_%=:\n"
    "v_readfirstlane_b32 s4, %2\n"
    "v_and_b32 v254, 63, %2\n"
    "s_mov_b32 s65, 0x10000\n"
    "s_lshr_b32 s4, s4, 6\n"
    "s_cmp_lt_u32 %1, 32\n"
    "s_cbranch_scc1 .LH_%=\n"
    "v_mov_b32 v4, 147968\n"
    "v_lshl_add_u32 v4, %2, 3, v4\n"
    "v_cmp_gt_u32 vcc, 416, %2\n"
    "v_min_u32 v4, 151304, v4\n"
    "s_cmp_lt_u32 s4, 4\n"
    "v_cndmask_b32 v6, 0, -1, vcc\n"
    "v_cndmask_b32 v7, 0, -1, vcc\n"
    "ds_write_b64 v4, v[6:7]\n"
    "s_cbranch_scc1 .LA_%=\n"
    "s_load_dwordx8 s[36:43], %0, 0x38 ; ROLE_START\n"
    "s_load_dwordx4 s[44:47], %0, 0x58\n"
    "s_load_dwordx2 s[12:13], %0, 0x0\n"
    "s_load_dwordx2 s[28:29], %0, 0x20\n"
    "s_load_dwordx2 s[20:21], %0, 0x28\n"
    "s_load_dwordx2 s[24:25], %0, 0x30\n"
    "s_load_dwordx2 s[16:17], %0, 0x18\n"
    "s_cmp_lg_u32 s4, 7\n"
    "s_cbranch_scc1 .La1_%=\n"
    "s_and_b32 s76, %1, 0xf8\n"
    "v_add_lshl_u32 v1, s76, v254, 7\n"
    "v_min_u32 v1, .Lend_%=-.Lpc_%=-4, v1\n"
    "s_mov_b64 exec, 0xff\n"
    "global_load_dword v2, v1, s[6:7] ; COND\n"
    "s_mov_b64 exec, -1\n"
    ".La1_%=:\n"
    "s_and_b32 s71, %1, 7\n"
    "s_lshr_b32 s72, %1, 3\n"
    "s_sub_u32 s72, s72, 4\n"
    "s_mul_i32 s73, s72, 37\n"
    "s_lshr_b32 s73, s73, 8\n"
    "s_mul_i32 s74, s73, 7\n"
    "s_sub_u32 s74, s72, s74\n"
    "s_lshl2_add_u32 s79, s71, s73\n"
    "s_mul_i32 s75, s71, 28\n"
    "s_add_u32 s75, s75, s72\n"
    "s_lshl_b32 s76, s75, 10\n"
    "v_and_b32 v3, 0xff, %2\n"
    "v_lshl_add_u32 v4, v3, 2, s76\n"
    "s_lshl_b32 s77, s75, 2\n"
    "s_waitcnt lgkmcnt(0)\n"
    "global_load_dword v8, v4, s[36:37]\n"
    "global_load_dword v9, v4, s[38:39]\n"
    "global_load_dword v10, v4, s[40:41]\n"
    "global_load_dword v11, v4, s[42:43]\n"
    "s_load_dword s48, s[44:45], s77\n"
    "s_load_dword s49, s[46:47], s77\n"
    "s_load_dwordx2 s[32:33], s[28:29], 0x0\n"
    "s_barrier\n"
    "s_branch .LB2_%=\n"
    ".LA_%=:\n"
    "s_setprio 2 ; ROLE_START\n"
    "s_load_dwordx2 s[12:13], %0, 0x0\n"
    "s_load_dwordx2 s[16:17], %0, 0x8\n"
    "s_load_dwordx2 s[28:29], %0, 0x20\n"
    "s_load_dwordx2 s[20:21], %0, 0x10\n"
    "s_load_dwordx2 s[24:25], %0, 0x18\n"
    "s_and_b32 s71, %1, 7\n"
    "s_lshr_b32 s72, %1, 3\n"
    "s_sub_u32 s72, s72, 4\n"
    "s_mul_i32 s73, s72, 37\n"
    "s_lshr_b32 s73, s73, 8\n"
    "s_mul_i32 s74, s73, 7\n"
    "s_sub_u32 s74, s72, s74\n"
    "s_lshl2_add_u32 s79, s71, s73\n"
    "s_lshr_b32 s75, s71, 1\n"
    "s_lshl2_add_u32 s75, s75, s73\n"
    "s_and_b32 s78, s71, 1\n"
    "s_mul_i32 s78, s78, 7\n"
    "s_add_u32 s78, s78, s74\n"
    "s_lshl2_add_u32 s72, s75, s4\n"
    "s_mul_i32 s76, s72, 28672\n"
    "v_lshrrev_b32 v32, 3, v254\n"
    "v_lshrrev_b32 v33, 4, v254\n"
    "v_and_b32 v34, 7, v254\n"
    "v_xor_b32 v34, v34, v33\n"
    "v_mul_u32_u24 v32, 896, v32\n"
    "v_lshl_add_u32 v32, v34, 4, v32\n"
    "v_add_u32 v4, s76, v32\n"
    "v_xor_b32 v5, 64, v4\n"
    "v_add_u32 v6, 12288, v4\n"
    "v_add_u32 v7, 18432, v5\n"
    "v_add_u32 v5, 6144, v5\n"
    "v_lshrrev_b32 v38, 2, v254\n"
    "v_and_b32 v39, 3, v254\n"
    "s_lshl_b32 s76, s4, 4\n"
    "v_add_u32 v40, s76, v38\n"
    "v_mul_u32_u24 v41, 3584, v40\n"
    "v_lshl_add_u32 v41, v39, 4, v41\n"
    "s_lshl_b32 s76, s78, 8\n"
    "s_add_u32 s76, s76, 192\n"
    "v_add_u32 v11, s76, v41\n"
    "v_add_u32 v8, 2880, v11\n"
    "v_add_u32 v9, 1920, v11\n"
    "v_add_u32 v10, 960, v11\n"
    "v_mov_b32 v20, 0x7ffffff0\n"
    "s_lshl_b32 s62, s4, 12\n"
    "s_add_u32 s63, s62, 114688\n"
    "s_mul_i32 s64, s4, 320\n"
    "s_add_u32 s64, s64, 150016\n"
    "s_mov_b32 s34, 0\n"
    "s_movk_i32 s5, 128\n"
    "s_mov_b32 s14, 1835008\n"
    "s_mov_b32 s15, 0x20000\n"
    "s_mov_b32 s18, 1608704\n"
    "s_mov_b32 s19, 0x20000\n"
    "s_add_u32 s35, s34, s5\n"
    "s_sub_u32 s76, s35, 896\n"
    "s_min_u32 s35, s35, s76\n"
    "s_waitcnt lgkmcnt(0)\n"
    "s_sub_u32 s16, s16, 3072\n"
    "s_subb_u32 s17, s17, 0\n"
    "s_add_u32 m0, s62, 0\n"
    "s_mul_i32 s41, s34, 1792\n"
    "buffer_load_dwordx4 v8, s[16:19], s41 offen offset:0 lds\n"
    "buffer_load_dwordx4 v9, s[16:19], s41 offen offset:1024 lds\n"
    "buffer_load_dwordx4 v10, s[16:19], s41 offen offset:2048 lds\n"
    "buffer_load_dwordx4 v11, s[16:19], s41 offen offset:3072 lds\n"
    "s_add_u32 m0, s63, 0\n"
    "s_nop 0\n"
    "buffer_load_dwordx4 v4, s[12:15], s34 offen offset:0 lds\n"
    "buffer_load_dwordx4 v5, s[12:15], s34 offen offset:1024 lds\n"
    "buffer_load_dwordx4 v6, s[12:15], s34 offen offset:2048 lds\n"
    "buffer_load_dwordx4 v7, s[12:15], s34 offen offset:3072 lds\n"
    "s_barrier\n"
    "s_add_u32 m0, s64, 60\n"
    "s_nop 0\n"
    "buffer_load_dword v20, s[12:15], 0 offen lds\n"
    "s_load_dwordx2 s[32:33], s[28:29], 0x0\n"
    "s_add_u32 m0, s62, 16384\n"
    "s_mul_i32 s41, s35, 1792\n"
    "buffer_load_dwordx4 v8, s[16:19], s41 offen offset:0 lds\n"
    "buffer_load_dwordx4 v9, s[16:19], s41 offen offset:1024 lds\n"
    "v_and_b32 v35, 31, v254\n"
    "v_lshrrev_b32 v36, 5, v254\n"
    "v_bfe_u32 v32, v254, 1, 3\n"
    "v_xor_b32 v32, v32, v36\n"
    "v_lshl_add_u32 v34, v35, 7, s63\n"
    "v_and_b32 v37, 3, v38\n"
    "v_lshlrev_b32 v37, 6, v37\n"
    "v_lshl_add_u32 v37, v39, 3, v37\n"
    "v_and_b32 v41, 1, v33\n"
    "v_lshl_add_u32 v37, v41, 5, v37\n"
    "v_lshl_add_u32 v43, v36, 9, v37\n"
    "s_lshl_b32 s76, s4, 1\n"
    "v_xor_b32 v12, s76, v32\n"
    "v_lshl_add_u32 v12, v12, 4, v34\n"
    "v_add_u32 v16, s62, v43\n"
    "v_xor_b32 v13, 32, v12\n"
    "v_xor_b32 v17, 4096, v16\n"
    "v_xor_b32 v14, 64, v12\n"
    "v_xor_b32 v18, 8192, v16\n"
    "v_xor_b32 v15, 96, v12\n"
    "v_xor_b32 v19, 12288, v16\n"
    "v_mul_u32_u24 v21, 320, v39\n"
    "v_add_u32 v21, 150016, v21\n"
    "v_and_b32 v43, 15, v254\n"
    "v_lshlrev_b32 v41, 2, v41\n"
    "v_sub_u32 v43, v43, v41\n"
    "v_mov_b32 v34, 0x3f803f80\n"
    "v_cmp_eq_u32 vcc, 0, v43\n"
    "v_cmp_eq_u32 s[76:77], 8, v43\n"
    "s_mov_b32 s30, 12058624\n"
    "s_mov_b32 s31, 0x20000\n"
    "v_cndmask_b32 v240, 0, v34, vcc\n"
    "v_cndmask_b32 v244, 0, v34, s[76:77]\n"
    "v_cndmask_b32 v241, 0, v34, vcc\n"
    "v_cndmask_b32 v245, 0, v34, s[76:77]\n"
    "v_cndmask_b32 v242, 0, v34, vcc\n"
    "v_cndmask_b32 v246, 0, v34, s[76:77]\n"
    "v_cndmask_b32 v243, 0, v34, vcc\n"
    "v_cndmask_b32 v247, 0, v34, s[76:77]\n"
    "s_add_u32 s36, s35, s5\n"
    "s_sub_u32 s76, s36, 896\n"
    "s_min_u32 s36, s36, s76\n"
    "s_add_u32 s37, s36, s5\n"
    "s_sub_u32 s76, s37, 896\n"
    "s_min_u32 s37, s37, s76\n"
    "buffer_load_dwordx4 v10, s[16:19], s41 offen offset:2048 lds\n"
    "buffer_load_dwordx4 v11, s[16:19], s41 offen offset:3072 lds\n"
    "s_add_u32 m0, s64, 56\n"
    "s_nop 0\n"
    "buffer_load_dword v20, s[12:15], 0 offen lds\n"
    "s_add_u32 m0, s63, 16384\n"
    "s_nop 0\n"
    "buffer_load_dwordx4 v4, s[12:15], s35 offen offset:0 lds\n"
    "buffer_load_dwordx4 v5, s[12:15], s35 offen offset:1024 lds\n"
    "buffer_load_dwordx4 v6, s[12:15], s35 offen offset:2048 lds\n"
    "buffer_load_dwordx4 v7, s[12:15], s35 offen offset:3072 lds\n"
    "s_add_u32 s38, s37, s5\n"
    "s_sub_u32 s76, s38, 896\n"
    "s_min_u32 s38, s38, s76\n"
    "s_add_u32 s39, s38, s5\n"
    "s_sub_u32 s76, s39, 896\n"
    "s_min_u32 s39, s39, s76\n"
    "s_add_u32 s40, s39, s5\n"
    "s_sub_u32 s76, s40, 896\n"
    "s_min_u32 s40, s40, s76\n"
    "s_lshl_b32 s69, s72, 9\n"
    "s_add_u32 s69, s69, 491520\n"
    "s_mov_b32 s22, 3584\n"
    "s_mov_b32 s23, 0x20000\n"
    "s_mov_b32 s26, 1606656\n"
    "s_mov_b32 s27, 0x20000\n"
    "s_sub_u32 s24, s24, 1024\n"
    "s_subb_u32 s25, s25, 0\n"
    "s_lshl_b32 s76, s78, 8\n"
    "v_lshl_add_u32 v46, v254, 1, s76\n"
    "v_mov_b32 v22, 147968\n"
    "v_lshl_add_u32 v22, v254, 2, v22\n"
    "s_lshl_b32 s76, s4, 8\n"
    "v_add_u32 v23, s76, v22\n"
    "s_lshr_b32 s76, s4, 1\n"
    "s_lshl_b32 s76, s76, 3\n"
    "s_and_b32 s77, s4, 1\n"
    "s_add_u32 s76, s76, s77\n"
    "s_lshl_b32 s76, s76, 4\n"
    "v_add_u32 v40, s76, v38\n"
    "v_mul_u32_u24 v40, 896, v40\n"
    "v_lshl_add_u32 v40, v39, 4, v40\n"
    "s_lshl_b32 s76, s74, 7\n"
    "s_add_u32 s76, s76, 64\n"
    "v_add_u32 v25, s76, v40\n"
    "v_add_u32 v24, 960, v25\n"
    "v_lshlrev_b32 v26, 4, v254\n"
    "v_lshrrev_b32 v40, 3, v254\n"
    "v_lshl_add_u32 v27, v40, 28, v26\n"
    "s_mul_i32 s76, s72, 14\n"
    "s_add_u32 s76, s76, s78\n"
    "s_lshl_b32 s66, s76, 10\n"
    "s_add_u32 s66, s66, 4718592\n"
    "s_lshl_b32 s67, s76, 7\n"
    "s_add_u32 s67, s67, 524288\n"
    "s_mul_i32 s68, s79, 3584\n"
    "s_add_u32 s68, s68, 524288\n"
    "v_lshlrev_b32 v47, 4, v35\n"
    "v_lshl_add_u32 v248, v36, 8, v37\n"
    "v_add_u32 v248, s62, v248\n"
    "v_add_u32 v249, 0x10000, v248\n"
    "s_and_b32 s76, s4, 1\n"
    "s_mul_i32 s76, s76, 896\n"
    "s_mul_i32 s77, s79, 28\n"
    "s_add_u32 s76, s76, s77\n"
    "s_lshr_b32 s77, s4, 1\n"
    "s_add_u32 s76, s76, s77\n"
    "s_lshl_b32 s76, s76, 10\n"
    "s_add_u32 s76, s76, 4718592\n"
    "v_add_u32 v250, s76, v26\n"
    "v_add_u32 v251, 14336, v250\n"
    "v_min_u32 v3, 13, v254\n"
    "s_lshl_b32 s77, s77, 7\n"
    "v_lshl_add_u32 v3, v3, 8, s77\n"
    "s_mov_b32 s48, 0\n"
    "s_mov_b32 s49, 2048\n"
    "s_mov_b32 s50, 4096\n"
    "s_mov_b32 s51, 6144\n"
    "s_mov_b32 s52, 8192\n"
    "s_mov_b32 s53, 10240\n"
    "s_mov_b32 s54, 12288\n"
    "s_add_u32 m0, s62, 32768\n"
    "s_mul_i32 s41, s36, 1792\n"
    "buffer_load_dwordx4 v8, s[16:19], s41 offen offset:0 lds\n"
    "buffer_load_dwordx4 v9, s[16:19], s41 offen offset:1024 lds\n"
    "buffer_load_dwordx4 v10, s[16:19], s41 offen offset:2048 lds\n"
    "buffer_load_dwordx4 v11, s[16:19], s41 offen offset:3072 lds\n"
    "s_add_u32 m0, s64, 52\n"
    "s_nop 0\n"
    "buffer_load_dword v20, s[12:15], 0 offen lds\n"
    ".La26_%=:\n"
    "ds_read_b32 v31, v21 offset:60\n"
    "s_sub_u32 s65, s65, 1\n"
    "s_cmp_lt_i32 s65, 0\n"
    "s_waitcnt lgkmcnt(0)\n"
    "v_cmp_ne_u32 vcc, 0, v31\n"
    "s_cbranch_scc1 .La27_%=\n"
    "s_cbranch_vccnz .La26_%=\n"
    ".La27_%=:\n"
    "ds_read_b128 v[112:115], v12 offset:0\n"
    "ds_read_b64_tr_b16 v[208:209], v16 offset:0\n"
    "ds_read_b64_tr_b16 v[210:211], v16 offset:256\n"
    "ds_read_b128 v[116:119], v13 offset:0\n"
    "ds_read_b128 v[120:123], v14 offset:0\n"
    "ds_read_b128 v[124:127], v15 offset:0\n"
    "ds_read_b64_tr_b16 v[212:213], v16 offset:1024\n"
    "ds_read_b64_tr_b16 v[214:215], v16 offset:1280\n"
    "ds_read_b64_tr_b16 v[216:217], v16 offset:2048\n"
    "ds_read_b64_tr_b16 v[218:219], v16 offset:2304\n"
    "ds_read_b64_tr_b16 v[220:221], v16 offset:3072\n"
    "ds_read_b64_tr_b16 v[222:223], v16 offset:3328\n"
    "s_add_u32 s32, s32, 1\n"
    "s_addc_u32 s33, s33, 0\n"
    "s_add_u32 m0, s63, 0\n"
    "s_waitcnt lgkmcnt(4)\n"
    "v_mfma_f32_32x32x16_bf16 v[144:159], v[208:211], v[112:115], 0\n"
    "ds_read_b64_tr_b16 v[224:225], v17 offset:0\n"
    "ds_read_b64_tr_b16 v[226:227], v17 offset:256\n"
    "buffer_load_dwordx4 v4, s[12:15], s36 offen offset:0 lds\n"
    "v_mfma_f32_16x16x32_bf16 v[104:107], v[240:243], v[208:211], 0\n"
    "v_mfma_f32_32x32x16_bf16 v[160:175], v[212:215], v[112:115], 0\n"
    "ds_read_b64_tr_b16 v[228:229], v17 offset:1024\n"
    "ds_read_b64_tr_b16 v[230:231], v17 offset:1280\n"
    "buffer_load_dwordx4 v5, s[12:15], s36 offen offset:1024 lds\n"
    "v_mfma_f32_16x16x32_bf16 v[104:107], v[244:247], v[212:215], v[104:107]\n"
    "s_waitcnt lgkmcnt(4)\n"
    "v_mfma_f32_32x32x16_bf16 v[176:191], v[216:219], v[112:115], 0\n"
    "ds_read_b64_tr_b16 v[232:233], v17 offset:2048\n"
    "ds_read_b64_tr_b16 v[234:235], v17 offset:2304\n"
    "buffer_load_dwordx4 v6, s[12:15], s36 offen offset:2048 lds\n"
    "v_mfma_f32_16x16x32_bf16 v[108:111], v[240:243], v[216:219], 0\n"
    "v_mfma_f32_32x32x16_bf16 v[192:207], v[220:223], v[112:115], 0\n"
    "ds_read_b64_tr_b16 v[236:237], v17 offset:3072\n"
    "ds_read_b64_tr_b16 v[238:239], v17 offset:3328\n"
    "buffer_load_dwordx4 v7, s[12:15], s36 offen offset:3072 lds\n"
    "s_add_u32 m0, s62, 49152\n"
    "s_mul_i32 s41, s37, 1792\n"
    "v_mfma_f32_16x16x32_bf16 v[108:111], v[244:247], v[220:223], v[108:111]\n"
    "s_waitcnt lgkmcnt(4)\n"
    "v_mfma_f32_32x32x16_bf16 v[144:159], v[224:227], v[116:119], v[144:159]\n"
    "ds_read_b64_tr_b16 v[208:209], v18 offset:0\n"
    "ds_read_b64_tr_b16 v[210:211], v18 offset:256\n"
    "buffer_load_dwordx4 v8, s[16:19], s41 offen offset:0 lds\n"
    "v_mfma_f32_32x32x16_bf16 v[160:175], v[228:231], v[116:119], v[160:175]\n"
    "ds_read_b64_tr_b16 v[212:213], v18 offset:1024\n"
    "ds_read_b64_tr_b16 v[214:215], v18 offset:1280\n"
    "buffer_load_dwordx4 v9, s[16:19], s41 offen offset:1024 lds\n"
    "s_waitcnt lgkmcnt(4)\n"
    "v_mfma_f32_32x32x16_bf16 v[176:191], v[232:235], v[116:119], v[176:191]\n"
    "ds_read_b64_tr_b16 v[216:217], v18 offset:2048\n"
    "ds_read_b64_tr_b16 v[218:219], v18 offset:2304\n"
    "buffer_load_dwordx4 v10, s[16:19], s41 offen offset:2048 lds\n"
    "v_mfma_f32_32x32x16_bf16 v[192:207], v[236:239], v[116:119], v[192:207]\n"
    "ds_read_b64_tr_b16 v[220:221], v18 offset:3072\n"
    "ds_read_b64_tr_b16 v[222:223], v18 offset:3328\n"
    "buffer_load_dwordx4 v11, s[16:19], s41 offen offset:3072 lds\n"
    "s_waitcnt lgkmcnt(4)\n"
    "v_mfma_f32_32x32x16_bf16 v[144:159], v[208:211], v[120:123], v[144:159]\n"
    "ds_read_b64_tr_b16 v[224:225], v19 offset:0\n"
    "ds_read_b64_tr_b16 v[226:227], v19 offset:256\n"
    "s_add_u32 m0, s64, 48\n"
    "ds_read_b32 v31, v21 offset:56\n"
    "v_mfma_f32_32x32x16_bf16 v[160:175], v[212:215], v[120:123], v[160:175]\n"
    "ds_read_b64_tr_b16 v[228:229], v19 offset:1024\n"
    "ds_read_b64_tr_b16 v[230:231], v19 offset:1280\n"
    "buffer_load_dword v20, s[12:15], 0 offen lds\n"
    "s_waitcnt lgkmcnt(5)\n"
    "v_mfma_f32_32x32x16_bf16 v[176:191], v[216:219], v[120:123], v[176:191]\n"
    "ds_read_b64_tr_b16 v[232:233], v19 offset:2048\n"
    "ds_read_b64_tr_b16 v[234:235], v19 offset:2304\n"
    "v_mfma_f32_32x32x16_bf16 v[192:207], v[220:223], v[120:123], v[192:207]\n"
    "ds_read_b64_tr_b16 v[236:237], v19 offset:3072\n"
    "ds_read_b64_tr_b16 v[238:239], v19 offset:3328\n"
    "s_waitcnt lgkmcnt(6)\n"
    "v_cmp_ne_u32 vcc, 0, v31\n"
    "s_cbranch_vccnz .La28_%=\n"
    ".La29_%=:\n"
    "s_waitcnt lgkmcnt(4)\n"
    "v_mfma_f32_32x32x16_bf16 v[144:159], v[224:227], v[124:127], v[144:159]\n"
    "s_waitcnt vmcnt(14)\n"
    "ds_read_b128 v[128:131], v12 offset:16384\n"
    "ds_read_b128 v[132:135], v13 offset:16384\n"
    "ds_read_b64_tr_b16 v[208:209], v16 offset:16384\n"
    "ds_read_b64_tr_b16 v[210:211], v16 offset:16640\n"
    "v_mfma_f32_32x32x16_bf16 v[160:175], v[228:231], v[124:127], v[160:175]\n"
    "ds_read_b128 v[136:139], v14 offset:16384\n"
    "ds_read_b128 v[140:143], v15 offset:16384\n"
    "ds_read_b64_tr_b16 v[212:213], v16 offset:17408\n"
    "ds_read_b64_tr_b16 v[214:215], v16 offset:17664\n"
    "s_waitcnt lgkmcnt(8)\n"
    "v_mfma_f32_32x32x16_bf16 v[176:191], v[232:235], v[124:127], v[176:191]\n"
    "ds_read_b64_tr_b16 v[216:217], v16 offset:18432\n"
    "ds_read_b64_tr_b16 v[218:219], v16 offset:18688\n"
    "v_mfma_f32_32x32x16_bf16 v[192:207], v[236:239], v[124:127], v[192:207]\n"
    "ds_read_b64_tr_b16 v[220:221], v16 offset:19456\n"
    "ds_read_b64_tr_b16 v[222:223], v16 offset:19712\n"
    "s_add_u32 m0, s63, 16384\n"
    "s_waitcnt lgkmcnt(4)\n"
    "v_mfma_f32_32x32x16_bf16 v[144:159], v[208:211], v[128:131], v[144:159]\n"
    "ds_read_b64_tr_b16 v[224:225], v17 offset:16384\n"
    "ds_read_b64_tr_b16 v[226:227], v17 offset:16640\n"
    "buffer_load_dwordx4 v4, s[12:15], s37 offen offset:0 lds\n"
    "v_mfma_f32_16x16x32_bf16 v[104:107], v[240:243], v[208:211], v[104:107]\n"
    "v_mfma_f32_32x32x16_bf16 v[160:175], v[212:215], v[128:131], v[160:175]\n"
    "ds_read_b64_tr_b16 v[228:229], v17 offset:17408\n"
    "ds_read_b64_tr_b16 v[230:231], v17 offset:17664\n"
    "buffer_load_dwordx4 v5, s[12:15], s37 offen offset:1024 lds\n"
    "v_mfma_f32_16x16x32_bf16 v[104:107], v[244:247], v[212:215], v[104:107]\n"
    "s_waitcnt lgkmcnt(4)\n"
    "v_mfma_f32_32x32x16_bf16 v[176:191], v[216:219], v[128:131], v[176:191]\n"
    "ds_read_b64_tr_b16 v[232:233], v17 offset:18432\n"
    "ds_read_b64_tr_b16 v[234:235], v17 offset:18688\n"
    "buffer_load_dwordx4 v6, s[12:15], s37 offen offset:2048 lds\n"
    "v_mfma_f32_16x16x32_bf16 v[108:111], v[240:243], v[216:219], v[108:111]\n"
    "v_mfma_f32_32x32x16_bf16 v[192:207], v[220:223], v[128:131], v[192:207]\n"
    "ds_read_b64_tr_b16 v[236:237], v17 offset:19456\n"
    "ds_read_b64_tr_b16 v[238:239], v17 offset:19712\n"
    "buffer_load_dwordx4 v7, s[12:15], s37 offen offset:3072 lds\n"
    "s_add_u32 m0, s62, 65536\n"
    "s_mul_i32 s41, s38, 1792\n"
    "v_mfma_f32_16x16x32_bf16 v[108:111], v[244:247], v[220:223], v[108:111]\n"
    "s_waitcnt lgkmcnt(4)\n"
    "v_mfma_f32_32x32x16_bf16 v[144:159], v[224:227], v[132:135], v[144:159]\n"
    "ds_read_b64_tr_b16 v[208:209], v18 offset:16384\n"
    "ds_read_b64_tr_b16 v[210:211], v18 offset:16640\n"
    "buffer_load_dwordx4 v8, s[16:19], s41 offen offset:0 lds\n"
    "v_mfma_f32_32x32x16_bf16 v[160:175], v[228:231], v[132:135], v[160:175]\n"
    "ds_read_b64_tr_b16 v[212:213], v18 offset:17408\n"
    "ds_read_b64_tr_b16 v[214:215], v18 offset:17664\n"
    "buffer_load_dwordx4 v9, s[16:19], s41 offen offset:1024 lds\n"
    "s_waitcnt lgkmcnt(4)\n"
    "v_mfma_f32_32x32x16_bf16 v[176:191], v[232:235], v[132:135], v[176:191]\n"
    "ds_read_b64_tr_b16 v[216:217], v18 offset:18432\n"
    "ds_read_b64_tr_b16 v[218:219], v18 offset:18688\n"
    "buffer_load_dwordx4 v10, s[16:19], s41 offen offset:2048 lds\n"
    "v_mfma_f32_32x32x16_bf16 v[192:207], v[236:239], v[132:135], v[192:207]\n"
    "ds_read_b64_tr_b16 v[220:221], v18 offset:19456\n"
    "ds_read_b64_tr_b16 v[222:223], v18 offset:19712\n"
    "buffer_load_dwordx4 v11, s[16:19], s41 offen offset:3072 lds\n"
    "s_waitcnt lgkmcnt(4)\n"
    "v_mfma_f32_32x32x16_bf16 v[144:159], v[208:211], v[136:139], v[144:159]\n"
    "ds_read_b64_tr_b16 v[224:225], v19 offset:16384\n"
    "ds_read_b64_tr_b16 v[226:227], v19 offset:16640\n"
    "s_add_u32 m0, s64, 44\n"
    "ds_read_b32 v31, v21 offset:52\n"
    "v_mfma_f32_32x32x16_bf16 v[160:175], v[212:215], v[136:139], v[160:175]\n"
    "ds_read_b64_tr_b16 v[228:229], v19 offset:17408\n"
    "ds_read_b64_tr_b16 v[230:231], v19 offset:17664\n"
    "buffer_load_dword v20, s[12:15], 0 offen lds\n"
    "s_waitcnt lgkmcnt(5)\n"
    "v_mfma_f32_32x32x16_bf16 v[176:191], v[216:219], v[136:139], v[176:191]\n"
    "ds_read_b64_tr_b16 v[232:233], v19 offset:18432\n"
    "ds_read_b64_tr_b16 v[234:235], v19 offset:18688\n"
    "v_mfma_f32_32x32x16_bf16 v[192:207], v[220:223], v[136:139], v[192:207]\n"
    "ds_read_b64_tr_b16 v[236:237], v19 offset:19456\n"
    "ds_read_b64_tr_b16 v[238:239], v19 offset:19712\n"
    "s_waitcnt lgkmcnt(6)\n"
    "v_cmp_ne_u32 vcc, 0, v31\n"
    "s_cbranch_vccnz .La30_%=\n"
    ".La31_%=:\n"
    "s_waitcnt lgkmcnt(4)\n"
    "v_mfma_f32_32x32x16_bf16 v[144:159], v[224:227], v[140:143], v[144:159]\n"
    "s_waitcnt vmcnt(14)\n"
    "ds_read_b128 v[112:115], v12 offset:0\n"
    "ds_read_b128 v[116:119], v13 offset:0\n"
    "ds_read_b64_tr_b16 v[208:209], v16 offset:32768\n"
    "ds_read_b64_tr_b16 v[210:211], v16 offset:33024\n"
    "v_mfma_f32_32x32x16_bf16 v[160:175], v[228:231], v[140:143], v[160:175]\n"
    "ds_read_b128 v[120:123], v14 offset:0\n"
    "ds_read_b128 v[124:127], v15 offset:0\n"
    "ds_read_b64_tr_b16 v[212:213], v16 offset:33792\n"
    "ds_read_b64_tr_b16 v[214:215], v16 offset:34048\n"
    "s_waitcnt lgkmcnt(8)\n"
    "v_mfma_f32_32x32x16_bf16 v[176:191], v[232:235], v[140:143], v[176:191]\n"
    "ds_read_b64_tr_b16 v[216:217], v16 offset:34816\n"
    "ds_read_b64_tr_b16 v[218:219], v16 offset:35072\n"
    "v_mfma_f32_32x32x16_bf16 v[192:207], v[236:239], v[140:143], v[192:207]\n"
    "ds_read_b64_tr_b16 v[220:221], v16 offset:35840\n"
    "ds_read_b64_tr_b16 v[222:223], v16 offset:36096\n"
    "s_add_u32 m0, s63, 0\n"
    "s_waitcnt lgkmcnt(4)\n"
    "v_mfma_f32_32x32x16_bf16 v[144:159], v[208:211], v[112:115], v[144:159]\n"
    "ds_read_b64_tr_b16 v[224:225], v17 offset:32768\n"
    "ds_read_b64_tr_b16 v[226:227], v17 offset:33024\n"
    "buffer_load_dwordx4 v4, s[12:15], s38 offen offset:0 lds\n"
    "v_mfma_f32_16x16x32_bf16 v[104:107], v[240:243], v[208:211], v[104:107]\n"
    "v_mfma_f32_32x32x16_bf16 v[160:175], v[212:215], v[112:115], v[160:175]\n"
    "ds_read_b64_tr_b16 v[228:229], v17 offset:33792\n"
    "ds_read_b64_tr_b16 v[230:231], v17 offset:34048\n"
    "buffer_load_dwordx4 v5, s[12:15], s38 offen offset:1024 lds\n"
    "v_mfma_f32_16x16x32_bf16 v[104:107], v[244:247], v[212:215], v[104:107]\n"
    "s_waitcnt lgkmcnt(4)\n"
    "v_mfma_f32_32x32x16_bf16 v[176:191], v[216:219], v[112:115], v[176:191]\n"
    "ds_read_b64_tr_b16 v[232:233], v17 offset:34816\n"
    "ds_read_b64_tr_b16 v[234:235], v17 offset:35072\n"
    "buffer_load_dwordx4 v6, s[12:15], s38 offen offset:2048 lds\n"
    "v_mfma_f32_16x16x32_bf16 v[108:111], v[240:243], v[216:219], v[108:111]\n"
    "v_mfma_f32_32x32x16_bf16 v[192:207], v[220:223], v[112:115], v[192:207]\n"
    "ds_read_b64_tr_b16 v[236:237], v17 offset:35840\n"
    "ds_read_b64_tr_b16 v[238:239], v17 offset:36096\n"
    "buffer_load_dwordx4 v7, s[12:15], s38 offen offset:3072 lds\n"
    "s_add_u32 m0, s62, 81920\n"
    "s_mul_i32 s41, s39, 1792\n"
    "v_mfma_f32_16x16x32_bf16 v[108:111], v[244:247], v[220:223], v[108:111]\n"
    "s_waitcnt lgkmcnt(4)\n"
    "v_mfma_f32_32x32x16_bf16 v[144:159], v[224:227], v[116:119], v[144:159]\n"
    "ds_read_b64_tr_b16 v[208:209], v18 offset:32768\n"
    "ds_read_b64_tr_b16 v[210:211], v18 offset:33024\n"
    "buffer_load_dwordx4 v8, s[16:19], s41 offen offset:0 lds\n"
    "v_mfma_f32_32x32x16_bf16 v[160:175], v[228:231], v[116:119], v[160:175]\n"
    "ds_read_b64_tr_b16 v[212:213], v18 offset:33792\n"
    "ds_read_b64_tr_b16 v[214:215], v18 offset:34048\n"
    "buffer_load_dwordx4 v9, s[16:19], s41 offen offset:1024 lds\n"
    "s_waitcnt lgkmcnt(4)\n"
    "v_mfma_f32_32x32x16_bf16 v[176:191], v[232:235], v[116:119], v[176:191]\n"
    "ds_read_b64_tr_b16 v[216:217], v18 offset:34816\n"
    "ds_read_b64_tr_b16 v[218:219], v18 offset:35072\n"
    "buffer_load_dwordx4 v10, s[16:19], s41 offen offset:2048 lds\n"
    "v_mfma_f32_32x32x16_bf16 v[192:207], v[236:239], v[116:119], v[192:207]\n"
    "ds_read_b64_tr_b16 v[220:221], v18 offset:35840\n"
    "ds_read_b64_tr_b16 v[222:223], v18 offset:36096\n"
    "buffer_load_dwordx4 v11, s[16:19], s41 offen offset:3072 lds\n"
    "s_waitcnt lgkmcnt(4)\n"
    "v_mfma_f32_32x32x16_bf16 v[144:159], v[208:211], v[120:123], v[144:159]\n"
    "ds_read_b64_tr_b16 v[224:225], v19 offset:32768\n"
    "ds_read_b64_tr_b16 v[226:227], v19 offset:33024\n"
    "s_add_u32 m0, s64, 40\n"
    "ds_read_b32 v31, v21 offset:48\n"
    "v_mfma_f32_32x32x16_bf16 v[160:175], v[212:215], v[120:123], v[160:175]\n"
    "ds_read_b64_tr_b16 v[228:229], v19 offset:33792\n"
    "ds_read_b64_tr_b16 v[230:231], v19 offset:34048\n"
    "buffer_load_dword v20, s[12:15], 0 offen lds\n"
    "s_waitcnt lgkmcnt(5)\n"
    "v_mfma_f32_32x32x16_bf16 v[176:191], v[216:219], v[120:123], v[176:191]\n"
    "ds_read_b64_tr_b16 v[232:233], v19 offset:34816\n"
    "ds_read_b64_tr_b16 v[234:235], v19 offset:35072\n"
    "v_mfma_f32_32x32x16_bf16 v[192:207], v[220:223], v[120:123], v[192:207]\n"
    "ds_read_b64_tr_b16 v[236:237], v19 offset:35840\n"
    "ds_read_b64_tr_b16 v[238:239], v19 offset:36096\n"
    "s_waitcnt lgkmcnt(6)\n"
    "v_cmp_ne_u32 vcc, 0, v31\n"
    "s_cbranch_vccnz .La32_%=\n"
    ".La33_%=:\n"
    "s_waitcnt lgkmcnt(4)\n"
    "v_mfma_f32_32x32x16_bf16 v[144:159], v[224:227], v[124:127], v[144:159]\n"
    "s_waitcnt vmcnt(14)\n"
    "ds_read_b128 v[128:131], v12 offset:16384\n"
    "ds_read_b128 v[132:135], v13 offset:16384\n"
    "ds_read_b64_tr_b16 v[208:209], v16 offset:49152\n"
    "ds_read_b64_tr_b16 v[210:211], v16 offset:49408\n"
    "v_mfma_f32_32x32x16_bf16 v[160:175], v[228:231], v[124:127], v[160:175]\n"
    "ds_read_b128 v[136:139], v14 offset:16384\n"
    "ds_read_b128 v[140:143], v15 offset:16384\n"
    "ds_read_b64_tr_b16 v[212:213], v16 offset:50176\n"
    "ds_read_b64_tr_b16 v[214:215], v16 offset:50432\n"
    "s_waitcnt lgkmcnt(8)\n"
    "v_mfma_f32_32x32x16_bf16 v[176:191], v[232:235], v[124:127], v[176:191]\n"
    "ds_read_b64_tr_b16 v[216:217], v16 offset:51200\n"
    "ds_read_b64_tr_b16 v[218:219], v16 offset:51456\n"
    "v_mfma_f32_32x32x16_bf16 v[192:207], v[236:239], v[124:127], v[192:207]\n"
    "ds_read_b64_tr_b16 v[220:221], v16 offset:52224\n"
    "ds_read_b64_tr_b16 v[222:223], v16 offset:52480\n"
    "s_add_u32 m0, s63, 16384\n"
    "s_waitcnt lgkmcnt(4)\n"
    "v_mfma_f32_32x32x16_bf16 v[144:159], v[208:211], v[128:131], v[144:159]\n"
    "ds_read_b64_tr_b16 v[224:225], v17 offset:49152\n"
    "ds_read_b64_tr_b16 v[226:227], v17 offset:49408\n"
    "buffer_load_dwordx4 v4, s[12:15], s39 offen offset:0 lds\n"
    "v_mfma_f32_16x16x32_bf16 v[104:107], v[240:243], v[208:211], v[104:107]\n"
    "v_mfma_f32_32x32x16_bf16 v[160:175], v[212:215], v[128:131], v[160:175]\n"
    "ds_read_b64_tr_b16 v[228:229], v17 offset:50176\n"
    "ds_read_b64_tr_b16 v[230:231], v17 offset:50432\n"
    "buffer_load_dwordx4 v5, s[12:15], s39 offen offset:1024 lds\n"
    "v_mfma_f32_16x16x32_bf16 v[104:107], v[244:247], v[212:215], v[104:107]\n"
    "s_waitcnt lgkmcnt(4)\n"
    "v_mfma_f32_32x32x16_bf16 v[176:191], v[216:219], v[128:131], v[176:191]\n"
    "ds_read_b64_tr_b16 v[232:233], v17 offset:51200\n"
    "ds_read_b64_tr_b16 v[234:235], v17 offset:51456\n"
    "buffer_load_dwordx4 v6, s[12:15], s39 offen offset:2048 lds\n"
    "v_mfma_f32_16x16x32_bf16 v[108:111], v[240:243], v[216:219], v[108:111]\n"
    "v_mfma_f32_32x32x16_bf16 v[192:207], v[220:223], v[128:131], v[192:207]\n"
    "ds_read_b64_tr_b16 v[236:237], v17 offset:52224\n"
    "ds_read_b64_tr_b16 v[238:239], v17 offset:52480\n"
    "buffer_load_dwordx4 v7, s[12:15], s39 offen offset:3072 lds\n"
    "s_add_u32 m0, s62, 98304\n"
    "s_mul_i32 s41, s40, 1792\n"
    "v_mfma_f32_16x16x32_bf16 v[108:111], v[244:247], v[220:223], v[108:111]\n"
    "s_waitcnt lgkmcnt(4)\n"
    "v_mfma_f32_32x32x16_bf16 v[144:159], v[224:227], v[132:135], v[144:159]\n"
    "ds_read_b64_tr_b16 v[208:209], v18 offset:49152\n"
    "ds_read_b64_tr_b16 v[210:211], v18 offset:49408\n"
    "buffer_load_dwordx4 v8, s[16:19], s41 offen offset:0 lds\n"
    "v_mfma_f32_32x32x16_bf16 v[160:175], v[228:231], v[132:135], v[160:175]\n"
    "ds_read_b64_tr_b16 v[212:213], v18 offset:50176\n"
    "ds_read_b64_tr_b16 v[214:215], v18 offset:50432\n"
    "buffer_load_dwordx4 v9, s[16:19], s41 offen offset:1024 lds\n"
    "s_waitcnt lgkmcnt(4)\n"
    "v_mfma_f32_32x32x16_bf16 v[176:191], v[232:235], v[132:135], v[176:191]\n"
    "ds_read_b64_tr_b16 v[216:217], v18 offset:51200\n"
    "ds_read_b64_tr_b16 v[218:219], v18 offset:51456\n"
    "buffer_load_dwordx4 v10, s[16:19], s41 offen offset:2048 lds\n"
    "v_mfma_f32_32x32x16_bf16 v[192:207], v[236:239], v[132:135], v[192:207]\n"
    "ds_read_b64_tr_b16 v[220:221], v18 offset:52224\n"
    "ds_read_b64_tr_b16 v[222:223], v18 offset:52480\n"
    "buffer_load_dwordx4 v11, s[16:19], s41 offen offset:3072 lds\n"
    "s_waitcnt lgkmcnt(4)\n"
    "v_mfma_f32_32x32x16_bf16 v[144:159], v[208:211], v[136:139], v[144:159]\n"
    "ds_read_b64_tr_b16 v[224:225], v19 offset:49152\n"
    "ds_read_b64_tr_b16 v[226:227], v19 offset:49408\n"
    "s_add_u32 m0, s64, 36\n"
    "ds_read_b32 v31, v21 offset:44\n"
    "v_mfma_f32_32x32x16_bf16 v[160:175], v[212:215], v[136:139], v[160:175]\n"
    "ds_read_b64_tr_b16 v[228:229], v19 offset:50176\n"
    "ds_read_b64_tr_b16 v[230:231], v19 offset:50432\n"
    "buffer_load_dword v20, s[12:15], 0 offen lds\n"
    "s_waitcnt lgkmcnt(5)\n"
    "v_mfma_f32_32x32x16_bf16 v[176:191], v[216:219], v[136:139], v[176:191]\n"
    "ds_read_b64_tr_b16 v[232:233], v19 offset:51200\n"
    "ds_read_b64_tr_b16 v[234:235], v19 offset:51456\n"
    "v_mfma_f32_32x32x16_bf16 v[192:207], v[220:223], v[136:139], v[192:207]\n"
    "ds_read_b64_tr_b16 v[236:237], v19 offset:52224\n"
    "ds_read_b64_tr_b16 v[238:239], v19 offset:52480\n"
    "s_waitcnt lgkmcnt(6)\n"
    "v_cmp_ne_u32 vcc, 0, v31\n"
    "s_cbranch_vccnz .La34_%=\n"
    ".La35_%=:\n"
    "s_waitcnt lgkmcnt(4)\n"
    "v_mfma_f32_32x32x16_bf16 v[144:159], v[224:227], v[140:143], v[144:159]\n"
    "s_waitcnt vmcnt(14)\n"
    "ds_read_b128 v[112:115], v12 offset:0\n"
    "ds_read_b128 v[116:119], v13 offset:0\n"
    "v_add_u32 v16, 0x10000, v16\n"
    "ds_read_b64_tr_b16 v[208:209], v16 offset:0\n"
    "ds_read_b64_tr_b16 v[210:211], v16 offset:256\n"
    "v_mfma_f32_32x32x16_bf16 v[160:175], v[228:231], v[140:143], v[160:175]\n"
    "ds_read_b128 v[120:123], v14 offset:0\n"
    "ds_read_b128 v[124:127], v15 offset:0\n"
    "ds_read_b64_tr_b16 v[212:213], v16 offset:1024\n"
    "ds_read_b64_tr_b16 v[214:215], v16 offset:1280\n"
    "s_waitcnt lgkmcnt(8)\n"
    "v_mfma_f32_32x32x16_bf16 v[176:191], v[232:235], v[140:143], v[176:191]\n"
    "ds_read_b64_tr_b16 v[216:217], v16 offset:2048\n"
    "ds_read_b64_tr_b16 v[218:219], v16 offset:2304\n"
    "v_mfma_f32_32x32x16_bf16 v[192:207], v[236:239], v[140:143], v[192:207]\n"
    "ds_read_b64_tr_b16 v[220:221], v16 offset:3072\n"
    "ds_read_b64_tr_b16 v[222:223], v16 offset:3328\n"
    "s_add_u32 m0, s63, 0\n"
    "s_waitcnt lgkmcnt(4)\n"
    "v_mfma_f32_32x32x16_bf16 v[144:159], v[208:211], v[112:115], v[144:159]\n"
    "v_add_u32 v17, 0x10000, v17\n"
    "ds_read_b64_tr_b16 v[224:225], v17 offset:0\n"
    "ds_read_b64_tr_b16 v[226:227], v17 offset:256\n"
    "buffer_load_dwordx4 v4, s[12:15], s40 offen offset:0 lds\n"
    "v_mfma_f32_16x16x32_bf16 v[104:107], v[240:243], v[208:211], v[104:107]\n"
    "v_mfma_f32_32x32x16_bf16 v[160:175], v[212:215], v[112:115], v[160:175]\n"
    "ds_read_b64_tr_b16 v[228:229], v17 offset:1024\n"
    "ds_read_b64_tr_b16 v[230:231], v17 offset:1280\n"
    "buffer_load_dwordx4 v5, s[12:15], s40 offen offset:1024 lds\n"
    "v_mfma_f32_16x16x32_bf16 v[104:107], v[244:247], v[212:215], v[104:107]\n"
    "s_waitcnt lgkmcnt(4)\n"
    "v_mfma_f32_32x32x16_bf16 v[176:191], v[216:219], v[112:115], v[176:191]\n"
    "ds_read_b64_tr_b16 v[232:233], v17 offset:2048\n"
    "ds_read_b64_tr_b16 v[234:235], v17 offset:2304\n"
    "buffer_load_dwordx4 v6, s[12:15], s40 offen offset:2048 lds\n"
    "v_mfma_f32_16x16x32_bf16 v[108:111], v[240:243], v[216:219], v[108:111]\n"
    "v_mfma_f32_32x32x16_bf16 v[192:207], v[220:223], v[112:115], v[192:207]\n"
    "ds_read_b64_tr_b16 v[236:237], v17 offset:3072\n"
    "ds_read_b64_tr_b16 v[238:239], v17 offset:3328\n"
    "buffer_load_dwordx4 v7, s[12:15], s40 offen offset:3072 lds\n"
    "s_add_u32 m0, s64, 32\n"
    "v_mfma_f32_16x16x32_bf16 v[108:111], v[244:247], v[220:223], v[108:111]\n"
    "s_waitcnt lgkmcnt(4)\n"
    "v_mfma_f32_32x32x16_bf16 v[144:159], v[224:227], v[116:119], v[144:159]\n"
    "v_add_u32 v18, 0x10000, v18\n"
    "ds_read_b64_tr_b16 v[208:209], v18 offset:0\n"
    "ds_read_b64_tr_b16 v[210:211], v18 offset:256\n"
    "buffer_load_dword v20, s[12:15], 0 offen lds\n"
    "v_mfma_f32_32x32x16_bf16 v[160:175], v[228:231], v[116:119], v[160:175]\n"
    "ds_read_b64_tr_b16 v[212:213], v18 offset:1024\n"
    "ds_read_b64_tr_b16 v[214:215], v18 offset:1280\n"
    "s_add_u32 m0, s62, 0\n"
    "s_mul_i32 s42, s48, 112\n"
    "s_waitcnt lgkmcnt(4)\n"
    "v_mfma_f32_32x32x16_bf16 v[176:191], v[232:235], v[116:119], v[176:191]\n"
    "ds_read_b64_tr_b16 v[216:217], v18 offset:2048\n"
    "ds_read_b64_tr_b16 v[218:219], v18 offset:2304\n"
    "buffer_load_dwordx4 v24, s[24:27], s42 offen offset:0 lds\n"
    "v_mfma_f32_32x32x16_bf16 v[192:207], v[236:239], v[116:119], v[192:207]\n"
    "ds_read_b64_tr_b16 v[220:221], v18 offset:3072\n"
    "ds_read_b64_tr_b16 v[222:223], v18 offset:3328\n"
    "buffer_load_dwordx4 v25, s[24:27], s42 offen offset:1024 lds\n"
    "buffer_load_ushort v1, v46, s[20:23], 0 offen\n"
    "buffer_load_ushort v2, v46, s[20:23], 0 offen offset:128\n"
    "buffer_load_dwordx4 v[48:51], v47, s[28:31], s69 offen sc1\n"
    "s_waitcnt lgkmcnt(4)\n"
    "v_mfma_f32_32x32x16_bf16 v[144:159], v[208:211], v[120:123], v[144:159]\n"
    "v_add_u32 v19, 0x10000, v19\n"
    "ds_read_b64_tr_b16 v[224:225], v19 offset:0\n"
    "ds_read_b64_tr_b16 v[226:227], v19 offset:256\n"
    "ds_read_b32 v31, v21 offset:40\n"
    "v_mfma_f32_32x32x16_bf16 v[160:175], v[212:215], v[120:123], v[160:175]\n"
    "ds_read_b64_tr_b16 v[228:229], v19 offset:1024\n"
    "ds_read_b64_tr_b16 v[230:231], v19 offset:1280\n"
    "s_waitcnt lgkmcnt(5)\n"
    "v_mfma_f32_32x32x16_bf16 v[176:191], v[216:219], v[120:123], v[176:191]\n"
    "ds_read_b64_tr_b16 v[232:233], v19 offset:2048\n"
    "ds_read_b64_tr_b16 v[234:235], v19 offset:2304\n"
    "v_mfma_f32_32x32x16_bf16 v[192:207], v[220:223], v[120:123], v[192:207]\n"
    "ds_read_b64_tr_b16 v[236:237], v19 offset:3072\n"
    "ds_read_b64_tr_b16 v[238:239], v19 offset:3328\n"
    "s_waitcnt lgkmcnt(6)\n"
    "v_cmp_ne_u32 vcc, 0, v31\n"
    "s_cbranch_vccnz .La36_%=\n"
    ".La37_%=:\n"
    "s_waitcnt lgkmcnt(4)\n"
    "v_mfma_f32_32x32x16_bf16 v[144:159], v[224:227], v[124:127], v[144:159]\n"
    "s_waitcnt vmcnt(15)\n"
    "ds_read_b128 v[128:131], v12 offset:16384\n"
    "ds_read_b128 v[132:135], v13 offset:16384\n"
    "ds_read_b64_tr_b16 v[208:209], v16 offset:16384\n"
    "ds_read_b64_tr_b16 v[210:211], v16 offset:16640\n"
    "v_mfma_f32_32x32x16_bf16 v[160:175], v[228:231], v[124:127], v[160:175]\n"
    "ds_read_b128 v[136:139], v14 offset:16384\n"
    "ds_read_b128 v[140:143], v15 offset:16384\n"
    "ds_read_b64_tr_b16 v[212:213], v16 offset:17408\n"
    "ds_read_b64_tr_b16 v[214:215], v16 offset:17664\n"
    "s_waitcnt lgkmcnt(8)\n"
    "v_mfma_f32_32x32x16_bf16 v[176:191], v[232:235], v[124:127], v[176:191]\n"
    "ds_read_b64_tr_b16 v[216:217], v16 offset:18432\n"
    "ds_read_b64_tr_b16 v[218:219], v16 offset:18688\n"
    "v_mfma_f32_32x32x16_bf16 v[192:207], v[236:239], v[124:127], v[192:207]\n"
    "ds_read_b64_tr_b16 v[220:221], v16 offset:19456\n"
    "ds_read_b64_tr_b16 v[222:223], v16 offset:19712\n"
    "s_add_u32 m0, s62, 2048\n"
    "s_mul_i32 s42, s49, 112\n"
    "s_waitcnt lgkmcnt(4)\n"
    "v_mfma_f32_32x32x16_bf16 v[144:159], v[208:211], v[128:131], v[144:159]\n"
    "ds_read_b64_tr_b16 v[224:225], v17 offset:16384\n"
    "ds_read_b64_tr_b16 v[226:227], v17 offset:16640\n"
    "buffer_load_dwordx4 v24, s[24:27], s42 offen offset:0 lds\n"
    "v_mfma_f32_16x16x32_bf16 v[104:107], v[240:243], v[208:211], v[104:107]\n"
    "v_mfma_f32_32x32x16_bf16 v[160:175], v[212:215], v[128:131], v[160:175]\n"
    "ds_read_b64_tr_b16 v[228:229], v17 offset:17408\n"
    "ds_read_b64_tr_b16 v[230:231], v17 offset:17664\n"
    "buffer_load_dwordx4 v25, s[24:27], s42 offen offset:1024 lds\n"
    "s_add_u32 m0, s62, 16384\n"
    "s_mul_i32 s42, s50, 112\n"
    "v_mfma_f32_16x16x32_bf16 v[104:107], v[244:247], v[212:215], v[104:107]\n"
    "s_waitcnt lgkmcnt(4)\n"
    "v_mfma_f32_32x32x16_bf16 v[176:191], v[216:219], v[128:131], v[176:191]\n"
    "ds_read_b64_tr_b16 v[232:233], v17 offset:18432\n"
    "ds_read_b64_tr_b16 v[234:235], v17 offset:18688\n"
    "buffer_load_dwordx4 v24, s[24:27], s42 offen offset:0 lds\n"
    "v_mfma_f32_16x16x32_bf16 v[108:111], v[240:243], v[216:219], v[108:111]\n"
    "v_mfma_f32_32x32x16_bf16 v[192:207], v[220:223], v[128:131], v[192:207]\n"
    "ds_read_b64_tr_b16 v[236:237], v17 offset:19456\n"
    "ds_read_b64_tr_b16 v[238:239], v17 offset:19712\n"
    "buffer_load_dwordx4 v25, s[24:27], s42 offen offset:1024 lds\n"
    "s_add_u32 m0, s62, 18432\n"
    "s_mul_i32 s42, s51, 112\n"
    "v_mfma_f32_16x16x32_bf16 v[108:111], v[244:247], v[220:223], v[108:111]\n"
    "s_waitcnt lgkmcnt(4)\n"
    "v_mfma_f32_32x32x16_bf16 v[144:159], v[224:227], v[132:135], v[144:159]\n"
    "ds_read_b64_tr_b16 v[208:209], v18 offset:16384\n"
    "ds_read_b64_tr_b16 v[210:211], v18 offset:16640\n"
    "buffer_load_dwordx4 v24, s[24:27], s42 offen offset:0 lds\n"
    "s_waitcnt vmcnt(5)\n"
    "v_cmp_ne_u32 vcc, s32, v48\n"
    "v_cmp_ne_u32 s[76:77], s33, v50\n"
    "s_or_b64 vcc, vcc, s[76:77]\n"
    "s_cbranch_vccnz .La38_%=\n"
    ".La39_%=:\n"
    "v_mfma_f32_32x32x16_bf16 v[160:175], v[228:231], v[132:135], v[160:175]\n"
    "ds_read_b64_tr_b16 v[212:213], v18 offset:17408\n"
    "ds_read_b64_tr_b16 v[214:215], v18 offset:17664\n"
    "buffer_load_dwordx4 v25, s[24:27], s42 offen offset:1024 lds\n"
    "v_lshlrev_b32 v32, 16, v51\n"
    "s_waitcnt lgkmcnt(4)\n"
    "v_mfma_f32_32x32x16_bf16 v[176:191], v[232:235], v[132:135], v[176:191]\n"
    "ds_read_b64_tr_b16 v[216:217], v18 offset:18432\n"
    "ds_read_b64_tr_b16 v[218:219], v18 offset:18688\n"
    "v_and_b32 v33, 0xffff0000, v51\n"
    "v_mfma_f32_32x32x16_bf16 v[192:207], v[236:239], v[132:135], v[192:207]\n"
    "ds_read_b64_tr_b16 v[220:221], v18 offset:19456\n"
    "ds_read_b64_tr_b16 v[222:223], v18 offset:19712\n"
    "v_mul_f32 v252, v32, v33\n"
    "s_waitcnt lgkmcnt(4)\n"
    "v_mfma_f32_32x32x16_bf16 v[144:159], v[208:211], v[136:139], v[144:159]\n"
    "ds_read_b64_tr_b16 v[224:225], v19 offset:16384\n"
    "ds_read_b64_tr_b16 v[226:227], v19 offset:16640\n"
    "ds_read_b32 v31, v21 offset:32\n"
    "v_rcp_f32 v34, v252\n"
    "v_mfma_f32_32x32x16_bf16 v[160:175], v[212:215], v[136:139], v[160:175]\n"
    "ds_read_b64_tr_b16 v[228:229], v19 offset:17408\n"
    "ds_read_b64_tr_b16 v[230:231], v19 offset:17664\n"
    "v_cvt_pk_bf16_f32 v33, v252, v252\n"
    "v_lshlrev_b32 v33, 16, v33\n"
    "s_waitcnt lgkmcnt(5)\n"
    "v_mfma_f32_32x32x16_bf16 v[176:191], v[216:219], v[136:139], v[176:191]\n"
    "ds_read_b64_tr_b16 v[232:233], v19 offset:18432\n"
    "ds_read_b64_tr_b16 v[234:235], v19 offset:18688\n"
    "v_sub_f32 v33, v252, v33\n"
    "v_mfma_f32_32x32x16_bf16 v[192:207], v[220:223], v[136:139], v[192:207]\n"
    "ds_read_b64_tr_b16 v[236:237], v19 offset:19456\n"
    "ds_read_b64_tr_b16 v[238:239], v19 offset:19712\n"
    "s_waitcnt lgkmcnt(6)\n"
    "v_cmp_ne_u32 vcc, 0, v31\n"
    "s_cbranch_vccnz .La40_%=\n"
    ".La41_%=:\n"
    "v_cvt_pk_bf16_f32 v33, v252, v33\n"
    "s_waitcnt lgkmcnt(4)\n"
    "v_mfma_f32_32x32x16_bf16 v[144:159], v[224:227], v[140:143], v[144:159]\n"
    "ds_read_b128 v[112:115], v12 offset:0\n"
    "ds_read_b128 v[116:119], v13 offset:0\n"
    "ds_read_b64_tr_b16 v[208:209], v16 offset:32768\n"
    "ds_read_b64_tr_b16 v[210:211], v16 offset:33024\n"
    "v_mul_f32 v28, 0xc0135761, v34\n"
    "v_mfma_f32_32x32x16_bf16 v[160:175], v[228:231], v[140:143], v[160:175]\n"
    "ds_read_b128 v[120:123], v14 offset:0\n"
    "ds_read_b128 v[124:127], v15 offset:0\n"
    "ds_read_b64_tr_b16 v[212:213], v16 offset:33792\n"
    "ds_read_b64_tr_b16 v[214:215], v16 offset:34048\n"
    "v_mul_f32 v30, 0xbdd2d3e8, v34\n"
    "s_waitcnt lgkmcnt(8)\n"
    "v_mfma_f32_32x32x16_bf16 v[176:191], v[232:235], v[140:143], v[176:191]\n"
    "ds_read_b64_tr_b16 v[216:217], v16 offset:34816\n"
    "ds_read_b64_tr_b16 v[218:219], v16 offset:35072\n"
    "v_mul_f32 v34, v34, v34\n"
    "v_mfma_f32_32x32x16_bf16 v[192:207], v[236:239], v[140:143], v[192:207]\n"
    "ds_read_b64_tr_b16 v[220:221], v16 offset:35840\n"
    "ds_read_b64_tr_b16 v[222:223], v16 offset:36096\n"
    "s_add_u32 m0, s62, 32768\n"
    "s_mul_i32 s42, s52, 112\n"
    "v_mul_f32 v30, v30, v34\n"
    "s_waitcnt lgkmcnt(4)\n"
    "v_mfma_f32_32x32x16_bf16 v[144:159], v[208:211], v[112:115], v[144:159]\n"
    "ds_read_b64_tr_b16 v[224:225], v17 offset:32768\n"
    "ds_read_b64_tr_b16 v[226:227], v17 offset:33024\n"
    "buffer_load_dwordx4 v24, s[24:27], s42 offen offset:0 lds\n"
    "v_bfe_i32 v32, v254, 5, 1\n"
    "v_mfma_f32_16x16x32_bf16 v[104:107], v[240:243], v[208:211], v[104:107]\n"
    "v_mfma_f32_32x32x16_bf16 v[160:175], v[212:215], v[112:115], v[160:175]\n"
    "ds_read_b64_tr_b16 v[228:229], v17 offset:33792\n"
    "ds_read_b64_tr_b16 v[230:231], v17 offset:34048\n"
    "buffer_load_dwordx4 v25, s[24:27], s42 offen offset:1024 lds\n"
    "s_add_u32 m0, s62, 34816\n"
    "s_mul_i32 s42, s53, 112\n"
    "v_and_b32 v56, v32, v49\n"
    "v_mfma_f32_16x16x32_bf16 v[104:107], v[244:247], v[212:215], v[104:107]\n"
    "s_waitcnt lgkmcnt(4)\n"
    "v_mfma_f32_32x32x16_bf16 v[176:191], v[216:219], v[112:115], v[176:191]\n"
    "ds_read_b64_tr_b16 v[232:233], v17 offset:34816\n"
    "ds_read_b64_tr_b16 v[234:235], v17 offset:35072\n"
    "buffer_load_dwordx4 v24, s[24:27], s42 offen offset:0 lds\n"
    "v_and_b32 v57, v32, v49\n"
    "v_mfma_f32_16x16x32_bf16 v[108:111], v[240:243], v[216:219], v[108:111]\n"
    "v_mfma_f32_32x32x16_bf16 v[192:207], v[220:223], v[112:115], v[192:207]\n"
    "ds_read_b64_tr_b16 v[236:237], v17 offset:35840\n"
    "ds_read_b64_tr_b16 v[238:239], v17 offset:36096\n"
    "buffer_load_dwordx4 v25, s[24:27], s42 offen offset:1024 lds\n"
    "s_add_u32 m0, s62, 49152\n"
    "s_mul_i32 s42, s54, 112\n"
    "v_and_b32 v58, v32, v33\n"
    "v_mov_b32 v31, s64\n"
    "v_mov_b32 v255, 0\n"
    "ds_write_b32 v31, v255 offset:24\n"
    "v_mfma_f32_16x16x32_bf16 v[108:111], v[244:247], v[220:223], v[108:111]\n"
    "s_waitcnt lgkmcnt(5)\n"
    "v_mfma_f32_32x32x16_bf16 v[144:159], v[224:227], v[116:119], v[144:159]\n"
    "ds_read_b64_tr_b16 v[208:209], v18 offset:32768\n"
    "ds_read_b64_tr_b16 v[210:211], v18 offset:33024\n"
    "buffer_load_dwordx4 v24, s[24:27], s42 offen offset:0 lds\n"
    "v_mov_b32 v59, 0\n"
    "v_mfma_f32_32x32x16_bf16 v[160:175], v[228:231], v[116:119], v[160:175]\n"
    "ds_read_b64_tr_b16 v[212:213], v18 offset:33792\n"
    "ds_read_b64_tr_b16 v[214:215], v18 offset:34048\n"
    "buffer_load_dwordx4 v25, s[24:27], s42 offen offset:1024 lds\n"
    "s_add_u32 m0, s62, 51200\n"
    "s_mul_i32 s42, s48, 112\n"
    "v_add_u32 v24, 28672, v24\n"
    "v_add_u32 v25, 28672, v25\n"
    "v_xor_b32 v52, v56, v49\n"
    "ds_write2st64_b32 v23, v104, v108 offset1:4\n"
    "s_waitcnt lgkmcnt(6)\n"
    "v_mfma_f32_32x32x16_bf16 v[176:191], v[232:235], v[116:119], v[176:191]\n"
    "ds_read_b64_tr_b16 v[216:217], v18 offset:34816\n"
    "ds_read_b64_tr_b16 v[218:219], v18 offset:35072\n"
    "buffer_load_dwordx4 v24, s[24:27], s42 offen offset:0 lds\n"
    "v_xor_b32 v53, v56, v49\n"
    "v_mfma_f32_32x32x16_bf16 v[192:207], v[236:239], v[116:119], v[192:207]\n"
    "ds_read_b64_tr_b16 v[220:221], v18 offset:35840\n"
    "ds_read_b64_tr_b16 v[222:223], v18 offset:36096\n"
    "buffer_load_dwordx4 v25, s[24:27], s42 offen offset:1024 lds\n"
    "v_xor_b32 v54, v58, v33\n"
    "s_waitcnt lgkmcnt(5)\n"
    "v_mfma_f32_32x32x16_bf16 v[144:159], v[208:211], v[120:123], v[144:159]\n"
    "ds_read_b64_tr_b16 v[224:225], v19 offset:32768\n"
    "ds_read_b64_tr_b16 v[226:227], v19 offset:33024\n"
    "v_mov_b32 v55, 0\n"
    "v_mfma_f32_32x32x16_bf16 v[160:175], v[212:215], v[120:123], v[160:175]\n"
    "ds_read_b64_tr_b16 v[228:229], v19 offset:33792\n"
    "ds_read_b64_tr_b16 v[230:231], v19 offset:34048\n"
    "v_lshl_or_b32 v62, v1, 16, v1\n"
    "v_lshl_or_b32 v66, v2, 16, v2\n"
    "s_waitcnt lgkmcnt(4)\n"
    "v_mfma_f32_32x32x16_bf16 v[176:191], v[216:219], v[120:123], v[176:191]\n"
    "ds_read_b64_tr_b16 v[232:233], v19 offset:34816\n"
    "ds_read_b64_tr_b16 v[234:235], v19 offset:35072\n"
    "v_mov_b32 v63, 0\n"
    "v_mfma_f32_32x32x16_bf16 v[192:207], v[220:223], v[120:123], v[192:207]\n"
    "ds_read_b64_tr_b16 v[236:237], v19 offset:35840\n"
    "ds_read_b64_tr_b16 v[238:239], v19 offset:36096\n"
    "v_mov_b32 v67, 0\n"
    "v_mov_b32 v240, s32\n"
    "v_mov_b32 v241, s33\n"
    "v_mov_b32 v242, s33\n"
    "v_mov_b32 v243, s32\n"
    "s_waitcnt lgkmcnt(4)\n"
    "v_mfma_f32_32x32x16_bf16 v[144:159], v[224:227], v[124:127], v[144:159]\n"
    "v_mfma_f32_32x32x16_bf16 v[160:175], v[228:231], v[124:127], v[160:175]\n"
    "ds_read_b32 v32, v22 offset:0\n"
    "ds_read_b32 v33, v22 offset:256\n"
    "ds_read_b32 v34, v22 offset:512\n"
    "ds_read_b32 v35, v22 offset:768\n"
    "ds_read_b32 v36, v22 offset:1024\n"
    "ds_read_b32 v37, v22 offset:1280\n"
    "ds_read_b32 v38, v22 offset:1536\n"
    "ds_read_b32 v39, v22 offset:1792\n"
    "s_waitcnt lgkmcnt(8)\n"
    "v_mfma_f32_32x32x16_bf16 v[176:191], v[232:235], v[124:127], v[176:191]\n"
    "v_mfma_f32_32x32x16_bf16 v[192:207], v[236:239], v[124:127], v[192:207]\n"
    "s_waitcnt lgkmcnt(0)\n"
    "v_add_f32 v32, v32, v33\n"
    "v_add_f32 v34, v34, v35\n"
    "v_add_f32 v36, v36, v37\n"
    "v_add_f32 v38, v38, v39\n"
    "v_add_f32 v32, v32, v34\n"
    "v_add_f32 v36, v36, v38\n"
    "v_cmp_u_f32 vcc, v32, v36\n"
    "s_cbranch_vccnz .La42_%=\n"
    ".La43_%=:\n"
    "v_cvt_pk_bf16_f32 v60, v32, v32\n"
    "v_lshlrev_b32 v61, 16, v60\n"
    "v_sub_f32 v61, v32, v61\n"
    "v_cvt_pk_bf16_f32 v61, v61, v61\n"
    "v_cvt_pk_bf16_f32 v64, v36, v36\n"
    "v_lshlrev_b32 v65, 16, v64\n"
    "v_sub_f32 v65, v36, v65\n"
    "v_cvt_pk_bf16_f32 v65, v65, v65\n"
    "v_mfma_f32_32x32x16_bf16 v[144:159], v[60:63], v[52:55], v[144:159]\n"
    "v_mfma_f32_32x32x16_bf16 v[160:175], v[60:63], v[56:59], v[160:175]\n"
    "v_mfma_f32_32x32x16_bf16 v[176:191], v[64:67], v[52:55], v[176:191]\n"
    "v_mfma_f32_32x32x16_bf16 v[192:207], v[64:67], v[56:59], v[192:207]\n"
    "v_mov_b32 v31, s64\n"
    "v_mov_b32 v255, 0\n"
    "ds_write_b32 v31, v255 offset:28\n"
    "s_add_u32 m0, s62, 65536\n"
    "s_mul_i32 s42, s49, 112\n"
    "buffer_load_dwordx4 v24, s[24:27], s42 offen offset:0 lds\n"
    "buffer_load_dwordx4 v25, s[24:27], s42 offen offset:1024 lds\n"
    "s_nop 3\n"
    "v_pk_mul_f32 v[32:33], v[144:145], v[144:145]\n"
    "v_pk_mul_f32 v[34:35], v[146:147], v[146:147]\n"
    "v_pk_mul_f32 v[36:37], v[148:149], v[148:149]\n"
    "v_pk_mul_f32 v[38:39], v[150:151], v[150:151]\n"
    "v_pk_fma_f32 v[32:33], v[32:33], v[30:31], v[28:29] op_sel_hi:[1,0,0]\n"
    "v_pk_fma_f32 v[34:35], v[34:35], v[30:31], v[28:29] op_sel_hi:[1,0,0]\n"
    "v_pk_fma_f32 v[36:37], v[36:37], v[30:31], v[28:29] op_sel_hi:[1,0,0]\n"
    "v_pk_fma_f32 v[38:39], v[38:39], v[30:31], v[28:29] op_sel_hi:[1,0,0]\n"
    "v_pk_mul_f32 v[32:33], v[32:33], v[144:145]\n"
    "v_pk_mul_f32 v[34:35], v[34:35], v[146:147]\n"
    "v_pk_mul_f32 v[36:37], v[36:37], v[148:149]\n"
    "ds_read_b32 v31, v21 offset:28\n"
    "v_pk_mul_f32 v[38:39], v[38:39], v[150:151]\n"
    "v_exp_f32 v32, v32\n"
    "v_exp_f32 v33, v33\n"
    "v_exp_f32 v34, v34\n"
    "v_exp_f32 v35, v35\n"
    "v_exp_f32 v36, v36\n"
    "v_exp_f32 v37, v37\n"
    "v_exp_f32 v38, v38\n"
    "v_exp_f32 v39, v39\n"
    "v_pk_fma_f32 v[32:33], v[32:33], v[252:253], v[252:253] op_sel_hi:[1,0,0]\n"
    "v_pk_fma_f32 v[34:35], v[34:35], v[252:253], v[252:253] op_sel_hi:[1,0,0]\n"
    "s_waitcnt lgkmcnt(0)\n"
    "v_cmp_ne_u32 vcc, 0, v31\n"
    "s_cbranch_vccnz .La44_%=\n"
    ".La45_%=:\n"
    "v_pk_fma_f32 v[36:37], v[36:37], v[252:253], v[252:253] op_sel_hi:[1,0,0]\n"
    "v_pk_fma_f32 v[38:39], v[38:39], v[252:253], v[252:253] op_sel_hi:[1,0,0]\n"
    "v_rcp_f32 v32, v32\n"
    "v_rcp_f32 v33, v33\n"
    "v_rcp_f32 v34, v34\n"
    "v_rcp_f32 v35, v35\n"
    "v_rcp_f32 v36, v36\n"
    "v_rcp_f32 v37, v37\n"
    "v_rcp_f32 v38, v38\n"
    "v_rcp_f32 v39, v39\n"
    "v_pk_mul_f32 v[32:33], v[32:33], v[144:145]\n"
    "s_add_u32 m0, s62, 67584\n"
    "s_mul_i32 s42, s50, 112\n"
    "buffer_load_dwordx4 v24, s[24:27], s42 offen offset:0 lds\n"
    "v_pk_mul_f32 v[34:35], v[34:35], v[146:147]\n"
    "v_pk_mul_f32 v[36:37], v[36:37], v[148:149]\n"
    "v_pk_mul_f32 v[38:39], v[38:39], v[150:151]\n"
    "v_cvt_pk_bf16_f32 v144, v32, v33\n"
    "v_cvt_pk_bf16_f32 v145, v34, v35\n"
    "v_cvt_pk_bf16_f32 v146, v36, v37\n"
    "v_cvt_pk_bf16_f32 v147, v38, v39\n"
    "v_pk_mul_f32 v[32:33], v[152:153], v[152:153]\n"
    "v_pk_mul_f32 v[34:35], v[154:155], v[154:155]\n"
    "v_pk_mul_f32 v[36:37], v[156:157], v[156:157]\n"
    "v_pk_mul_f32 v[38:39], v[158:159], v[158:159]\n"
    "buffer_load_dwordx4 v25, s[24:27], s42 offen offset:1024 lds\n"
    "v_pk_fma_f32 v[32:33], v[32:33], v[30:31], v[28:29] op_sel_hi:[1,0,0]\n"
    "v_pk_fma_f32 v[34:35], v[34:35], v[30:31], v[28:29] op_sel_hi:[1,0,0]\n"
    "v_pk_fma_f32 v[36:37], v[36:37], v[30:31], v[28:29] op_sel_hi:[1,0,0]\n"
    "v_pk_fma_f32 v[38:39], v[38:39], v[30:31], v[28:29] op_sel_hi:[1,0,0]\n"
    "v_pk_mul_f32 v[32:33], v[32:33], v[152:153]\n"
    "v_pk_mul_f32 v[34:35], v[34:35], v[154:155]\n"
    "v_pk_mul_f32 v[36:37], v[36:37], v[156:157]\n"
    "v_pk_mul_f32 v[38:39], v[38:39], v[158:159]\n"
    "v_exp_f32 v32, v32\n"
    "v_exp_f32 v33, v33\n"
    "v_exp_f32 v34, v34\n"
    "s_add_u32 m0, s62, 81920\n"
    "s_mul_i32 s42, s51, 112\n"
    "buffer_load_dwordx4 v24, s[24:27], s42 offen offset:0 lds\n"
    "v_exp_f32 v35, v35\n"
    "v_exp_f32 v36, v36\n"
    "v_exp_f32 v37, v37\n"
    "v_exp_f32 v38, v38\n"
    "v_exp_f32 v39, v39\n"
    "v_pk_fma_f32 v[32:33], v[32:33], v[252:253], v[252:253] op_sel_hi:[1,0,0]\n"
    "v_pk_fma_f32 v[34:35], v[34:35], v[252:253], v[252:253] op_sel_hi:[1,0,0]\n"
    "v_pk_fma_f32 v[36:37], v[36:37], v[252:253], v[252:253] op_sel_hi:[1,0,0]\n"
    "v_pk_fma_f32 v[38:39], v[38:39], v[252:253], v[252:253] op_sel_hi:[1,0,0]\n"
    "v_rcp_f32 v32, v32\n"
    "v_rcp_f32 v33, v33\n"
    "buffer_load_dwordx4 v25, s[24:27], s42 offen offset:1024 lds\n"
    "v_rcp_f32 v34, v34\n"
    "v_rcp_f32 v35, v35\n"
    "v_rcp_f32 v36, v36\n"
    "v_rcp_f32 v37, v37\n"
    "v_rcp_f32 v38, v38\n"
    "v_rcp_f32 v39, v39\n"
    "v_pk_mul_f32 v[32:33], v[32:33], v[152:153]\n"
    "v_pk_mul_f32 v[34:35], v[34:35], v[154:155]\n"
    "v_pk_mul_f32 v[36:37], v[36:37], v[156:157]\n"
    "v_pk_mul_f32 v[38:39], v[38:39], v[158:159]\n"
    "v_cvt_pk_bf16_f32 v148, v32, v33\n"
    "v_cvt_pk_bf16_f32 v149, v34, v35\n"
    "v_cvt_pk_bf16_f32 v150, v36, v37\n"
    "v_cvt_pk_bf16_f32 v151, v38, v39\n"
    "buffer_store_dwordx4 v[144:147], v26, s[28:31], s66 offen sc1\n"
    "s_add_u32 s66, s66, 917504\n"
    "buffer_store_dwordx4 v[148:151], v26, s[28:31], s66 offen sc1\n"
    "s_add_u32 s66, s66, 917504\n"
    "v_pk_mul_f32 v[32:33], v[160:161], v[160:161]\n"
    "v_pk_mul_f32 v[34:35], v[162:163], v[162:163]\n"
    "v_pk_mul_f32 v[36:37], v[164:165], v[164:165]\n"
    "v_pk_mul_f32 v[38:39], v[166:167], v[166:167]\n"
    "v_pk_fma_f32 v[32:33], v[32:33], v[30:31], v[28:29] op_sel_hi:[1,0,0]\n"
    "v_pk_fma_f32 v[34:35], v[34:35], v[30:31], v[28:29] op_sel_hi:[1,0,0]\n"
    "v_pk_fma_f32 v[36:37], v[36:37], v[30:31], v[28:29] op_sel_hi:[1,0,0]\n"
    "v_pk_fma_f32 v[38:39], v[38:39], v[30:31], v[28:29] op_sel_hi:[1,0,0]\n"
    "v_pk_mul_f32 v[32:33], v[32:33], v[160:161]\n"
    "v_pk_mul_f32 v[34:35], v[34:35], v[162:163]\n"
    "v_pk_mul_f32 v[36:37], v[36:37], v[164:165]\n"
    "v_pk_mul_f32 v[38:39], v[38:39], v[166:167]\n"
    "v_exp_f32 v32, v32\n"
    "v_exp_f32 v33, v33\n"
    "v_exp_f32 v34, v34\n"
    "v_exp_f32 v35, v35\n"
    "v_exp_f32 v36, v36\n"
    "v_exp_f32 v37, v37\n"
    "v_exp_f32 v38, v38\n"
    "v_exp_f32 v39, v39\n"
    "v_pk_fma_f32 v[32:33], v[32:33], v[252:253], v[252:253] op_sel_hi:[1,0,0]\n"
    "v_pk_fma_f32 v[34:35], v[34:35], v[252:253], v[252:253] op_sel_hi:[1,0,0]\n"
    "v_pk_fma_f32 v[36:37], v[36:37], v[252:253], v[252:253] op_sel_hi:[1,0,0]\n"
    "v_pk_fma_f32 v[38:39], v[38:39], v[252:253], v[252:253] op_sel_hi:[1,0,0]\n"
    "v_rcp_f32 v32, v32\n"
    "v_rcp_f32 v33, v33\n"
    "s_add_u32 m0, s62, 83968\n"
    "s_mul_i32 s42, s52, 112\n"
    "buffer_load_dwordx4 v24, s[24:27], s42 offen offset:0 lds\n"
    "v_rcp_f32 v34, v34\n"
    "v_rcp_f32 v35, v35\n"
    "v_rcp_f32 v36, v36\n"
    "v_rcp_f32 v37, v37\n"
    "v_rcp_f32 v38, v38\n"
    "v_rcp_f32 v39, v39\n"
    "v_pk_mul_f32 v[32:33], v[32:33], v[160:161]\n"
    "v_pk_mul_f32 v[34:35], v[34:35], v[162:163]\n"
    "v_pk_mul_f32 v[36:37], v[36:37], v[164:165]\n"
    "v_pk_mul_f32 v[38:39], v[38:39], v[166:167]\n"
    "v_cvt_pk_bf16_f32 v160, v32, v33\n"
    "v_cvt_pk_bf16_f32 v161, v34, v35\n"
    "v_cvt_pk_bf16_f32 v162, v36, v37\n"
    "v_cvt_pk_bf16_f32 v163, v38, v39\n"
    "v_pk_mul_f32 v[32:33], v[168:169], v[168:169]\n"
    "v_pk_mul_f32 v[34:35], v[170:171], v[170:171]\n"
    "v_pk_mul_f32 v[36:37], v[172:173], v[172:173]\n"
    "v_pk_mul_f32 v[38:39], v[174:175], v[174:175]\n"
    "v_pk_fma_f32 v[32:33], v[32:33], v[30:31], v[28:29] op_sel_hi:[1,0,0]\n"
    "v_pk_fma_f32 v[34:35], v[34:35], v[30:31], v[28:29] op_sel_hi:[1,0,0]\n"
    "v_pk_fma_f32 v[36:37], v[36:37], v[30:31], v[28:29] op_sel_hi:[1,0,0]\n"
    "v_pk_fma_f32 v[38:39], v[38:39], v[30:31], v[28:29] op_sel_hi:[1,0,0]\n"
    "v_pk_mul_f32 v[32:33], v[32:33], v[168:169]\n"
    "v_pk_mul_f32 v[34:35], v[34:35], v[170:171]\n"
    "v_pk_mul_f32 v[36:37], v[36:37], v[172:173]\n"
    "v_pk_mul_f32 v[38:39], v[38:39], v[174:175]\n"
    "buffer_load_dwordx4 v25, s[24:27], s42 offen offset:1024 lds\n"
    "v_exp_f32 v32, v32\n"
    "v_exp_f32 v33, v33\n"
    "v_exp_f32 v34, v34\n"
    "v_exp_f32 v35, v35\n"
    "v_exp_f32 v36, v36\n"
    "v_exp_f32 v37, v37\n"
    "v_exp_f32 v38, v38\n"
    "v_exp_f32 v39, v39\n"
    "v_pk_fma_f32 v[32:33], v[32:33], v[252:253], v[252:253] op_sel_hi:[1,0,0]\n"
    "v_pk_fma_f32 v[34:35], v[34:35], v[252:253], v[252:253] op_sel_hi:[1,0,0]\n"
    "v_pk_fma_f32 v[36:37], v[36:37], v[252:253], v[252:253] op_sel_hi:[1,0,0]\n"
    "v_pk_fma_f32 v[38:39], v[38:39], v[252:253], v[252:253] op_sel_hi:[1,0,0]\n"
    "v_rcp_f32 v32, v32\n"
    "v_rcp_f32 v33, v33\n"
    "v_rcp_f32 v34, v34\n"
    "v_rcp_f32 v35, v35\n"
    "v_rcp_f32 v36, v36\n"
    "v_rcp_f32 v37, v37\n"
    "v_rcp_f32 v38, v38\n"
    "v_rcp_f32 v39, v39\n"
    "v_pk_mul_f32 v[32:33], v[32:33], v[168:169]\n"
    "v_pk_mul_f32 v[34:35], v[34:35], v[170:171]\n"
    "v_pk_mul_f32 v[36:37], v[36:37], v[172:173]\n"
    "v_pk_mul_f32 v[38:39], v[38:39], v[174:175]\n"
    "v_cvt_pk_bf16_f32 v164, v32, v33\n"
    "v_cvt_pk_bf16_f32 v165, v34, v35\n"
    "v_cvt_pk_bf16_f32 v166, v36, v37\n"
    "v_cvt_pk_bf16_f32 v167, v38, v39\n"
    "buffer_store_dwordx4 v[160:163], v26, s[28:31], s66 offen sc1\n"
    "s_add_u32 s66, s66, 917504\n"
    "buffer_store_dwordx4 v[164:167], v26, s[28:31], s66 offen sc1\n"
    "s_add_u32 s66, s66, 917504\n"
    "v_pk_mul_f32 v[32:33], v[176:177], v[176:177]\n"
    "v_pk_mul_f32 v[34:35], v[178:179], v[178:179]\n"
    "v_pk_mul_f32 v[36:37], v[180:181], v[180:181]\n"
    "v_pk_mul_f32 v[38:39], v[182:183], v[182:183]\n"
    "v_pk_fma_f32 v[32:33], v[32:33], v[30:31], v[28:29] op_sel_hi:[1,0,0]\n"
    "v_pk_fma_f32 v[34:35], v[34:35], v[30:31], v[28:29] op_sel_hi:[1,0,0]\n"
    "v_pk_fma_f32 v[36:37], v[36:37], v[30:31], v[28:29] op_sel_hi:[1,0,0]\n"
    "v_pk_fma_f32 v[38:39], v[38:39], v[30:31], v[28:29] op_sel_hi:[1,0,0]\n"
    "v_pk_mul_f32 v[32:33], v[32:33], v[176:177]\n"
    "v_pk_mul_f32 v[34:35], v[34:35], v[178:179]\n"
    "v_pk_mul_f32 v[36:37], v[36:37], v[180:181]\n"
    "v_pk_mul_f32 v[38:39], v[38:39], v[182:183]\n"
    "v_exp_f32 v32, v32\n"
    "v_exp_f32 v33, v33\n"
    "v_exp_f32 v34, v34\n"
    "v_exp_f32 v35, v35\n"
    "v_exp_f32 v36, v36\n"
    "v_exp_f32 v37, v37\n"
    "v_exp_f32 v38, v38\n"
    "v_exp_f32 v39, v39\n"
    "v_pk_fma_f32 v[32:33], v[32:33], v[252:253], v[252:253] op_sel_hi:[1,0,0]\n"
    "v_pk_fma_f32 v[34:35], v[34:35], v[252:253], v[252:253] op_sel_hi:[1,0,0]\n"
    "v_pk_fma_f32 v[36:37], v[36:37], v[252:253], v[252:253] op_sel_hi:[1,0,0]\n"
    "v_pk_fma_f32 v[38:39], v[38:39], v[252:253], v[252:253] op_sel_hi:[1,0,0]\n"
    "v_rcp_f32 v32, v32\n"
    "v_rcp_f32 v33, v33\n"
    "v_rcp_f32 v34, v34\n"
    "v_rcp_f32 v35, v35\n"
    "v_rcp_f32 v36, v36\n"
    "v_rcp_f32 v37, v37\n"
    "v_rcp_f32 v38, v38\n"
    "v_rcp_f32 v39, v39\n"
    "v_pk_mul_f32 v[32:33], v[32:33], v[176:177]\n"
    "v_pk_mul_f32 v[34:35], v[34:35], v[178:179]\n"
    "v_pk_mul_f32 v[36:37], v[36:37], v[180:181]\n"
    "v_pk_mul_f32 v[38:39], v[38:39], v[182:183]\n"
    "v_cvt_pk_bf16_f32 v176, v32, v33\n"
    "v_cvt_pk_bf16_f32 v177, v34, v35\n"
    "v_cvt_pk_bf16_f32 v178, v36, v37\n"
    "v_cvt_pk_bf16_f32 v179, v38, v39\n"
    "v_pk_mul_f32 v[32:33], v[184:185], v[184:185]\n"
    "v_pk_mul_f32 v[34:35], v[186:187], v[186:187]\n"
    "v_pk_mul_f32 v[36:37], v[188:189], v[188:189]\n"
    "v_pk_mul_f32 v[38:39], v[190:191], v[190:191]\n"
    "v_pk_fma_f32 v[32:33], v[32:33], v[30:31], v[28:29] op_sel_hi:[1,0,0]\n"
    "v_pk_fma_f32 v[34:35], v[34:35], v[30:31], v[28:29] op_sel_hi:[1,0,0]\n"
    "v_pk_fma_f32 v[36:37], v[36:37], v[30:31], v[28:29] op_sel_hi:[1,0,0]\n"
    "v_pk_fma_f32 v[38:39], v[38:39], v[30:31], v[28:29] op_sel_hi:[1,0,0]\n"
    "v_pk_mul_f32 v[32:33], v[32:33], v[184:185]\n"
    "v_pk_mul_f32 v[34:35], v[34:35], v[186:187]\n"
    "v_pk_mul_f32 v[36:37], v[36:37], v[188:189]\n"
    "v_pk_mul_f32 v[38:39], v[38:39], v[190:191]\n"
    "v_exp_f32 v32, v32\n"
    "v_exp_f32 v33, v33\n"
    "v_exp_f32 v34, v34\n"
    "v_exp_f32 v35, v35\n"
    "v_exp_f32 v36, v36\n"
    "v_exp_f32 v37, v37\n"
    "v_exp_f32 v38, v38\n"
    "v_exp_f32 v39, v39\n"
    "v_pk_fma_f32 v[32:33], v[32:33], v[252:253], v[252:253] op_sel_hi:[1,0,0]\n"
    "v_pk_fma_f32 v[34:35], v[34:35], v[252:253], v[252:253] op_sel_hi:[1,0,0]\n"
    "v_pk_fma_f32 v[36:37], v[36:37], v[252:253], v[252:253] op_sel_hi:[1,0,0]\n"
    "v_pk_fma_f32 v[38:39], v[38:39], v[252:253], v[252:253] op_sel_hi:[1,0,0]\n"
    "v_rcp_f32 v32, v32\n"
    "v_rcp_f32 v33, v33\n"
    "v_rcp_f32 v34, v34\n"
    "v_rcp_f32 v35, v35\n"
    "v_rcp_f32 v36, v36\n"
    "v_rcp_f32 v37, v37\n"
    "v_rcp_f32 v38, v38\n"
    "v_rcp_f32 v39, v39\n"
    "v_pk_mul_f32 v[32:33], v[32:33], v[184:185]\n"
    "v_pk_mul_f32 v[34:35], v[34:35], v[186:187]\n"
    "v_pk_mul_f32 v[36:37], v[36:37], v[188:189]\n"
    "v_pk_mul_f32 v[38:39], v[38:39], v[190:191]\n"
    "v_cvt_pk_bf16_f32 v180, v32, v33\n"
    "v_cvt_pk_bf16_f32 v181, v34, v35\n"
    "v_cvt_pk_bf16_f32 v182, v36, v37\n"
    "v_cvt_pk_bf16_f32 v183, v38, v39\n"
    "buffer_store_dwordx4 v[176:179], v26, s[28:31], s66 offen sc1\n"
    "s_add_u32 s66, s66, 917504\n"
    "buffer_store_dwordx4 v[180:183], v26, s[28:31], s66 offen sc1\n"
    "s_add_u32 s66, s66, 917504\n"
    "v_pk_mul_f32 v[32:33], v[192:193], v[192:193]\n"
    "v_pk_mul_f32 v[34:35], v[194:195], v[194:195]\n"
    "v_pk_mul_f32 v[36:37], v[196:197], v[196:197]\n"
    "v_pk_mul_f32 v[38:39], v[198:199], v[198:199]\n"
    "v_pk_fma_f32 v[32:33], v[32:33], v[30:31], v[28:29] op_sel_hi:[1,0,0]\n"
    "v_pk_fma_f32 v[34:35], v[34:35], v[30:31], v[28:29] op_sel_hi:[1,0,0]\n"
    "v_pk_fma_f32 v[36:37], v[36:37], v[30:31], v[28:29] op_sel_hi:[1,0,0]\n"
    "v_pk_fma_f32 v[38:39], v[38:39], v[30:31], v[28:29] op_sel_hi:[1,0,0]\n"
    "v_pk_mul_f32 v[32:33], v[32:33], v[192:193]\n"
    "v_pk_mul_f32 v[34:35], v[34:35], v[194:195]\n"
    "v_pk_mul_f32 v[36:37], v[36:37], v[196:197]\n"
    "v_pk_mul_f32 v[38:39], v[38:39], v[198:199]\n"
    "v_exp_f32 v32, v32\n"
    "v_exp_f32 v33, v33\n"
    "v_exp_f32 v34, v34\n"
    "v_exp_f32 v35, v35\n"
    "v_exp_f32 v36, v36\n"
    "v_exp_f32 v37, v37\n"
    "v_exp_f32 v38, v38\n"
    "v_exp_f32 v39, v39\n"
    "v_pk_fma_f32 v[32:33], v[32:33], v[252:253], v[252:253] op_sel_hi:[1,0,0]\n"
    "v_pk_fma_f32 v[34:35], v[34:35], v[252:253], v[252:253] op_sel_hi:[1,0,0]\n"
    "v_pk_fma_f32 v[36:37], v[36:37], v[252:253], v[252:253] op_sel_hi:[1,0,0]\n"
    "v_pk_fma_f32 v[38:39], v[38:39], v[252:253], v[252:253] op_sel_hi:[1,0,0]\n"
    "v_rcp_f32 v32, v32\n"
    "v_rcp_f32 v33, v33\n"
    "v_rcp_f32 v34, v34\n"
    "v_rcp_f32 v35, v35\n"
    "v_rcp_f32 v36, v36\n"
    "v_rcp_f32 v37, v37\n"
    "v_rcp_f32 v38, v38\n"
    "v_rcp_f32 v39, v39\n"
    "v_pk_mul_f32 v[32:33], v[32:33], v[192:193]\n"
    "v_pk_mul_f32 v[34:35], v[34:35], v[194:195]\n"
    "v_pk_mul_f32 v[36:37], v[36:37], v[196:197]\n"
    "v_pk_mul_f32 v[38:39], v[38:39], v[198:199]\n"
    "v_cvt_pk_bf16_f32 v192, v32, v33\n"
    "v_cvt_pk_bf16_f32 v193, v34, v35\n"
    "v_cvt_pk_bf16_f32 v194, v36, v37\n"
    "v_cvt_pk_bf16_f32 v195, v38, v39\n"
    "v_pk_mul_f32 v[32:33], v[200:201], v[200:201]\n"
    "v_pk_mul_f32 v[34:35], v[202:203], v[202:203]\n"
    "v_pk_mul_f32 v[36:37], v[204:205], v[204:205]\n"
    "v_pk_mul_f32 v[38:39], v[206:207], v[206:207]\n"
    "v_pk_fma_f32 v[32:33], v[32:33], v[30:31], v[28:29] op_sel_hi:[1,0,0]\n"
    "v_pk_fma_f32 v[34:35], v[34:35], v[30:31], v[28:29] op_sel_hi:[1,0,0]\n"
    "v_pk_fma_f32 v[36:37], v[36:37], v[30:31], v[28:29] op_sel_hi:[1,0,0]\n"
    "v_pk_fma_f32 v[38:39], v[38:39], v[30:31], v[28:29] op_sel_hi:[1,0,0]\n"
    "v_pk_mul_f32 v[32:33], v[32:33], v[200:201]\n"
    "v_pk_mul_f32 v[34:35], v[34:35], v[202:203]\n"
    "v_pk_mul_f32 v[36:37], v[36:37], v[204:205]\n"
    "v_pk_mul_f32 v[38:39], v[38:39], v[206:207]\n"
    "v_exp_f32 v32, v32\n"
    "v_exp_f32 v33, v33\n"
    "v_exp_f32 v34, v34\n"
    "v_exp_f32 v35, v35\n"
    "v_exp_f32 v36, v36\n"
    "v_exp_f32 v37, v37\n"
    "v_exp_f32 v38, v38\n"
    "v_exp_f32 v39, v39\n"
    "v_pk_fma_f32 v[32:33], v[32:33], v[252:253], v[252:253] op_sel_hi:[1,0,0]\n"
    "v_pk_fma_f32 v[34:35], v[34:35], v[252:253], v[252:253] op_sel_hi:[1,0,0]\n"
    "v_pk_fma_f32 v[36:37], v[36:37], v[252:253], v[252:253] op_sel_hi:[1,0,0]\n"
    "v_pk_fma_f32 v[38:39], v[38:39], v[252:253], v[252:253] op_sel_hi:[1,0,0]\n"
    "v_rcp_f32 v32, v32\n"
    "v_rcp_f32 v33, v33\n"
    "v_rcp_f32 v34, v34\n"
    "v_rcp_f32 v35, v35\n"
    "v_rcp_f32 v36, v36\n"
    "v_rcp_f32 v37, v37\n"
    "v_rcp_f32 v38, v38\n"
    "v_rcp_f32 v39, v39\n"
    "v_pk_mul_f32 v[32:33], v[32:33], v[200:201]\n"
    "v_pk_mul_f32 v[34:35], v[34:35], v[202:203]\n"
    "v_pk_mul_f32 v[36:37], v[36:37], v[204:205]\n"
    "v_pk_mul_f32 v[38:39], v[38:39], v[206:207]\n"
    "v_mov_b32 v31, s64\n"
    "v_mov_b32 v255, 0\n"
    "ds_write_b32 v31, v255 offset:16\n"
    "v_cvt_pk_bf16_f32 v196, v32, v33\n"
    "v_cvt_pk_bf16_f32 v197, v34, v35\n"
    "v_cvt_pk_bf16_f32 v198, v36, v37\n"
    "v_cvt_pk_bf16_f32 v199, v38, v39\n"
    "buffer_store_dwordx4 v[192:195], v26, s[28:31], s66 offen sc1\n"
    "s_add_u32 s66, s66, 917504\n"
    "buffer_store_dwordx4 v[196:199], v26, s[28:31], s66 offen sc1\n"
    "s_add_u32 s66, s66, 917504\n"
    "s_add_u32 m0, s62, 98304\n"
    "s_mul_i32 s42, s53, 112\n"
    "buffer_load_dwordx4 v24, s[24:27], s42 offen offset:0 lds\n"
    "buffer_load_dwordx4 v25, s[24:27], s42 offen offset:1024 lds\n"
    "s_add_u32 m0, s62, 100352\n"
    "s_mul_i32 s42, s54, 112\n"
    "buffer_load_dwordx4 v24, s[24:27], s42 offen offset:0 lds\n"
    "buffer_load_dwordx4 v25, s[24:27], s42 offen offset:1024 lds\n"
    "v_bfe_u32 v8, v254, 4, 1\n"
    "v_and_b32 v9, 15, v254\n"
    "v_lshlrev_b32 v8, 14, v8\n"
    "v_lshl_add_u32 v8, v9, 8, v8\n"
    "v_add_u32 v8, s62, v8\n"
    "v_lshrrev_b32 v10, 5, v254\n"
    "v_and_b32 v9, 7, v9\n"
    "v_xor_b32 v9, v9, v10\n"
    "v_lshl_add_u32 v4, v9, 4, v8\n"
    "v_xor_b32 v5, 32, v4\n"
    "v_xor_b32 v6, 64, v4\n"
    "v_xor_b32 v7, 96, v4\n"
    "ds_read_b64_tr_b16 v[68:69], v248 offset:0\n"
    "ds_read_b64_tr_b16 v[70:71], v248 offset:512\n"
    "ds_read_b64_tr_b16 v[72:73], v248 offset:1024\n"
    "ds_read_b64_tr_b16 v[74:75], v248 offset:1536\n"
    "s_waitcnt vmcnt(32)\n"
    "ds_read_b64_tr_b16 v[76:77], v248 offset:2048\n"
    "ds_read_b64_tr_b16 v[78:79], v248 offset:2560\n"
    "ds_read_b64_tr_b16 v[80:81], v248 offset:3072\n"
    "ds_read_b64_tr_b16 v[82:83], v248 offset:3584\n"
    "s_waitcnt vmcnt(30)\n"
    "ds_read_b64_tr_b16 v[84:85], v248 offset:16384\n"
    "ds_read_b64_tr_b16 v[86:87], v248 offset:16896\n"
    "ds_read_b64_tr_b16 v[88:89], v248 offset:17408\n"
    "ds_read_b64_tr_b16 v[90:91], v248 offset:17920\n"
    "s_waitcnt vmcnt(28)\n"
    "ds_read_b64_tr_b16 v[92:93], v248 offset:18432\n"
    "ds_read_b64_tr_b16 v[94:95], v248 offset:18944\n"
    "ds_read_b64_tr_b16 v[96:97], v248 offset:19456\n"
    "ds_read_b64_tr_b16 v[98:99], v248 offset:19968\n"
    "s_waitcnt vmcnt(26)\n"
    "ds_read_b64_tr_b16 v[100:101], v248 offset:32768\n"
    "ds_read_b64_tr_b16 v[102:103], v248 offset:33280\n"
    "ds_read_b64_tr_b16 v[104:105], v248 offset:33792\n"
    "ds_read_b64_tr_b16 v[106:107], v248 offset:34304\n"
    "s_waitcnt vmcnt(24)\n"
    "ds_read_b64_tr_b16 v[108:109], v248 offset:34816\n"
    "ds_read_b64_tr_b16 v[110:111], v248 offset:35328\n"
    "ds_read_b64_tr_b16 v[112:113], v248 offset:35840\n"
    "ds_read_b64_tr_b16 v[114:115], v248 offset:36352\n"
    "s_waitcnt vmcnt(22)\n"
    "ds_read_b64_tr_b16 v[116:117], v248 offset:49152\n"
    "ds_read_b64_tr_b16 v[118:119], v248 offset:49664\n"
    "ds_read_b64_tr_b16 v[120:121], v248 offset:50176\n"
    "ds_read_b64_tr_b16 v[122:123], v248 offset:50688\n"
    "s_waitcnt vmcnt(20)\n"
    "ds_read_b64_tr_b16 v[124:125], v248 offset:51200\n"
    "ds_read_b64_tr_b16 v[126:127], v248 offset:51712\n"
    "ds_read_b64_tr_b16 v[128:129], v248 offset:52224\n"
    "ds_read_b64_tr_b16 v[130:131], v248 offset:52736\n"
    "s_waitcnt vmcnt(8)\n"
    "buffer_store_dwordx4 v[240:243], v27, s[28:31], s67 offen sc1\n"
    "s_add_u32 s67, s67, 114688\n"
    "s_waitcnt vmcnt(5)\n"
    "buffer_store_dwordx4 v[240:243], v27, s[28:31], s67 offen sc1\n"
    "s_add_u32 s67, s67, 114688\n"
    "ds_read_b64_tr_b16 v[132:133], v249 offset:0\n"
    "ds_read_b64_tr_b16 v[134:135], v249 offset:512\n"
    "ds_read_b64_tr_b16 v[136:137], v249 offset:1024\n"
    "ds_read_b64_tr_b16 v[138:139], v249 offset:1536\n"
    "ds_read_b64_tr_b16 v[140:141], v249 offset:2048\n"
    "ds_read_b64_tr_b16 v[142:143], v249 offset:2560\n"
    "ds_read_b64_tr_b16 v[32:33], v249 offset:3072\n"
    "ds_read_b64_tr_b16 v[34:35], v249 offset:3584\n"
    "ds_read_b64_tr_b16 v[36:37], v249 offset:16384\n"
    "ds_read_b64_tr_b16 v[38:39], v249 offset:16896\n"
    "ds_read_b64_tr_b16 v[40:41], v249 offset:17408\n"
    "ds_read_b64_tr_b16 v[42:43], v249 offset:17920\n"
    "ds_read_b64_tr_b16 v[44:45], v249 offset:18432\n"
    "ds_read_b64_tr_b16 v[46:47], v249 offset:18944\n"
    "ds_read_b64_tr_b16 v[48:49], v249 offset:19456\n"
    "ds_read_b64_tr_b16 v[50:51], v249 offset:19968\n"
    "s_waitcnt vmcnt(4)\n"
    "ds_read_b64_tr_b16 v[52:53], v249 offset:32768\n"
    "ds_read_b64_tr_b16 v[54:55], v249 offset:33280\n"
    "ds_read_b64_tr_b16 v[56:57], v249 offset:33792\n"
    "ds_read_b64_tr_b16 v[58:59], v249 offset:34304\n"
    "s_waitcnt lgkmcnt(15)\n"
    "s_add_u32 m0, s62, 0\n"
    "s_mul_i32 s42, s48, 112\n"
    "v_add_u32 v24, 28672, v24\n"
    "v_add_u32 v25, 28672, v25\n"
    "buffer_load_dwordx4 v24, s[24:27], s42 offen offset:0 lds\n"
    "buffer_load_dwordx4 v25, s[24:27], s42 offen offset:1024 lds\n"
    "s_add_u32 m0, s62, 2048\n"
    "s_mul_i32 s42, s49, 112\n"
    "buffer_load_dwordx4 v24, s[24:27], s42 offen offset:0 lds\n"
    "buffer_load_dwordx4 v25, s[24:27], s42 offen offset:1024 lds\n"
    "s_add_u32 m0, s62, 16384\n"
    "s_mul_i32 s42, s50, 112\n"
    "buffer_load_dwordx4 v24, s[24:27], s42 offen offset:0 lds\n"
    "buffer_load_dwordx4 v25, s[24:27], s42 offen offset:1024 lds\n"
    "s_add_u32 m0, s62, 18432\n"
    "s_mul_i32 s42, s51, 112\n"
    "s_waitcnt vmcnt(9)\n"
    "buffer_load_dwordx4 v24, s[24:27], s42 offen offset:0 lds\n"
    "s_waitcnt vmcnt(9)\n"
    "buffer_load_dwordx4 v25, s[24:27], s42 offen offset:1024 lds\n"
    "s_add_u32 m0, s62, 32768\n"
    "s_mul_i32 s42, s52, 112\n"
    "s_waitcnt vmcnt(9)\n"
    "buffer_load_dwordx4 v24, s[24:27], s42 offen offset:0 lds\n"
    "s_waitcnt vmcnt(9)\n"
    "buffer_load_dwordx4 v25, s[24:27], s42 offen offset:1024 lds\n"
    "s_add_u32 m0, s62, 34816\n"
    "s_mul_i32 s42, s53, 112\n"
    "s_waitcnt vmcnt(9)\n"
    "buffer_load_dwordx4 v24, s[24:27], s42 offen offset:0 lds\n"
    "s_waitcnt vmcnt(9)\n"
    "buffer_load_dwordx4 v25, s[24:27], s42 offen offset:1024 lds\n"
    "s_waitcnt vmcnt(9)\n"
    "buffer_load_dwordx2 v[244:245], v3, s[28:31], s68 offen sc1\n"
    "s_add_u32 m0, s62, 49152\n"
    "s_mul_i32 s42, s54, 112\n"
    "s_waitcnt vmcnt(9)\n"
    "buffer_load_dwordx4 v24, s[24:27], s42 offen offset:0 lds\n"
    "s_waitcnt vmcnt(9)\n"
    "buffer_load_dwordx4 v25, s[24:27], s42 offen offset:1024 lds\n"
    "s_add_u32 m0, s62, 51200\n"
    "s_mul_i32 s42, s48, 112\n"
    "v_add_u32 v24, 28672, v24\n"
    "v_add_u32 v25, 28672, v25\n"
    "s_waitcnt vmcnt(9)\n"
    "buffer_load_dwordx4 v24, s[24:27], s42 offen offset:0 lds\n"
    "s_waitcnt vmcnt(9)\n"
    "buffer_load_dwordx4 v25, s[24:27], s42 offen offset:1024 lds\n"
    "s_add_u32 m0, s62, 65536\n"
    "s_mul_i32 s42, s49, 112\n"
    "s_waitcnt vmcnt(9)\n"
    "buffer_load_dwordx4 v24, s[24:27], s42 offen offset:0 lds\n"
    "s_waitcnt vmcnt(9)\n"
    "buffer_load_dwordx4 v25, s[24:27], s42 offen offset:1024 lds\n"
    "s_waitcnt lgkmcnt(12)\n"
    "s_add_u32 m0, s62, 67584\n"
    "s_mul_i32 s42, s50, 112\n"
    "s_waitcnt vmcnt(9)\n"
    "buffer_load_dwordx4 v24, s[24:27], s42 offen offset:0 lds\n"
    "s_waitcnt vmcnt(9)\n"
    "buffer_load_dwordx4 v25, s[24:27], s42 offen offset:1024 lds\n"
    "s_waitcnt lgkmcnt(8)\n"
    "s_add_u32 m0, s62, 81920\n"
    "s_mul_i32 s42, s51, 112\n"
    "s_waitcnt vmcnt(9)\n"
    "buffer_load_dwordx4 v24, s[24:27], s42 offen offset:0 lds\n"
    "s_waitcnt vmcnt(9)\n"
    "buffer_load_dwordx4 v25, s[24:27], s42 offen offset:1024 lds\n"
    "v_cmp_ne_u64 vcc, s[32:33], v[244:245]\n"
    "s_cbranch_vccnz .La46_%=\n"
    ".La47_%=:\n"
    "s_add_u32 s68, s68, 114688\n"
    "s_waitcnt vmcnt(9)\n"
    "buffer_load_dwordx2 v[244:245], v3, s[28:31], s68 offen sc1\n"
    "s_waitcnt vmcnt(9)\n"
    "buffer_load_dwordx4 v[60:63], v250, s[28:31], s48 offen sc1\n"
    "s_waitcnt vmcnt(9)\n"
    "buffer_load_dwordx4 v[64:67], v251, s[28:31], s48 offen sc1\n"
    "s_waitcnt vmcnt(9)\n"
    "buffer_load_dwordx4 v[208:211], v250, s[28:31], s49 offen sc1\n"
    "s_waitcnt vmcnt(9)\n"
    "buffer_load_dwordx4 v[212:215], v251, s[28:31], s49 offen sc1\n"
    "s_waitcnt vmcnt(9)\n"
    "buffer_load_dwordx4 v[216:219], v250, s[28:31], s50 offen sc1\n"
    "s_waitcnt vmcnt(9)\n"
    "buffer_load_dwordx4 v[220:223], v251, s[28:31], s50 offen sc1\n"
    "s_waitcnt vmcnt(9)\n"
    "buffer_load_dwordx4 v[224:227], v250, s[28:31], s51 offen sc1\n"
    "s_waitcnt vmcnt(9)\n"
    "buffer_load_dwordx4 v[228:231], v251, s[28:31], s51 offen sc1\n"
    "s_waitcnt vmcnt(7)\n"
    "v_mfma_f32_32x32x16_bf16 v[144:159], v[68:71], v[60:63], 0\n"
    "v_mfma_f32_32x32x16_bf16 v[176:191], v[72:75], v[60:63], 0\n"
    "buffer_load_dwordx4 v[232:235], v250, s[28:31], s52 offen sc1\n"
    "s_waitcnt vmcnt(7)\n"
    "v_mfma_f32_32x32x16_bf16 v[160:175], v[68:71], v[64:67], 0\n"
    "v_mfma_f32_32x32x16_bf16 v[192:207], v[72:75], v[64:67], 0\n"
    "buffer_load_dwordx4 v[236:239], v251, s[28:31], s52 offen sc1\n"
    "s_waitcnt vmcnt(7)\n"
    "v_mfma_f32_32x32x16_bf16 v[144:159], v[76:79], v[208:211], v[144:159]\n"
    "buffer_load_dwordx4 v[60:63], v250, s[28:31], s53 offen sc1\n"
    "v_mfma_f32_32x32x16_bf16 v[176:191], v[80:83], v[208:211], v[176:191]\n"
    "s_waitcnt vmcnt(7)\n"
    "v_mfma_f32_32x32x16_bf16 v[160:175], v[76:79], v[212:215], v[160:175]\n"
    "buffer_load_dwordx4 v[64:67], v251, s[28:31], s53 offen sc1\n"
    "v_mfma_f32_32x32x16_bf16 v[192:207], v[80:83], v[212:215], v[192:207]\n"
    "s_waitcnt vmcnt(7)\n"
    "v_mfma_f32_32x32x16_bf16 v[144:159], v[84:87], v[216:219], v[144:159]\n"
    "buffer_load_dwordx4 v[68:71], v250, s[28:31], s54 offen sc1\n"
    "v_mfma_f32_32x32x16_bf16 v[176:191], v[88:91], v[216:219], v[176:191]\n"
    "s_waitcnt vmcnt(7)\n"
    "v_mfma_f32_32x32x16_bf16 v[160:175], v[84:87], v[220:223], v[160:175]\n"
    "buffer_load_dwordx4 v[72:75], v251, s[28:31], s54 offen sc1\n"
    "v_mfma_f32_32x32x16_bf16 v[192:207], v[88:91], v[220:223], v[192:207]\n"
    "v_cmp_ne_u64 vcc, s[32:33], v[244:245]\n"
    "s_cbranch_vccnz .La48_%=\n"
    ".La49_%=:\n"
    "s_add_u32 s68, s68, 114688\n"
    "s_waitcnt vmcnt(7)\n"
    "v_mfma_f32_32x32x16_bf16 v[144:159], v[92:95], v[224:227], v[144:159]\n"
    "v_add_u32 v250, 1835008, v250\n"
    "buffer_load_dwordx4 v[208:211], v250, s[28:31], s48 offen sc1\n"
    "v_mfma_f32_32x32x16_bf16 v[176:191], v[96:99], v[224:227], v[176:191]\n"
    "s_waitcnt vmcnt(7)\n"
    "v_mfma_f32_32x32x16_bf16 v[160:175], v[92:95], v[228:231], v[160:175]\n"
    "buffer_load_dwordx4 v[212:215], v250, s[28:31], s49 offen sc1\n"
    "v_mfma_f32_32x32x16_bf16 v[192:207], v[96:99], v[228:231], v[192:207]\n"
    "s_waitcnt vmcnt(7)\n"
    "v_mfma_f32_32x32x16_bf16 v[144:159], v[100:103], v[232:235], v[144:159]\n"
    "v_add_u32 v251, 1835008, v251\n"
    "buffer_load_dwordx4 v[76:79], v251, s[28:31], s48 offen sc1\n"
    "v_mfma_f32_32x32x16_bf16 v[176:191], v[104:107], v[232:235], v[176:191]\n"
    "s_waitcnt vmcnt(7)\n"
    "v_mfma_f32_32x32x16_bf16 v[160:175], v[100:103], v[236:239], v[160:175]\n"
    "buffer_load_dwordx4 v[80:83], v250, s[28:31], s50 offen sc1\n"
    "v_mfma_f32_32x32x16_bf16 v[192:207], v[104:107], v[236:239], v[192:207]\n"
    "s_waitcnt vmcnt(7)\n"
    "v_mfma_f32_32x32x16_bf16 v[144:159], v[108:111], v[60:63], v[144:159]\n"
    "buffer_load_dwordx4 v[216:219], v250, s[28:31], s51 offen sc1\n"
    "v_mfma_f32_32x32x16_bf16 v[176:191], v[112:115], v[60:63], v[176:191]\n"
    "s_waitcnt vmcnt(7)\n"
    "v_mfma_f32_32x32x16_bf16 v[160:175], v[108:111], v[64:67], v[160:175]\n"
    "buffer_load_dwordx4 v[220:223], v251, s[28:31], s49 offen sc1\n"
    "v_mfma_f32_32x32x16_bf16 v[192:207], v[112:115], v[64:67], v[192:207]\n"
    "s_waitcnt vmcnt(7)\n"
    "v_mfma_f32_32x32x16_bf16 v[144:159], v[116:119], v[68:71], v[144:159]\n"
    "buffer_load_dwordx4 v[84:87], v250, s[28:31], s52 offen sc1\n"
    "v_mfma_f32_32x32x16_bf16 v[176:191], v[120:123], v[68:71], v[176:191]\n"
    "v_mov_b32 v31, s64\n"
    "v_mov_b32 v255, 0\n"
    "ds_write_b32 v31, v255 offset:12\n"
    "s_waitcnt vmcnt(7)\n"
    "v_mfma_f32_32x32x16_bf16 v[160:175], v[116:119], v[72:75], v[160:175]\n"
    "buffer_load_dwordx4 v[88:91], v250, s[28:31], s53 offen sc1\n"
    "v_mfma_f32_32x32x16_bf16 v[192:207], v[120:123], v[72:75], v[192:207]\n"
    "s_waitcnt vmcnt(7)\n"
    "v_mfma_f32_32x32x16_bf16 v[144:159], v[124:127], v[208:211], v[144:159]\n"
    "buffer_load_dwordx4 v[224:227], v251, s[28:31], s50 offen sc1\n"
    "v_mfma_f32_32x32x16_bf16 v[176:191], v[128:131], v[208:211], v[176:191]\n"
    "s_waitcnt vmcnt(7)\n"
    "v_mfma_f32_32x32x16_bf16 v[144:159], v[132:135], v[212:215], v[144:159]\n"
    "buffer_load_dwordx4 v[228:231], v250, s[28:31], s54 offen sc1\n"
    "v_mfma_f32_32x32x16_bf16 v[176:191], v[136:139], v[212:215], v[176:191]\n"
    "s_waitcnt vmcnt(7)\n"
    "v_mfma_f32_32x32x16_bf16 v[160:175], v[124:127], v[76:79], v[160:175]\n"
    "v_add_u32 v250, 1835008, v250\n"
    "buffer_load_dwordx4 v[92:95], v250, s[28:31], s48 offen sc1\n"
    "v_mfma_f32_32x32x16_bf16 v[192:207], v[128:131], v[76:79], v[192:207]\n"
    "s_waitcnt vmcnt(7)\n"
    "v_mfma_f32_32x32x16_bf16 v[144:159], v[140:143], v[80:83], v[144:159]\n"
    "buffer_load_dwordx4 v[96:99], v251, s[28:31], s51 offen sc1\n"
    "v_mfma_f32_32x32x16_bf16 v[176:191], v[32:35], v[80:83], v[176:191]\n"
    "s_waitcnt vmcnt(7)\n"
    "v_mfma_f32_32x32x16_bf16 v[144:159], v[36:39], v[216:219], v[144:159]\n"
    "buffer_load_dwordx4 v[232:235], v250, s[28:31], s49 offen sc1\n"
    "v_mfma_f32_32x32x16_bf16 v[176:191], v[40:43], v[216:219], v[176:191]\n"
    "s_waitcnt vmcnt(7)\n"
    "v_mfma_f32_32x32x16_bf16 v[160:175], v[132:135], v[220:223], v[160:175]\n"
    "ds_read_b64_tr_b16 v[236:237], v249 offset:34816\n"
    "ds_read_b64_tr_b16 v[238:239], v249 offset:35328\n"
    "buffer_load_dwordx4 v[100:103], v250, s[28:31], s50 offen sc1\n"
    "v_mfma_f32_32x32x16_bf16 v[192:207], v[136:139], v[220:223], v[192:207]\n"
    "ds_read_b64_tr_b16 v[104:105], v249 offset:35840\n"
    "ds_read_b64_tr_b16 v[106:107], v249 offset:36352\n"
    "s_waitcnt vmcnt(7) lgkmcnt(9)\n"
    "v_mfma_f32_32x32x16_bf16 v[144:159], v[44:47], v[84:87], v[144:159]\n"
    "ds_read_b64_tr_b16 v[60:61], v248 offset:0\n"
    "ds_read_b64_tr_b16 v[62:63], v248 offset:512\n"
    "buffer_load_dwordx4 v[64:67], v251, s[28:31], s52 offen sc1\n"
    "v_mfma_f32_32x32x16_bf16 v[176:191], v[48:51], v[84:87], v[176:191]\n"
    "ds_read_b64_tr_b16 v[108:109], v248 offset:1024\n"
    "ds_read_b64_tr_b16 v[110:111], v248 offset:1536\n"
    "s_add_u32 m0, s62, 83968\n"
    "s_mul_i32 s42, s52, 112\n"
    "buffer_load_dwordx4 v24, s[24:27], s42 offen offset:0 lds\n"
    "s_waitcnt vmcnt(8) lgkmcnt(9)\n"
    "v_mfma_f32_32x32x16_bf16 v[144:159], v[52:55], v[88:91], v[144:159]\n"
    "buffer_load_dwordx4 v[112:115], v250, s[28:31], s51 offen sc1\n"
    "v_mfma_f32_32x32x16_bf16 v[176:191], v[56:59], v[88:91], v[176:191]\n"
    "buffer_load_dwordx4 v25, s[24:27], s42 offen offset:1024 lds\n"
    "s_waitcnt vmcnt(9)\n"
    "v_mfma_f32_32x32x16_bf16 v[160:175], v[140:143], v[224:227], v[160:175]\n"
    "ds_read_b64_tr_b16 v[68:69], v248 offset:2048\n"
    "ds_read_b64_tr_b16 v[70:71], v248 offset:2560\n"
    "buffer_load_dwordx4 v[72:75], v250, s[28:31], s52 offen sc1\n"
    "v_mfma_f32_32x32x16_bf16 v[192:207], v[32:35], v[224:227], v[192:207]\n"
    "ds_read_b64_tr_b16 v[116:117], v248 offset:3072\n"
    "ds_read_b64_tr_b16 v[118:119], v248 offset:3584\n"
    "s_add_u32 m0, s62, 98304\n"
    "s_mul_i32 s42, s53, 112\n"
    "s_waitcnt vmcnt(9)\n"
    "buffer_load_dwordx4 v24, s[24:27], s42 offen offset:0 lds\n"
    "s_waitcnt lgkmcnt(8)\n"
    "v_mfma_f32_32x32x16_bf16 v[144:159], v[236:239], v[228:231], v[144:159]\n"
    "ds_read_b64_tr_b16 v[120:121], v248 offset:16384\n"
    "ds_read_b64_tr_b16 v[122:123], v248 offset:16896\n"
    "s_waitcnt vmcnt(9)\n"
    "buffer_load_dwordx4 v[208:211], v251, s[28:31], s53 offen sc1\n"
    "v_mfma_f32_32x32x16_bf16 v[176:191], v[104:107], v[228:231], v[176:191]\n"
    "ds_read_b64_tr_b16 v[212:213], v248 offset:17408\n"
    "ds_read_b64_tr_b16 v[214:215], v248 offset:17920\n"
    "s_waitcnt vmcnt(9)\n"
    "buffer_load_dwordx4 v25, s[24:27], s42 offen offset:1024 lds\n"
    "s_waitcnt lgkmcnt(8)\n"
    "v_mfma_f32_32x32x16_bf16 v[144:159], v[60:63], v[92:95], v[144:159]\n"
    "s_waitcnt vmcnt(9)\n"
    "buffer_load_dwordx4 v[76:79], v250, s[28:31], s53 offen sc1\n"
    "v_mfma_f32_32x32x16_bf16 v[176:191], v[108:111], v[92:95], v[176:191]\n"
    "s_add_u32 m0, s62, 100352\n"
    "s_mul_i32 s42, s54, 112\n"
    "s_waitcnt vmcnt(9)\n"
    "buffer_load_dwordx4 v24, s[24:27], s42 offen offset:0 lds\n"
    "v_mfma_f32_32x32x16_bf16 v[160:175], v[36:39], v[96:99], v[160:175]\n"
    "ds_read_b64_tr_b16 v[124:125], v248 offset:18432\n"
    "ds_read_b64_tr_b16 v[126:127], v248 offset:18944\n"
    "s_waitcnt vmcnt(9)\n"
    "buffer_load_dwordx4 v[128:131], v250, s[28:31], s54 offen sc1\n"
    "v_mfma_f32_32x32x16_bf16 v[192:207], v[40:43], v[96:99], v[192:207]\n"
    "ds_read_b64_tr_b16 v[80:81], v248 offset:19456\n"
    "ds_read_b64_tr_b16 v[82:83], v248 offset:19968\n"
    "s_waitcnt vmcnt(9)\n"
    "buffer_load_dwordx4 v25, s[24:27], s42 offen offset:1024 lds\n"
    "s_waitcnt lgkmcnt(8)\n"
    "v_mfma_f32_32x32x16_bf16 v[144:159], v[68:71], v[232:235], v[144:159]\n"
    "ds_read_b64_tr_b16 v[216:217], v248 offset:32768\n"
    "ds_read_b64_tr_b16 v[218:219], v248 offset:33280\n"
    "s_waitcnt vmcnt(9)\n"
    "buffer_load_dwordx4 v[220:223], v251, s[28:31], s54 offen sc1\n"
    "v_mfma_f32_32x32x16_bf16 v[176:191], v[116:119], v[232:235], v[176:191]\n"
    "ds_read_b64_tr_b16 v[132:133], v248 offset:33792\n"
    "ds_read_b64_tr_b16 v[134:135], v248 offset:34304\n"
    "v_add_u32 v250, 1835008, v250\n"
    "s_waitcnt vmcnt(9)\n"
    "buffer_load_dwordx4 v[136:139], v250, s[28:31], s48 offen sc1\n"
    "s_waitcnt lgkmcnt(8)\n"
    "v_mfma_f32_32x32x16_bf16 v[144:159], v[120:123], v[100:103], v[144:159]\n"
    "s_waitcnt vmcnt(9)\n"
    "buffer_load_dwordx4 v[84:87], v250, s[28:31], s49 offen sc1\n"
    "v_mfma_f32_32x32x16_bf16 v[176:191], v[212:215], v[100:103], v[176:191]\n"
    "v_add_u32 v251, 1835008, v251\n"
    "s_waitcnt vmcnt(9)\n"
    "buffer_load_dwordx4 v[88:91], v251, s[28:31], s48 offen sc1\n"
    "v_mfma_f32_32x32x16_bf16 v[160:175], v[44:47], v[64:67], v[160:175]\n"
    "ds_read_b64_tr_b16 v[224:225], v248 offset:34816\n"
    "ds_read_b64_tr_b16 v[226:227], v248 offset:35328\n"
    "s_waitcnt vmcnt(9)\n"
    "buffer_load_dwordx4 v[140:143], v250, s[28:31], s50 offen sc1\n"
    "v_mfma_f32_32x32x16_bf16 v[192:207], v[48:51], v[64:67], v[192:207]\n"
    "ds_read_b64_tr_b16 v[32:33], v248 offset:35840\n"
    "ds_read_b64_tr_b16 v[34:35], v248 offset:36352\n"
    "s_waitcnt lgkmcnt(8)\n"
    "v_mfma_f32_32x32x16_bf16 v[144:159], v[124:127], v[112:115], v[144:159]\n"
    "ds_read_b64_tr_b16 v[228:229], v248 offset:49152\n"
    "ds_read_b64_tr_b16 v[230:231], v248 offset:49664\n"
    "s_waitcnt vmcnt(9)\n"
    "buffer_load_dwordx4 v[92:95], v250, s[28:31], s51 offen sc1\n"
    "v_mfma_f32_32x32x16_bf16 v[176:191], v[80:83], v[112:115], v[176:191]\n"
    "ds_read_b64_tr_b16 v[96:97], v248 offset:50176\n"
    "ds_read_b64_tr_b16 v[98:99], v248 offset:50688\n"
    "s_waitcnt lgkmcnt(8)\n"
    "v_mfma_f32_32x32x16_bf16 v[144:159], v[216:219], v[72:75], v[144:159]\n"
    "s_waitcnt vmcnt(9)\n"
    "buffer_load_dwordx4 v[36:39], v251, s[28:31], s49 offen sc1\n"
    "v_mfma_f32_32x32x16_bf16 v[176:191], v[132:135], v[72:75], v[176:191]\n"
    "v_mfma_f32_32x32x16_bf16 v[160:175], v[52:55], v[208:211], v[160:175]\n"
    "ds_read_b64_tr_b16 v[40:41], v248 offset:51200\n"
    "ds_read_b64_tr_b16 v[42:43], v248 offset:51712\n"
    "s_waitcnt vmcnt(9)\n"
    "buffer_load_dwordx4 v[232:235], v250, s[28:31], s52 offen sc1\n"
    "v_mfma_f32_32x32x16_bf16 v[192:207], v[56:59], v[208:211], v[192:207]\n"
    "ds_read_b64_tr_b16 v[100:101], v248 offset:52224\n"
    "ds_read_b64_tr_b16 v[102:103], v248 offset:52736\n"
    "s_waitcnt lgkmcnt(8)\n"
    "v_mfma_f32_32x32x16_bf16 v[144:159], v[224:227], v[76:79], v[144:159]\n"
    "ds_read_b64_tr_b16 v[64:65], v249 offset:0\n"
    "ds_read_b64_tr_b16 v[66:67], v249 offset:512\n"
    "s_waitcnt vmcnt(9)\n"
    "buffer_load_dwordx4 v[44:47], v250, s[28:31], s53 offen sc1\n"
    "v_mfma_f32_32x32x16_bf16 v[176:191], v[32:35], v[76:79], v[176:191]\n"
    "ds_read_b64_tr_b16 v[48:49], v249 offset:1024\n"
    "ds_read_b64_tr_b16 v[50:51], v249 offset:1536\n"
    "s_waitcnt lgkmcnt(8)\n"
    "v_mfma_f32_32x32x16_bf16 v[144:159], v[228:231], v[128:131], v[144:159]\n"
    "s_waitcnt vmcnt(9)\n"
    "buffer_load_dwordx4 v[112:115], v251, s[28:31], s50 offen sc1\n"
    "v_mfma_f32_32x32x16_bf16 v[176:191], v[96:99], v[128:131], v[176:191]\n"
    "s_waitcnt vmcnt(9)\n"
    "v_mfma_f32_32x32x16_bf16 v[160:175], v[236:239], v[220:223], v[160:175]\n"
    "ds_read_b64_tr_b16 v[72:73], v249 offset:2048\n"
    "ds_read_b64_tr_b16 v[74:75], v249 offset:2560\n"
    "buffer_load_dwordx4 v[208:211], v250, s[28:31], s54 offen sc1\n"
    "v_mfma_f32_32x32x16_bf16 v[192:207], v[104:107], v[220:223], v[192:207]\n"
    "ds_read_b64_tr_b16 v[52:53], v249 offset:3072\n"
    "ds_read_b64_tr_b16 v[54:55], v249 offset:3584\n"
    "s_waitcnt vmcnt(9) lgkmcnt(8)\n"
    "v_mfma_f32_32x32x16_bf16 v[144:159], v[40:43], v[136:139], v[144:159]\n"
    "ds_read_b64_tr_b16 v[56:57], v249 offset:16384\n"
    "ds_read_b64_tr_b16 v[58:59], v249 offset:16896\n"
    "buffer_load_dwordx4 v[76:79], v251, s[28:31], s51 offen sc1\n"
    "v_mfma_f32_32x32x16_bf16 v[176:191], v[100:103], v[136:139], v[176:191]\n"
    "ds_read_b64_tr_b16 v[128:129], v249 offset:17408\n"
    "ds_read_b64_tr_b16 v[130:131], v249 offset:17920\n"
    "s_waitcnt vmcnt(9) lgkmcnt(8)\n"
    "v_mfma_f32_32x32x16_bf16 v[144:159], v[64:67], v[84:87], v[144:159]\n"
    "buffer_load_dwordx4 v[220:223], v251, s[28:31], s52 offen sc1\n"
    "v_mfma_f32_32x32x16_bf16 v[176:191], v[48:51], v[84:87], v[176:191]\n"
    "s_waitcnt vmcnt(9)\n"
    "v_mfma_f32_32x32x16_bf16 v[160:175], v[60:63], v[88:91], v[160:175]\n"
    "ds_read_b64_tr_b16 v[236:237], v249 offset:18432\n"
    "ds_read_b64_tr_b16 v[238:239], v249 offset:18944\n"
    "buffer_load_dwordx4 v[104:107], v251, s[28:31], s53 offen sc1\n"
    "v_mfma_f32_32x32x16_bf16 v[192:207], v[108:111], v[88:91], v[192:207]\n"
    "ds_read_b64_tr_b16 v[136:137], v249 offset:19456\n"
    "ds_read_b64_tr_b16 v[138:139], v249 offset:19968\n"
    "s_waitcnt vmcnt(9) lgkmcnt(8)\n"
    "v_mfma_f32_32x32x16_bf16 v[144:159], v[72:75], v[140:143], v[144:159]\n"
    "ds_read_b64_tr_b16 v[84:85], v249 offset:32768\n"
    "ds_read_b64_tr_b16 v[86:87], v249 offset:33280\n"
    "buffer_load_dwordx4 v[88:91], v251, s[28:31], s54 offen sc1\n"
    "v_mfma_f32_32x32x16_bf16 v[176:191], v[52:55], v[140:143], v[176:191]\n"
    "ds_read_b64_tr_b16 v[60:61], v249 offset:33792\n"
    "ds_read_b64_tr_b16 v[62:63], v249 offset:34304\n"
    "s_waitcnt vmcnt(9) lgkmcnt(8)\n"
    "v_mfma_f32_32x32x16_bf16 v[144:159], v[56:59], v[92:95], v[144:159]\n"
    "v_mfma_f32_32x32x16_bf16 v[176:191], v[128:131], v[92:95], v[176:191]\n"
    "v_add_u32 v251, 1835008, v251\n"
    "buffer_load_dwordx4 v[108:111], v251, s[28:31], s48 offen sc1\n"
    "s_waitcnt vmcnt(9)\n"
    "v_mfma_f32_32x32x16_bf16 v[160:175], v[68:71], v[36:39], v[160:175]\n"
    "ds_read_b64_tr_b16 v[140:141], v249 offset:34816\n"
    "ds_read_b64_tr_b16 v[142:143], v249 offset:35328\n"
    "v_mfma_f32_32x32x16_bf16 v[192:207], v[116:119], v[36:39], v[192:207]\n"
    "ds_read_b64_tr_b16 v[92:93], v249 offset:35840\n"
    "ds_read_b64_tr_b16 v[94:95], v249 offset:36352\n"
    "buffer_load_dwordx4 v[36:39], v251, s[28:31], s49 offen sc1\n"
    "s_waitcnt vmcnt(9) lgkmcnt(8)\n"
    "v_mfma_f32_32x32x16_bf16 v[144:159], v[236:239], v[232:235], v[144:159]\n"
    "v_mfma_f32_32x32x16_bf16 v[176:191], v[136:139], v[232:235], v[176:191]\n"
    "buffer_load_dwordx4 v[68:71], v251, s[28:31], s50 offen sc1\n"
    "s_waitcnt vmcnt(9) lgkmcnt(4)\n"
    "v_mfma_f32_32x32x16_bf16 v[144:159], v[84:87], v[44:47], v[144:159]\n"
    "v_mfma_f32_32x32x16_bf16 v[176:191], v[60:63], v[44:47], v[176:191]\n"
    "buffer_load_dwordx4 v[116:119], v251, s[28:31], s51 offen sc1\n"
    "s_waitcnt vmcnt(9)\n"
    "v_mfma_f32_32x32x16_bf16 v[160:175], v[120:123], v[112:115], v[160:175]\n"
    "v_mfma_f32_32x32x16_bf16 v[192:207], v[212:215], v[112:115], v[192:207]\n"
    "buffer_load_dwordx4 v[232:235], v251, s[28:31], s52 offen sc1\n"
    "s_waitcnt vmcnt(9) lgkmcnt(0)\n"
    "v_mfma_f32_32x32x16_bf16 v[144:159], v[140:143], v[208:211], v[144:159]\n"
    "buffer_load_dwordx4 v[44:47], v251, s[28:31], s53 offen sc1\n"
    "v_mfma_f32_32x32x16_bf16 v[176:191], v[92:95], v[208:211], v[176:191]\n"
    "s_nop 7\n"
    "s_waitcnt vmcnt(9)\n"
    "v_mfma_f32_32x32x16_bf16 v[160:175], v[124:127], v[76:79], v[160:175]\n"
    "buffer_load_dwordx4 v[112:115], v251, s[28:31], s54 offen sc1\n"
    "v_mfma_f32_32x32x16_bf16 v[192:207], v[80:83], v[76:79], v[192:207]\n"
    "s_waitcnt vmcnt(9)\n"
    "v_mfma_f32_32x32x16_bf16 v[160:175], v[216:219], v[220:223], v[160:175]\n"
    "ds_write_b128 v4, v[144:147] offset:0\n"
    "v_mfma_f32_32x32x16_bf16 v[192:207], v[132:135], v[220:223], v[192:207]\n"
    "ds_write_b128 v5, v[148:151] offset:0\n"
    "s_waitcnt vmcnt(8)\n"
    "v_mfma_f32_32x32x16_bf16 v[160:175], v[224:227], v[104:107], v[160:175]\n"
    "ds_write_b128 v6, v[152:155] offset:0\n"
    "v_mfma_f32_32x32x16_bf16 v[192:207], v[32:35], v[104:107], v[192:207]\n"
    "ds_write_b128 v7, v[156:159] offset:0\n"
    "s_waitcnt vmcnt(7)\n"
    "v_mfma_f32_32x32x16_bf16 v[160:175], v[228:231], v[88:91], v[160:175]\n"
    "ds_write_b128 v4, v[176:179] offset:128\n"
    "v_mfma_f32_32x32x16_bf16 v[192:207], v[96:99], v[88:91], v[192:207]\n"
    "ds_write_b128 v5, v[180:183] offset:128\n"
    "s_waitcnt vmcnt(6)\n"
    "v_mfma_f32_32x32x16_bf16 v[160:175], v[40:43], v[108:111], v[160:175]\n"
    "ds_write_b128 v6, v[184:187] offset:128\n"
    "v_mfma_f32_32x32x16_bf16 v[192:207], v[100:103], v[108:111], v[192:207]\n"
    "ds_write_b128 v7, v[188:191] offset:128\n"
    "s_waitcnt vmcnt(5)\n"
    "v_mfma_f32_32x32x16_bf16 v[160:175], v[64:67], v[36:39], v[160:175]\n"
    "s_nop 0\n"
    "v_mfma_f32_32x32x16_bf16 v[192:207], v[48:51], v[36:39], v[192:207]\n"
    "s_nop 0\n"
    "s_waitcnt lgkmcnt(0)\n"
    "s_barrier\n"
    "s_waitcnt vmcnt(4)\n"
    "v_mfma_f32_32x32x16_bf16 v[160:175], v[72:75], v[68:71], v[160:175]\n"
    "v_mfma_f32_32x32x16_bf16 v[192:207], v[52:55], v[68:71], v[192:207]\n"
    "s_waitcnt vmcnt(3)\n"
    "v_mfma_f32_32x32x16_bf16 v[160:175], v[56:59], v[116:119], v[160:175]\n"
    "v_mfma_f32_32x32x16_bf16 v[192:207], v[128:131], v[116:119], v[192:207]\n"
    "s_waitcnt vmcnt(2)\n"
    "v_mfma_f32_32x32x16_bf16 v[160:175], v[236:239], v[232:235], v[160:175]\n"
    "v_mfma_f32_32x32x16_bf16 v[192:207], v[136:139], v[232:235], v[192:207]\n"
    "s_waitcnt vmcnt(1)\n"
    "v_mfma_f32_32x32x16_bf16 v[160:175], v[84:87], v[44:47], v[160:175]\n"
    "v_mfma_f32_32x32x16_bf16 v[192:207], v[60:63], v[44:47], v[192:207]\n"
    "s_waitcnt vmcnt(0)\n"
    "v_mfma_f32_32x32x16_bf16 v[160:175], v[140:143], v[112:115], v[160:175]\n"
    "v_mfma_f32_32x32x16_bf16 v[192:207], v[92:95], v[112:115], v[192:207]\n"
    "s_nop 10\n"
    "ds_write_b128 v4, v[160:163] offset:32768\n"
    "ds_write_b128 v5, v[164:167] offset:32768\n"
    "ds_write_b128 v6, v[168:171] offset:32768\n"
    "ds_write_b128 v7, v[172:175] offset:32768\n"
    "ds_write_b128 v4, v[192:195] offset:32896\n"
    "ds_write_b128 v5, v[196:199] offset:32896\n"
    "ds_write_b128 v6, v[200:203] offset:32896\n"
    "ds_write_b128 v7, v[204:207] offset:32896\n"
    "s_waitcnt lgkmcnt(0)\n"
    "s_barrier\n"
    "s_endpgm\n"
    "; RETRY_BEGIN\n"
    ".La28_%=:\n"
    "ds_read_b32 v31, v21 offset:56\n"
    "s_waitcnt lgkmcnt(0)\n"
    "v_cmp_ne_u32 vcc, 0, v31\n"
    "s_cbranch_vccz .La29_%=\n"
    "s_sub_u32 s65, s65, 1\n"
    "s_cmp_lt_i32 s65, 0\n"
    "s_cbranch_scc0 .La28_%=\n"
    "s_branch .La29_%=\n"
    ".La30_%=:\n"
    "ds_read_b32 v31, v21 offset:52\n"
    "s_waitcnt lgkmcnt(0)\n"
    "v_cmp_ne_u32 vcc, 0, v31\n"
    "s_cbranch_vccz .La31_%=\n"
    "s_sub_u32 s65, s65, 1\n"
    "s_cmp_lt_i32 s65, 0\n"
    "s_cbranch_scc0 .La30_%=\n"
    "s_branch .La31_%=\n"
    ".La32_%=:\n"
    "ds_read_b32 v31, v21 offset:48\n"
    "s_waitcnt lgkmcnt(0)\n"
    "v_cmp_ne_u32 vcc, 0, v31\n"
    "s_cbranch_vccz .La33_%=\n"
    "s_sub_u32 s65, s65, 1\n"
    "s_cmp_lt_i32 s65, 0\n"
    "s_cbranch_scc0 .La32_%=\n"
    "s_branch .La33_%=\n"
    ".La34_%=:\n"
    "ds_read_b32 v31, v21 offset:44\n"
    "s_waitcnt lgkmcnt(0)\n"
    "v_cmp_ne_u32 vcc, 0, v31\n"
    "s_cbranch_vccz .La35_%=\n"
    "s_sub_u32 s65, s65, 1\n"
    "s_cmp_lt_i32 s65, 0\n"
    "s_cbranch_scc0 .La34_%=\n"
    "s_branch .La35_%=\n"
    ".La36_%=:\n"
    "ds_read_b32 v31, v21 offset:40\n"
    "s_waitcnt lgkmcnt(0)\n"
    "v_cmp_ne_u32 vcc, 0, v31\n"
    "s_cbranch_vccz .La37_%=\n"
    "s_sub_u32 s65, s65, 1\n"
    "s_cmp_lt_i32 s65, 0\n"
    "s_cbranch_scc0 .La36_%=\n"
    "s_branch .La37_%=\n"
    ".La38_%=:\n"
    "s_sleep 2\n"
    "buffer_load_dwordx4 v[48:51], v47, s[28:31], s69 offen sc1\n"
    "s_waitcnt vmcnt(0)\n"
    "v_cmp_ne_u32 vcc, s32, v48\n"
    "v_cmp_ne_u32 s[76:77], s33, v50\n"
    "s_or_b64 vcc, vcc, s[76:77]\n"
    "s_cbranch_vccz .La39_%=\n"
    "s_sub_u32 s65, s65, 1\n"
    "s_cmp_lt_i32 s65, 0\n"
    "s_cbranch_scc0 .La38_%=\n"
    "s_branch .La39_%=\n"
    ".La40_%=:\n"
    "ds_read_b32 v31, v21 offset:32\n"
    "s_waitcnt lgkmcnt(0)\n"
    "v_cmp_ne_u32 vcc, 0, v31\n"
    "s_cbranch_vccz .La41_%=\n"
    "s_sub_u32 s65, s65, 1\n"
    "s_cmp_lt_i32 s65, 0\n"
    "s_cbranch_scc0 .La40_%=\n"
    "s_branch .La41_%=\n"
    ".La42_%=:\n"
    "ds_read_b32 v32, v22 offset:0\n"
    "ds_read_b32 v33, v22 offset:256\n"
    "ds_read_b32 v34, v22 offset:512\n"
    "ds_read_b32 v35, v22 offset:768\n"
    "ds_read_b32 v36, v22 offset:1024\n"
    "ds_read_b32 v37, v22 offset:1280\n"
    "ds_read_b32 v38, v22 offset:1536\n"
    "ds_read_b32 v39, v22 offset:1792\n"
    "s_waitcnt lgkmcnt(0)\n"
    "v_add_f32 v32, v32, v33\n"
    "v_add_f32 v34, v34, v35\n"
    "v_add_f32 v36, v36, v37\n"
    "v_add_f32 v38, v38, v39\n"
    "v_add_f32 v32, v32, v34\n"
    "v_add_f32 v36, v36, v38\n"
    "v_cmp_u_f32 vcc, v32, v36\n"
    "s_cbranch_vccz .La43_%=\n"
    "s_sub_u32 s65, s65, 1\n"
    "s_cmp_lt_i32 s65, 0\n"
    "s_cbranch_scc0 .La42_%=\n"
    "s_branch .La43_%=\n"
    ".La44_%=:\n"
    "ds_read_b32 v31, v21 offset:28\n"
    "s_waitcnt lgkmcnt(0)\n"
    "v_cmp_ne_u32 vcc, 0, v31\n"
    "s_cbranch_vccz .La45_%=\n"
    "s_sub_u32 s65, s65, 1\n"
    "s_cmp_lt_i32 s65, 0\n"
    "s_cbranch_scc0 .La44_%=\n"
    "s_branch .La45_%=\n"
    ".La46_%=:\n"
    "s_sleep 4\n"
    "buffer_load_dwordx2 v[244:245], v3, s[28:31], s68 offen sc1\n"
    "s_waitcnt vmcnt(0)\n"
    "v_cmp_ne_u64 vcc, s[32:33], v[244:245]\n"
    "s_cbranch_vccz .La47_%=\n"
    "s_sub_u32 s65, s65, 1\n"
    "s_cmp_lt_i32 s65, 0\n"
    "s_cbranch_scc0 .La46_%=\n"
    "s_branch .La47_%=\n"
    ".La48_%=:\n"
    "s_sleep 4\n"
    "buffer_load_dwordx2 v[244:245], v3, s[28:31], s68 offen sc1\n"
    "s_waitcnt vmcnt(0)\n"
    "v_cmp_ne_u64 vcc, s[32:33], v[244:245]\n"
    "s_cbranch_vccz .La49_%=\n"
    "s_sub_u32 s65, s65, 1\n"
    "s_cmp_lt_i32 s65, 0\n"
    "s_cbranch_scc0 .La48_%=\n"
    "s_branch .La49_%=\n"
    "; RETRY_END\n"
    ".LB2_%=:\n"
    "s_lshr_b32 s50, s4, 1\n"
    "s_and_b32 s50, s50, 1\n"
    "v_and_b32 v24, 31, v254\n"
    "v_lshrrev_b32 v26, 5, v254\n"
    "v_lshlrev_b32 v23, 4, v26\n"
    "v_sub_u32 v23, v24, v23\n"
    "v_and_b32 v24, 1, v23\n"
    "v_lshlrev_b32 v24, 4, v24\n"
    "v_mov_b32 v28, 0x3f80\n"
    "v_lshlrev_b32 v24, v24, v28\n"
    "v_lshrrev_b32 v23, 1, v23\n"
    "s_mov_b32 s58, 0x07060302\n"
    "s_waitcnt lgkmcnt(0)\n"
    "s_lshl_b32 s76, s48, 16\n"
    "s_and_b32 s77, s48, 0xffff0000\n"
    "s_cmp_eq_u32 s50, 0\n"
    "s_cselect_b32 s76, s76, s77\n"
    "v_mov_b32 v28, 0x3fb8aa3b\n"
    "v_mul_f32 v29, s76, v28\n"
    "v_fma_f32 v28, s76, v28, -v29\n"
    "v_mov_b32 v30, 0x32a5705f\n"
    "v_fmac_f32 v28, s76, v30\n"
    "v_rndne_f32 v30, v29\n"
    "v_sub_f32 v29, v29, v30\n"
    "v_add_f32 v28, v29, v28\n"
    "v_exp_f32 v28, v28\n"
    "v_cvt_i32_f32 v29, v30\n"
    "v_ldexp_f32 v28, v28, v29\n"
    "v_cvt_pk_bf16_f32 v4, v28, v28\n"
    "v_lshlrev_b32 v4, 16, v4\n"
    "v_mul_f32 v5, 0xbfb8aa3b, v4\n"
    "v_mul_f32 v6, 0.15915494, v4\n"
    "v_add_f32 v7, v4, v4\n"
    "v_cmp_eq_u32 s[36:37], 0, v23\n"
    "v_cmp_eq_u32 s[38:39], 1, v23\n"
    "v_cmp_eq_u32 s[40:41], 2, v23\n"
    "v_cmp_eq_u32 s[42:43], 3, v23\n"
    "v_cndmask_b32 v136, 0, v24, s[36:37]\n"
    "v_cmp_eq_u32 s[36:37], 4, v23\n"
    "v_cndmask_b32 v137, 0, v24, s[38:39]\n"
    "v_cmp_eq_u32 s[38:39], 5, v23\n"
    "v_cndmask_b32 v138, 0, v24, s[40:41]\n"
    "v_cmp_eq_u32 s[40:41], 6, v23\n"
    "v_cndmask_b32 v139, 0, v24, s[42:43]\n"
    "v_cmp_eq_u32 s[42:43], 7, v23\n"
    "v_cndmask_b32 v28, 0, v24, s[36:37]\n"
    "v_cndmask_b32 v29, 0, v24, s[38:39]\n"
    "v_cndmask_b32 v30, 0, v24, s[40:41]\n"
    "v_cndmask_b32 v31, 0, v24, s[42:43]\n"
    "s_waitcnt vmcnt(0)\n"
    "s_setprio 0\n"
    "v_lshlrev_b32 v140, 16, v8\n"
    "v_and_b32 v141, 0xffff0000, v8\n"
    "v_lshlrev_b32 v142, 16, v9\n"
    "v_and_b32 v143, 0xffff0000, v9\n"
    "v_mul_f32 v140, 0x3fb8aa3b, v140\n"
    "v_mul_f32 v141, 0x3fb8aa3b, v141\n"
    "v_exp_f32 v140, v140\n"
    "v_exp_f32 v141, v141\n"
    "v_mul_f32 v142, v142, v6\n"
    "v_mul_f32 v143, v143, v6\n"
    "v_cvt_pk_bf16_f32 v168, v140, v141\n"
    "v_lshlrev_b32 v140, 16, v168\n"
    "v_and_b32 v141, 0xffff0000, v168\n"
    "v_mul_f32 v140, v140, v5\n"
    "v_mul_f32 v141, v141, v5\n"
    "v_mul_f32 v168, 0x41000000, v140\n"
    "v_mul_f32 v170, 0x41000000, v142\n"
    "v_mul_f32 v169, 0x41000000, v141\n"
    "v_mul_f32 v171, 0x41000000, v143\n"
    "v_exp_f32 v168, v168\n"
    "v_fract_f32 v170, v170\n"
    "v_exp_f32 v169, v169\n"
    "v_fract_f32 v171, v171\n"
    "v_cos_f32 v144, v170\n"
    "v_sin_f32 v145, v170\n"
    "v_cos_f32 v146, v171\n"
    "v_sin_f32 v147, v171\n"
    "v_mul_f32 v144, v144, v168\n"
    "v_mul_f32 v145, v145, v168\n"
    "v_mul_f32 v146, v146, v169\n"
    "v_mul_f32 v147, v147, v169\n"
    "v_cvt_pk_bf16_f32 v32, 1.0, v144\n"
    "v_cvt_pk_bf16_f32 v40, 0, v145\n"
    "v_cvt_pk_bf16_f32 v48, 1.0, v146\n"
    "v_cvt_pk_bf16_f32 v56, 0, v147\n"
    "v_mul_f32 v148, v144, v144\n"
    "v_mul_f32 v149, v145, v144\n"
    "v_mul_f32 v150, v146, v146\n"
    "v_mul_f32 v151, v147, v146\n"
    "v_fma_f32 v148, -v145, v145, v148\n"
    "v_fmac_f32 v149, v144, v145\n"
    "v_fma_f32 v150, -v147, v147, v150\n"
    "v_fmac_f32 v151, v146, v147\n"
    "v_mul_f32 v152, v148, v144\n"
    "v_mul_f32 v153, v149, v144\n"
    "v_mul_f32 v154, v150, v146\n"
    "v_mul_f32 v155, v151, v146\n"
    "v_fma_f32 v152, -v149, v145, v152\n"
    "v_fmac_f32 v153, v148, v145\n"
    "v_fma_f32 v154, -v151, v147, v154\n"
    "v_fmac_f32 v155, v150, v147\n"
    "v_cvt_pk_bf16_f32 v33, v148, v152\n"
    "v_cvt_pk_bf16_f32 v41, v149, v153\n"
    "v_cvt_pk_bf16_f32 v49, v150, v154\n"
    "v_cvt_pk_bf16_f32 v57, v151, v155\n"
    "v_mul_f32 v148, v152, v144\n"
    "v_mul_f32 v149, v153, v144\n"
    "v_mul_f32 v150, v154, v146\n"
    "v_mul_f32 v151, v155, v146\n"
    "v_fma_f32 v148, -v153, v145, v148\n"
    "v_fmac_f32 v149, v152, v145\n"
    "v_fma_f32 v150, -v155, v147, v150\n"
    "v_fmac_f32 v151, v154, v147\n"
    "v_mul_f32 v152, v148, v144\n"
    "v_mul_f32 v153, v149, v144\n"
    "v_mul_f32 v154, v150, v146\n"
    "v_mul_f32 v155, v151, v146\n"
    "v_fma_f32 v152, -v149, v145, v152\n"
    "v_fmac_f32 v153, v148, v145\n"
    "v_fma_f32 v154, -v151, v147, v154\n"
    "v_fmac_f32 v155, v150, v147\n"
    "v_cvt_pk_bf16_f32 v34, v148, v152\n"
    "v_cvt_pk_bf16_f32 v42, v149, v153\n"
    "v_cvt_pk_bf16_f32 v50, v150, v154\n"
    "v_cvt_pk_bf16_f32 v58, v151, v155\n"
    "v_mul_f32 v148, v152, v144\n"
    "v_mul_f32 v149, v153, v144\n"
    "v_mul_f32 v150, v154, v146\n"
    "v_mul_f32 v151, v155, v146\n"
    "v_fma_f32 v148, -v153, v145, v148\n"
    "v_fmac_f32 v149, v152, v145\n"
    "v_fma_f32 v150, -v155, v147, v150\n"
    "v_fmac_f32 v151, v154, v147\n"
    "v_mul_f32 v152, v148, v144\n"
    "v_mul_f32 v153, v149, v144\n"
    "v_mul_f32 v154, v150, v146\n"
    "v_mul_f32 v155, v151, v146\n"
    "v_fma_f32 v152, -v149, v145, v152\n"
    "v_fmac_f32 v153, v148, v145\n"
    "v_fma_f32 v154, -v151, v147, v154\n"
    "v_fmac_f32 v155, v150, v147\n"
    "v_cvt_pk_bf16_f32 v35, v148, v152\n"
    "v_cvt_pk_bf16_f32 v43, v149, v153\n"
    "v_cvt_pk_bf16_f32 v51, v150, v154\n"
    "v_cvt_pk_bf16_f32 v59, v151, v155\n"
    "v_mul_f32 v148, v152, v144\n"
    "v_mul_f32 v149, v153, v144\n"
    "v_mul_f32 v150, v154, v146\n"
    "s_setprio 3\n"
    "v_mfma_f32_32x32x16_bf16 v[64:79], v[32:35], v[136:139], 0\n"
    "s_setprio 0\n"
    "v_mul_f32 v151, v155, v146\n"
    "v_fma_f32 v148, -v153, v145, v148\n"
    "v_fmac_f32 v149, v152, v145\n"
    "v_fma_f32 v150, -v155, v147, v150\n"
    "v_fmac_f32 v151, v154, v147\n"
    "v_mul_f32 v152, v148, v144\n"
    "s_setprio 3\n"
    "v_mfma_f32_32x32x16_bf16 v[80:95], v[40:43], v[136:139], 0\n"
    "s_setprio 0\n"
    "v_mul_f32 v153, v149, v144\n"
    "v_mul_f32 v154, v150, v146\n"
    "v_mul_f32 v155, v151, v146\n"
    "v_fma_f32 v152, -v149, v145, v152\n"
    "v_fmac_f32 v153, v148, v145\n"
    "v_fma_f32 v154, -v151, v147, v154\n"
    "s_setprio 3\n"
    "v_mfma_f32_32x32x16_bf16 v[96:111], v[48:51], v[136:139], 0\n"
    "s_setprio 0\n"
    "v_fmac_f32 v155, v150, v147\n"
    "v_cvt_pk_bf16_f32 v36, v148, v152\n"
    "v_cvt_pk_bf16_f32 v44, v149, v153\n"
    "v_cvt_pk_bf16_f32 v52, v150, v154\n"
    "v_cvt_pk_bf16_f32 v60, v151, v155\n"
    "v_mul_f32 v148, v152, v144\n"
    "s_setprio 3\n"
    "v_mfma_f32_32x32x16_bf16 v[112:127], v[56:59], v[136:139], 0\n"
    "s_setprio 0\n"
    "v_mul_f32 v149, v153, v144\n"
    "v_mul_f32 v150, v154, v146\n"
    "v_mul_f32 v151, v155, v146\n"
    "v_fma_f32 v148, -v153, v145, v148\n"
    "v_fmac_f32 v149, v152, v145\n"
    "v_fma_f32 v150, -v155, v147, v150\n"
    "v_fmac_f32 v151, v154, v147\n"
    "v_mul_f32 v152, v148, v144\n"
    "v_mul_f32 v153, v149, v144\n"
    "v_mul_f32 v154, v150, v146\n"
    "v_mul_f32 v155, v151, v146\n"
    "v_fma_f32 v152, -v149, v145, v152\n"
    "v_fmac_f32 v153, v148, v145\n"
    "v_fma_f32 v154, -v151, v147, v154\n"
    "v_fmac_f32 v155, v150, v147\n"
    "v_cvt_pk_bf16_f32 v37, v148, v152\n"
    "v_cvt_pk_bf16_f32 v45, v149, v153\n"
    "v_cvt_pk_bf16_f32 v53, v150, v154\n"
    "v_cvt_pk_bf16_f32 v61, v151, v155\n"
    "v_mul_f32 v148, v152, v144\n"
    "v_mul_f32 v149, v153, v144\n"
    "v_mul_f32 v150, v154, v146\n"
    "v_mul_f32 v151, v155, v146\n"
    "v_fma_f32 v148, -v153, v145, v148\n"
    "v_fmac_f32 v149, v152, v145\n"
    "v_fma_f32 v150, -v155, v147, v150\n"
    "v_fmac_f32 v151, v154, v147\n"
    "v_mul_f32 v152, v148, v144\n"
    "v_mul_f32 v153, v149, v144\n"
    "v_mul_f32 v154, v150, v146\n"
    "v_mul_f32 v155, v151, v146\n"
    "v_fma_f32 v152, -v149, v145, v152\n"
    "v_fmac_f32 v153, v148, v145\n"
    "v_fma_f32 v154, -v151, v147, v154\n"
    "v_fmac_f32 v155, v150, v147\n"
    "v_cvt_pk_bf16_f32 v38, v148, v152\n"
    "v_cvt_pk_bf16_f32 v46, v149, v153\n"
    "v_cvt_pk_bf16_f32 v54, v150, v154\n"
    "v_cvt_pk_bf16_f32 v62, v151, v155\n"
    "v_mul_f32 v148, v152, v144\n"
    "v_mul_f32 v149, v153, v144\n"
    "v_mul_f32 v150, v154, v146\n"
    "v_mul_f32 v151, v155, v146\n"
    "v_fma_f32 v148, -v153, v145, v148\n"
    "v_fmac_f32 v149, v152, v145\n"
    "v_fma_f32 v150, -v155, v147, v150\n"
    "v_fmac_f32 v151, v154, v147\n"
    "v_mul_f32 v152, v148, v144\n"
    "v_mul_f32 v153, v149, v144\n"
    "v_mul_f32 v154, v150, v146\n"
    "v_mul_f32 v155, v151, v146\n"
    "v_fma_f32 v152, -v149, v145, v152\n"
    "v_fmac_f32 v153, v148, v145\n"
    "v_fma_f32 v154, -v151, v147, v154\n"
    "v_fmac_f32 v155, v150, v147\n"
    "v_cvt_pk_bf16_f32 v39, v148, v152\n"
    "v_cvt_pk_bf16_f32 v47, v149, v153\n"
    "v_cvt_pk_bf16_f32 v55, v150, v154\n"
    "v_cvt_pk_bf16_f32 v63, v151, v155\n"
    "v_mul_f32 v148, v152, v144\n"
    "v_mul_f32 v149, v153, v144\n"
    "v_mul_f32 v150, v154, v146\n"
    "s_setprio 3\n"
    "v_mfma_f32_32x32x16_bf16 v[64:79], v[36:39], v[28:31], v[64:79]\n"
    "s_setprio 0\n"
    "v_mul_f32 v151, v155, v146\n"
    "v_fma_f32 v148, -v153, v145, v148\n"
    "v_fmac_f32 v149, v152, v145\n"
    "v_fma_f32 v150, -v155, v147, v150\n"
    "v_fmac_f32 v151, v154, v147\n"
    "v_mul_f32 v152, v148, v144\n"
    "s_setprio 3\n"
    "v_mfma_f32_32x32x16_bf16 v[80:95], v[44:47], v[28:31], v[80:95]\n"
    "s_setprio 0\n"
    "v_mul_f32 v153, v149, v144\n"
    "v_mul_f32 v154, v150, v146\n"
    "v_mul_f32 v155, v151, v146\n"
    "v_fma_f32 v152, -v149, v145, v152\n"
    "v_fmac_f32 v153, v148, v145\n"
    "v_fma_f32 v154, -v151, v147, v154\n"
    "s_setprio 3\n"
    "v_mfma_f32_32x32x16_bf16 v[96:111], v[52:55], v[28:31], v[96:111]\n"
    "s_setprio 0\n"
    "v_fmac_f32 v155, v150, v147\n"
    "v_cvt_pk_bf16_f32 v32, v148, v152\n"
    "v_cvt_pk_bf16_f32 v40, v149, v153\n"
    "v_cvt_pk_bf16_f32 v48, v150, v154\n"
    "v_cvt_pk_bf16_f32 v56, v151, v155\n"
    "v_mul_f32 v148, v152, v144\n"
    "s_setprio 3\n"
    "v_mfma_f32_32x32x16_bf16 v[112:127], v[60:63], v[28:31], v[112:127]\n"
    "s_setprio 0\n"
    "v_mul_f32 v149, v153, v144\n"
    "v_mul_f32 v150, v154, v146\n"
    "v_mul_f32 v151, v155, v146\n"
    "v_fma_f32 v148, -v153, v145, v148\n"
    "v_fmac_f32 v149, v152, v145\n"
    "v_fma_f32 v150, -v155, v147, v150\n"
    "v_fmac_f32 v151, v154, v147\n"
    "v_mul_f32 v152, v148, v144\n"
    "v_mul_f32 v153, v149, v144\n"
    "v_mul_f32 v154, v150, v146\n"
    "v_mul_f32 v155, v151, v146\n"
    "v_fma_f32 v152, -v149, v145, v152\n"
    "v_perm_b32 v172, v65, v64, s58\n"
    "v_fmac_f32 v153, v148, v145\n"
    "v_perm_b32 v173, v67, v66, s58\n"
    "v_fma_f32 v154, -v151, v147, v154\n"
    "v_perm_b32 v174, v69, v68, s58\n"
    "v_fmac_f32 v155, v150, v147\n"
    "v_perm_b32 v175, v71, v70, s58\n"
    "v_cvt_pk_bf16_f32 v33, v148, v152\n"
    "v_perm_b32 v176, v73, v72, s58\n"
    "v_cvt_pk_bf16_f32 v41, v149, v153\n"
    "v_perm_b32 v177, v75, v74, s58\n"
    "v_cvt_pk_bf16_f32 v49, v150, v154\n"
    "v_perm_b32 v178, v77, v76, s58\n"
    "v_cvt_pk_bf16_f32 v57, v151, v155\n"
    "v_perm_b32 v179, v79, v78, s58\n"
    "v_mul_f32 v148, v152, v144\n"
    "v_perm_b32 v180, v81, v80, s58\n"
    "v_mul_f32 v149, v153, v144\n"
    "v_perm_b32 v181, v83, v82, s58\n"
    "v_mul_f32 v150, v154, v146\n"
    "v_perm_b32 v182, v85, v84, s58\n"
    "v_mul_f32 v151, v155, v146\n"
    "v_perm_b32 v183, v87, v86, s58\n"
    "v_fma_f32 v148, -v153, v145, v148\n"
    "v_perm_b32 v184, v89, v88, s58\n"
    "v_fmac_f32 v149, v152, v145\n"
    "v_perm_b32 v185, v91, v90, s58\n"
    "v_fma_f32 v150, -v155, v147, v150\n"
    "v_perm_b32 v186, v93, v92, s58\n"
    "v_fmac_f32 v151, v154, v147\n"
    "v_perm_b32 v187, v95, v94, s58\n"
    "v_mul_f32 v152, v148, v144\n"
    "v_perm_b32 v188, v97, v96, s58\n"
    "v_mul_f32 v153, v149, v144\n"
    "v_perm_b32 v189, v99, v98, s58\n"
    "v_mul_f32 v154, v150, v146\n"
    "v_perm_b32 v190, v101, v100, s58\n"
    "v_mul_f32 v155, v151, v146\n"
    "v_perm_b32 v191, v103, v102, s58\n"
    "v_fma_f32 v152, -v149, v145, v152\n"
    "v_perm_b32 v192, v105, v104, s58\n"
    "v_fmac_f32 v153, v148, v145\n"
    "v_perm_b32 v193, v107, v106, s58\n"
    "v_fma_f32 v154, -v151, v147, v154\n"
    "v_perm_b32 v194, v109, v108, s58\n"
    "v_fmac_f32 v155, v150, v147\n"
    "v_perm_b32 v195, v111, v110, s58\n"
    "v_cvt_pk_bf16_f32 v34, v148, v152\n"
    "v_perm_b32 v196, v113, v112, s58\n"
    "v_cvt_pk_bf16_f32 v42, v149, v153\n"
    "v_perm_b32 v197, v115, v114, s58\n"
    "v_cvt_pk_bf16_f32 v50, v150, v154\n"
    "v_perm_b32 v198, v117, v116, s58\n"
    "v_cvt_pk_bf16_f32 v58, v151, v155\n"
    "v_perm_b32 v199, v119, v118, s58\n"
    "v_mul_f32 v148, v152, v144\n"
    "v_perm_b32 v200, v121, v120, s58\n"
    "v_mul_f32 v149, v153, v144\n"
    "v_perm_b32 v201, v123, v122, s58\n"
    "v_mul_f32 v150, v154, v146\n"
    "v_perm_b32 v202, v125, v124, s58\n"
    "v_mul_f32 v151, v155, v146\n"
    "v_perm_b32 v203, v127, v126, s58\n"
    "v_fma_f32 v148, -v153, v145, v148\n"
    "v_fmac_f32 v149, v152, v145\n"
    "v_fma_f32 v150, -v155, v147, v150\n"
    "v_fmac_f32 v151, v154, v147\n"
    "v_mul_f32 v152, v148, v144\n"
    "v_mul_f32 v153, v149, v144\n"
    "v_mul_f32 v154, v150, v146\n"
    "v_mul_f32 v155, v151, v146\n"
    "v_fma_f32 v152, -v149, v145, v152\n"
    "v_fmac_f32 v153, v148, v145\n"
    "v_fma_f32 v154, -v151, v147, v154\n"
    "v_fmac_f32 v155, v150, v147\n"
    "v_cvt_pk_bf16_f32 v35, v148, v152\n"
    "v_cvt_pk_bf16_f32 v43, v149, v153\n"
    "v_cvt_pk_bf16_f32 v51, v150, v154\n"
    "v_cvt_pk_bf16_f32 v59, v151, v155\n"
    "v_mul_f32 v148, v152, v144\n"
    "v_mul_f32 v149, v153, v144\n"
    "v_mul_f32 v150, v154, v146\n"
    "s_setprio 3\n"
    "v_mfma_f32_32x32x16_bf16 v[64:79], v[32:35], v[136:139], 0\n"
    "s_setprio 0\n"
    "v_mul_f32 v151, v155, v146\n"
    "v_fma_f32 v148, -v153, v145, v148\n"
    "v_fmac_f32 v149, v152, v145\n"
    "v_fma_f32 v150, -v155, v147, v150\n"
    "v_fmac_f32 v151, v154, v147\n"
    "v_mul_f32 v152, v148, v144\n"
    "s_setprio 3\n"
    "v_mfma_f32_32x32x16_bf16 v[80:95], v[40:43], v[136:139], 0\n"
    "s_setprio 0\n"
    "v_mul_f32 v153, v149, v144\n"
    "v_mul_f32 v154, v150, v146\n"
    "v_mul_f32 v155, v151, v146\n"
    "v_fma_f32 v152, -v149, v145, v152\n"
    "v_fmac_f32 v153, v148, v145\n"
    "v_fma_f32 v154, -v151, v147, v154\n"
    "s_setprio 3\n"
    "v_mfma_f32_32x32x16_bf16 v[96:111], v[48:51], v[136:139], 0\n"
    "s_setprio 0\n"
    "v_fmac_f32 v155, v150, v147\n"
    "v_cvt_pk_bf16_f32 v36, v148, v152\n"
    "v_cvt_pk_bf16_f32 v44, v149, v153\n"
    "v_cvt_pk_bf16_f32 v52, v150, v154\n"
    "v_cvt_pk_bf16_f32 v60, v151, v155\n"
    "v_mul_f32 v148, v152, v144\n"
    "s_setprio 3\n"
    "v_mfma_f32_32x32x16_bf16 v[112:127], v[56:59], v[136:139], 0\n"
    "s_setprio 0\n"
    "v_mul_f32 v149, v153, v144\n"
    "v_mul_f32 v150, v154, v146\n"
    "v_mul_f32 v151, v155, v146\n"
    "v_fma_f32 v148, -v153, v145, v148\n"
    "v_fmac_f32 v149, v152, v145\n"
    "v_fma_f32 v150, -v155, v147, v150\n"
    "v_fmac_f32 v151, v154, v147\n"
    "v_mul_f32 v152, v148, v144\n"
    "v_mul_f32 v153, v149, v144\n"
    "v_mul_f32 v154, v150, v146\n"
    "v_mul_f32 v155, v151, v146\n"
    "v_fma_f32 v152, -v149, v145, v152\n"
    "v_fmac_f32 v153, v148, v145\n"
    "v_fma_f32 v154, -v151, v147, v154\n"
    "v_fmac_f32 v155, v150, v147\n"
    "v_cvt_pk_bf16_f32 v37, v148, v152\n"
    "v_cvt_pk_bf16_f32 v45, v149, v153\n"
    "v_cvt_pk_bf16_f32 v53, v150, v154\n"
    "v_cvt_pk_bf16_f32 v61, v151, v155\n"
    "v_mul_f32 v148, v152, v144\n"
    "v_mul_f32 v149, v153, v144\n"
    "v_mul_f32 v150, v154, v146\n"
    "v_mul_f32 v151, v155, v146\n"
    "v_fma_f32 v148, -v153, v145, v148\n"
    "v_fmac_f32 v149, v152, v145\n"
    "v_fma_f32 v150, -v155, v147, v150\n"
    "v_fmac_f32 v151, v154, v147\n"
    "v_mul_f32 v152, v148, v144\n"
    "v_mul_f32 v153, v149, v144\n"
    "v_mul_f32 v154, v150, v146\n"
    "v_mul_f32 v155, v151, v146\n"
    "v_fma_f32 v152, -v149, v145, v152\n"
    "v_fmac_f32 v153, v148, v145\n"
    "v_fma_f32 v154, -v151, v147, v154\n"
    "v_fmac_f32 v155, v150, v147\n"
    "v_cvt_pk_bf16_f32 v38, v148, v152\n"
    "v_cvt_pk_bf16_f32 v46, v149, v153\n"
    "v_cvt_pk_bf16_f32 v54, v150, v154\n"
    "v_cvt_pk_bf16_f32 v62, v151, v155\n"
    "v_mul_f32 v148, v152, v144\n"
    "v_mul_f32 v149, v153, v144\n"
    "v_mul_f32 v150, v154, v146\n"
    "v_mul_f32 v151, v155, v146\n"
    "v_fma_f32 v148, -v153, v145, v148\n"
    "v_fmac_f32 v149, v152, v145\n"
    "v_fma_f32 v150, -v155, v147, v150\n"
    "v_fmac_f32 v151, v154, v147\n"
    "v_mul_f32 v152, v148, v144\n"
    "v_mul_f32 v153, v149, v144\n"
    "v_mul_f32 v154, v150, v146\n"
    "v_mul_f32 v155, v151, v146\n"
    "v_fma_f32 v152, -v149, v145, v152\n"
    "v_fmac_f32 v153, v148, v145\n"
    "v_fma_f32 v154, -v151, v147, v154\n"
    "v_fmac_f32 v155, v150, v147\n"
    "v_cvt_pk_bf16_f32 v39, v148, v152\n"
    "v_cvt_pk_bf16_f32 v47, v149, v153\n"
    "v_cvt_pk_bf16_f32 v55, v150, v154\n"
    "v_cvt_pk_bf16_f32 v63, v151, v155\n"
    "v_mul_f32 v168, 1.0, v140\n"
    "v_mul_f32 v170, 1.0, v142\n"
    "v_mul_f32 v169, 1.0, v141\n"
    "s_setprio 3\n"
    "v_mfma_f32_32x32x16_bf16 v[64:79], v[36:39], v[28:31], v[64:79]\n"
    "s_setprio 0\n"
    "v_mul_f32 v171, 1.0, v143\n"
    "v_exp_f32 v168, v168\n"
    "v_fract_f32 v170, v170\n"
    "v_exp_f32 v169, v169\n"
    "v_fract_f32 v171, v171\n"
    "v_cos_f32 v144, v170\n"
    "s_setprio 3\n"
    "v_mfma_f32_32x32x16_bf16 v[80:95], v[44:47], v[28:31], v[80:95]\n"
    "s_setprio 0\n"
    "v_sin_f32 v145, v170\n"
    "v_cos_f32 v146, v171\n"
    "v_sin_f32 v147, v171\n"
    "v_mul_f32 v144, v144, v168\n"
    "v_mul_f32 v145, -v145, v168\n"
    "v_mul_f32 v146, v146, v169\n"
    "s_setprio 3\n"
    "v_mfma_f32_32x32x16_bf16 v[96:111], v[52:55], v[28:31], v[96:111]\n"
    "s_setprio 0\n"
    "v_mul_f32 v147, -v147, v169\n"
    "v_mul_f32 v168, 0x43800000, v140\n"
    "v_mul_f32 v170, 0x43800000, v142\n"
    "v_mul_f32 v169, 0x43800000, v141\n"
    "v_mul_f32 v171, 0x43800000, v143\n"
    "v_exp_f32 v168, v168\n"
    "s_setprio 3\n"
    "v_mfma_f32_32x32x16_bf16 v[112:127], v[60:63], v[28:31], v[112:127]\n"
    "s_setprio 0\n"
    "v_fract_f32 v170, v170\n"
    "v_exp_f32 v169, v169\n"
    "v_fract_f32 v171, v171\n"
    "v_cos_f32 v156, v170\n"
    "v_sin_f32 v157, v170\n"
    "v_cos_f32 v158, v171\n"
    "v_sin_f32 v159, v171\n"
    "v_mul_f32 v156, v156, v168\n"
    "v_mul_f32 v157, -v157, v168\n"
    "v_mul_f32 v158, v158, v169\n"
    "v_mul_f32 v159, -v159, v169\n"
    "v_lshlrev_b32 v168, 16, v10\n"
    "v_perm_b32 v204, v65, v64, s58\n"
    "v_and_b32 v169, 0xffff0000, v10\n"
    "v_perm_b32 v205, v67, v66, s58\n"
    "v_lshlrev_b32 v170, 16, v11\n"
    "v_perm_b32 v206, v69, v68, s58\n"
    "v_and_b32 v171, 0xffff0000, v11\n"
    "v_perm_b32 v207, v71, v70, s58\n"
    "v_mul_f32 v148, v168, v7\n"
    "v_perm_b32 v208, v73, v72, s58\n"
    "v_mul_f32 v149, -v170, v7\n"
    "v_perm_b32 v209, v75, v74, s58\n"
    "v_mul_f32 v150, v169, v7\n"
    "v_perm_b32 v210, v77, v76, s58\n"
    "v_mul_f32 v151, -v171, v7\n"
    "v_perm_b32 v211, v79, v78, s58\n"
    "v_mul_f32 v152, v148, v144\n"
    "v_perm_b32 v212, v81, v80, s58\n"
    "v_mul_f32 v153, v149, v144\n"
    "v_perm_b32 v213, v83, v82, s58\n"
    "v_mul_f32 v154, v150, v146\n"
    "v_perm_b32 v214, v85, v84, s58\n"
    "v_mul_f32 v155, v151, v146\n"
    "v_perm_b32 v215, v87, v86, s58\n"
    "v_fma_f32 v152, -v149, v145, v152\n"
    "v_perm_b32 v216, v89, v88, s58\n"
    "v_fmac_f32 v153, v148, v145\n"
    "v_perm_b32 v217, v91, v90, s58\n"
    "v_fma_f32 v154, -v151, v147, v154\n"
    "v_perm_b32 v218, v93, v92, s58\n"
    "v_fmac_f32 v155, v150, v147\n"
    "v_perm_b32 v219, v95, v94, s58\n"
    "v_cvt_pk_bf16_f32 v32, v148, v152\n"
    "v_perm_b32 v220, v97, v96, s58\n"
    "v_cvt_pk_bf16_f32 v40, v149, v153\n"
    "v_perm_b32 v221, v99, v98, s58\n"
    "v_cvt_pk_bf16_f32 v48, v150, v154\n"
    "v_perm_b32 v222, v101, v100, s58\n"
    "v_cvt_pk_bf16_f32 v56, v151, v155\n"
    "v_perm_b32 v223, v103, v102, s58\n"
    "v_mul_f32 v160, v148, v156\n"
    "v_perm_b32 v224, v105, v104, s58\n"
    "v_mul_f32 v161, v149, v156\n"
    "v_perm_b32 v225, v107, v106, s58\n"
    "v_mul_f32 v162, v150, v158\n"
    "v_perm_b32 v226, v109, v108, s58\n"
    "v_mul_f32 v163, v151, v158\n"
    "v_perm_b32 v227, v111, v110, s58\n"
    "v_fma_f32 v160, -v149, v157, v160\n"
    "v_perm_b32 v228, v113, v112, s58\n"
    "v_fmac_f32 v161, v148, v157\n"
    "v_perm_b32 v229, v115, v114, s58\n"
    "v_fma_f32 v162, -v151, v159, v162\n"
    "v_perm_b32 v230, v117, v116, s58\n"
    "v_fmac_f32 v163, v150, v159\n"
    "v_perm_b32 v231, v119, v118, s58\n"
    "v_mul_f32 v148, v152, v144\n"
    "v_perm_b32 v232, v121, v120, s58\n"
    "v_mul_f32 v149, v153, v144\n"
    "v_perm_b32 v233, v123, v122, s58\n"
    "v_mul_f32 v150, v154, v146\n"
    "v_perm_b32 v234, v125, v124, s58\n"
    "v_mul_f32 v151, v155, v146\n"
    "v_perm_b32 v235, v127, v126, s58\n"
    "v_fma_f32 v148, -v153, v145, v148\n"
    "v_fmac_f32 v149, v152, v145\n"
    "v_fma_f32 v150, -v155, v147, v150\n"
    "v_fmac_f32 v151, v154, v147\n"
    "v_mul_f32 v152, v148, v144\n"
    "v_mul_f32 v153, v149, v144\n"
    "v_mul_f32 v154, v150, v146\n"
    "v_mul_f32 v155, v151, v146\n"
    "v_fma_f32 v152, -v149, v145, v152\n"
    "v_fmac_f32 v153, v148, v145\n"
    "v_fma_f32 v154, -v151, v147, v154\n"
    "v_fmac_f32 v155, v150, v147\n"
    "v_cvt_pk_bf16_f32 v33, v148, v152\n"
    "v_cvt_pk_bf16_f32 v41, v149, v153\n"
    "v_cvt_pk_bf16_f32 v49, v150, v154\n"
    "v_cvt_pk_bf16_f32 v57, v151, v155\n"
    "v_mul_f32 v8, v148, v156\n"
    "v_mul_f32 v9, v149, v156\n"
    "v_mul_f32 v10, v150, v158\n"
    "v_mul_f32 v11, v151, v158\n"
    "v_fma_f32 v8, -v149, v157, v8\n"
    "v_fmac_f32 v9, v148, v157\n"
    "v_fma_f32 v10, -v151, v159, v10\n"
    "v_fmac_f32 v11, v150, v159\n"
    "v_mul_f32 v148, v152, v144\n"
    "v_mul_f32 v149, v153, v144\n"
    "v_mul_f32 v150, v154, v146\n"
    "v_mul_f32 v151, v155, v146\n"
    "v_fma_f32 v148, -v153, v145, v148\n"
    "v_fmac_f32 v149, v152, v145\n"
    "v_fma_f32 v150, -v155, v147, v150\n"
    "v_fmac_f32 v151, v154, v147\n"
    "v_mul_f32 v152, v148, v144\n"
    "v_mul_f32 v153, v149, v144\n"
    "v_mul_f32 v154, v150, v146\n"
    "v_mul_f32 v155, v151, v146\n"
    "v_fma_f32 v152, -v149, v145, v152\n"
    "v_fmac_f32 v153, v148, v145\n"
    "v_fma_f32 v154, -v151, v147, v154\n"
    "v_fmac_f32 v155, v150, v147\n"
    "v_cvt_pk_bf16_f32 v34, v148, v152\n"
    "v_cvt_pk_bf16_f32 v42, v149, v153\n"
    "v_cvt_pk_bf16_f32 v50, v150, v154\n"
    "v_cvt_pk_bf16_f32 v58, v151, v155\n"
    "v_mul_f32 v164, v148, v156\n"
    "v_mul_f32 v165, v149, v156\n"
    "v_mul_f32 v166, v150, v158\n"
    "v_mul_f32 v167, v151, v158\n"
    "v_fma_f32 v164, -v149, v157, v164\n"
    "v_fmac_f32 v165, v148, v157\n"
    "v_fma_f32 v166, -v151, v159, v166\n"
    "v_fmac_f32 v167, v150, v159\n"
    "v_mul_f32 v148, v152, v144\n"
    "v_mul_f32 v149, v153, v144\n"
    "v_mul_f32 v150, v154, v146\n"
    "v_mul_f32 v151, v155, v146\n"
    "v_fma_f32 v148, -v153, v145, v148\n"
    "v_fmac_f32 v149, v152, v145\n"
    "v_fma_f32 v150, -v155, v147, v150\n"
    "v_fmac_f32 v151, v154, v147\n"
    "v_mul_f32 v152, v148, v144\n"
    "v_mul_f32 v153, v149, v144\n"
    "v_mul_f32 v154, v150, v146\n"
    "v_mul_f32 v155, v151, v146\n"
    "v_fma_f32 v152, -v149, v145, v152\n"
    "v_fmac_f32 v153, v148, v145\n"
    "v_fma_f32 v154, -v151, v147, v154\n"
    "v_fmac_f32 v155, v150, v147\n"
    "v_cvt_pk_bf16_f32 v35, v148, v152\n"
    "v_cvt_pk_bf16_f32 v43, v149, v153\n"
    "v_cvt_pk_bf16_f32 v51, v150, v154\n"
    "v_cvt_pk_bf16_f32 v59, v151, v155\n"
    "v_mul_f32 v168, v148, v156\n"
    "v_mul_f32 v169, v149, v156\n"
    "v_mul_f32 v170, v150, v158\n"
    "v_mul_f32 v171, v151, v158\n"
    "v_fma_f32 v168, -v149, v157, v168\n"
    "v_fmac_f32 v169, v148, v157\n"
    "v_fma_f32 v170, -v151, v159, v170\n"
    "v_fmac_f32 v171, v150, v159\n"
    "v_cvt_pk_bf16_f32 v36, v160, v8\n"
    "v_cvt_pk_bf16_f32 v44, v161, v9\n"
    "v_cvt_pk_bf16_f32 v52, v162, v10\n"
    "s_setprio 3\n"
    "v_mfma_f32_32x32x16_bf16 v[64:79], v[32:35], v[136:139], 0\n"
    "s_setprio 0\n"
    "v_cvt_pk_bf16_f32 v60, v163, v11\n"
    "v_cvt_pk_bf16_f32 v37, v164, v168\n"
    "v_cvt_pk_bf16_f32 v45, v165, v169\n"
    "v_cvt_pk_bf16_f32 v53, v166, v170\n"
    "v_cvt_pk_bf16_f32 v61, v167, v171\n"
    "v_mul_f32 v148, v160, v156\n"
    "s_setprio 3\n"
    "v_mfma_f32_32x32x16_bf16 v[80:95], v[40:43], v[136:139], 0\n"
    "s_setprio 0\n"
    "v_mul_f32 v149, v161, v156\n"
    "v_mul_f32 v150, v162, v158\n"
    "v_mul_f32 v151, v163, v158\n"
    "v_fma_f32 v148, -v161, v157, v148\n"
    "v_fmac_f32 v149, v160, v157\n"
    "v_fma_f32 v150, -v163, v159, v150\n"
    "s_setprio 3\n"
    "v_mfma_f32_32x32x16_bf16 v[96:111], v[48:51], v[136:139], 0\n"
    "s_setprio 0\n"
    "v_fmac_f32 v151, v162, v159\n"
    "v_mul_f32 v152, v164, v156\n"
    "v_mul_f32 v153, v165, v156\n"
    "v_mul_f32 v154, v166, v158\n"
    "v_mul_f32 v155, v167, v158\n"
    "v_fma_f32 v152, -v165, v157, v152\n"
    "s_setprio 3\n"
    "v_mfma_f32_32x32x16_bf16 v[112:127], v[56:59], v[136:139], 0\n"
    "s_setprio 0\n"
    "v_fmac_f32 v153, v164, v157\n"
    "v_fma_f32 v154, -v167, v159, v154\n"
    "v_fmac_f32 v155, v166, v159\n"
    "v_cvt_pk_bf16_f32 v38, v148, v152\n"
    "v_cvt_pk_bf16_f32 v46, v149, v153\n"
    "v_cvt_pk_bf16_f32 v54, v150, v154\n"
    "v_cvt_pk_bf16_f32 v62, v151, v155\n"
    "v_mul_f32 v160, v148, v156\n"
    "v_mul_f32 v161, v149, v156\n"
    "v_mul_f32 v162, v150, v158\n"
    "v_mul_f32 v163, v151, v158\n"
    "v_fma_f32 v160, -v149, v157, v160\n"
    "v_fmac_f32 v161, v148, v157\n"
    "v_fma_f32 v162, -v151, v159, v162\n"
    "v_fmac_f32 v163, v150, v159\n"
    "v_mul_f32 v164, v152, v156\n"
    "v_mul_f32 v165, v153, v156\n"
    "v_mul_f32 v166, v154, v158\n"
    "v_mul_f32 v167, v155, v158\n"
    "v_fma_f32 v164, -v153, v157, v164\n"
    "v_fmac_f32 v165, v152, v157\n"
    "v_fma_f32 v166, -v155, v159, v166\n"
    "v_fmac_f32 v167, v154, v159\n"
    "v_cvt_pk_bf16_f32 v39, v160, v164\n"
    "v_cvt_pk_bf16_f32 v47, v161, v165\n"
    "v_cvt_pk_bf16_f32 v55, v162, v166\n"
    "v_cvt_pk_bf16_f32 v63, v163, v167\n"
    "v_mul_f32 v148, v160, v156\n"
    "v_mul_f32 v149, v161, v156\n"
    "v_mul_f32 v150, v162, v158\n"
    "s_setprio 3\n"
    "v_mfma_f32_32x32x16_bf16 v[64:79], v[36:39], v[28:31], v[64:79]\n"
    "s_setprio 0\n"
    "v_mul_f32 v151, v163, v158\n"
    "v_fma_f32 v148, -v161, v157, v148\n"
    "v_fmac_f32 v149, v160, v157\n"
    "v_fma_f32 v150, -v163, v159, v150\n"
    "v_fmac_f32 v151, v162, v159\n"
    "v_mul_f32 v152, v148, v156\n"
    "s_setprio 3\n"
    "v_mfma_f32_32x32x16_bf16 v[80:95], v[44:47], v[28:31], v[80:95]\n"
    "s_setprio 0\n"
    "v_mul_f32 v153, v149, v156\n"
    "v_mul_f32 v154, v150, v158\n"
    "v_mul_f32 v155, v151, v158\n"
    "v_fma_f32 v152, -v149, v157, v152\n"
    "v_fmac_f32 v153, v148, v157\n"
    "v_fma_f32 v154, -v151, v159, v154\n"
    "s_setprio 3\n"
    "v_mfma_f32_32x32x16_bf16 v[96:111], v[52:55], v[28:31], v[96:111]\n"
    "s_setprio 0\n"
    "v_fmac_f32 v155, v150, v159\n"
    "v_cvt_pk_bf16_f32 v32, v148, v152\n"
    "v_cvt_pk_bf16_f32 v40, v149, v153\n"
    "v_cvt_pk_bf16_f32 v48, v150, v154\n"
    "v_cvt_pk_bf16_f32 v56, v151, v155\n"
    "v_mul_f32 v148, v152, v156\n"
    "s_setprio 3\n"
    "v_mfma_f32_32x32x16_bf16 v[112:127], v[60:63], v[28:31], v[112:127]\n"
    "s_setprio 0\n"
    "v_mul_f32 v149, v153, v156\n"
    "v_mul_f32 v150, v154, v158\n"
    "v_mul_f32 v151, v155, v158\n"
    "v_fma_f32 v148, -v153, v157, v148\n"
    "v_fmac_f32 v149, v152, v157\n"
    "v_fma_f32 v150, -v155, v159, v150\n"
    "v_fmac_f32 v151, v154, v159\n"
    "v_mul_f32 v152, v148, v156\n"
    "v_mul_f32 v153, v149, v156\n"
    "v_mul_f32 v154, v150, v158\n"
    "v_mul_f32 v155, v151, v158\n"
    "v_fma_f32 v152, -v149, v157, v152\n"
    "v_perm_b32 v128, v65, v64, s58\n"
    "v_fmac_f32 v153, v148, v157\n"
    "v_perm_b32 v129, v67, v66, s58\n"
    "v_fma_f32 v154, -v151, v159, v154\n"
    "v_perm_b32 v130, v69, v68, s58\n"
    "v_fmac_f32 v155, v150, v159\n"
    "v_perm_b32 v131, v71, v70, s58\n"
    "v_cvt_pk_bf16_f32 v33, v148, v152\n"
    "v_perm_b32 v132, v73, v72, s58\n"
    "v_cvt_pk_bf16_f32 v41, v149, v153\n"
    "v_perm_b32 v133, v75, v74, s58\n"
    "v_cvt_pk_bf16_f32 v49, v150, v154\n"
    "v_perm_b32 v134, v77, v76, s58\n"
    "v_cvt_pk_bf16_f32 v57, v151, v155\n"
    "v_perm_b32 v135, v79, v78, s58\n"
    "v_mul_f32 v148, v152, v156\n"
    "v_mul_f32 v149, v153, v156\n"
    "s_setprio 3\n"
    "v_mfma_f32_16x16x32_bf16 v[236:239], v[128:131], v[172:175], 0\n"
    "s_setprio 0\n"
    "v_mul_f32 v150, v154, v158\n"
    "v_mul_f32 v151, v155, v158\n"
    "s_setprio 3\n"
    "v_mfma_f32_16x16x32_bf16 v[240:243], v[128:131], v[204:207], 0\n"
    "s_setprio 0\n"
    "v_fma_f32 v148, -v153, v157, v148\n"
    "v_fmac_f32 v149, v152, v157\n"
    "s_setprio 3\n"
    "v_mfma_f32_16x16x32_bf16 v[236:239], v[132:135], v[176:179], v[236:239]\n"
    "s_setprio 0\n"
    "v_fma_f32 v150, -v155, v159, v150\n"
    "v_fmac_f32 v151, v154, v159\n"
    "s_setprio 3\n"
    "v_mfma_f32_16x16x32_bf16 v[240:243], v[132:135], v[208:211], v[240:243]\n"
    "s_setprio 0\n"
    "v_cvt_pk_bf16_f32 v34, v148, v148\n"
    "v_perm_b32 v128, v81, v80, s58\n"
    "v_cvt_pk_bf16_f32 v42, v149, v149\n"
    "v_perm_b32 v129, v83, v82, s58\n"
    "v_cvt_pk_bf16_f32 v50, v150, v150\n"
    "v_perm_b32 v130, v85, v84, s58\n"
    "v_cvt_pk_bf16_f32 v58, v151, v151\n"
    "v_perm_b32 v131, v87, v86, s58\n"
    "s_setprio 3\n"
    "v_perm_b32 v132, v89, v88, s58\n"
    "v_perm_b32 v133, v91, v90, s58\n"
    "v_perm_b32 v134, v93, v92, s58\n"
    "v_perm_b32 v135, v95, v94, s58\n"
    "s_nop 1\n"
    "v_mfma_f32_16x16x32_bf16 v[236:239], v[128:131], v[180:183], v[236:239]\n"
    "s_nop 1\n"
    "v_mfma_f32_16x16x32_bf16 v[240:243], v[128:131], v[212:215], v[240:243]\n"
    "s_nop 1\n"
    "v_mfma_f32_16x16x32_bf16 v[236:239], v[132:135], v[184:187], v[236:239]\n"
    "s_nop 1\n"
    "v_mfma_f32_16x16x32_bf16 v[240:243], v[132:135], v[216:219], v[240:243]\n"
    "v_perm_b32 v128, v97, v96, s58\n"
    "v_perm_b32 v129, v99, v98, s58\n"
    "v_perm_b32 v130, v101, v100, s58\n"
    "v_perm_b32 v131, v103, v102, s58\n"
    "v_perm_b32 v132, v105, v104, s58\n"
    "v_perm_b32 v133, v107, v106, s58\n"
    "v_perm_b32 v134, v109, v108, s58\n"
    "v_perm_b32 v135, v111, v110, s58\n"
    "s_nop 1\n"
    "v_mfma_f32_16x16x32_bf16 v[236:239], v[128:131], v[188:191], v[236:239]\n"
    "s_nop 1\n"
    "v_mfma_f32_16x16x32_bf16 v[240:243], v[128:131], v[220:223], v[240:243]\n"
    "s_nop 1\n"
    "v_mfma_f32_16x16x32_bf16 v[236:239], v[132:135], v[192:195], v[236:239]\n"
    "s_nop 1\n"
    "v_mfma_f32_16x16x32_bf16 v[240:243], v[132:135], v[224:227], v[240:243]\n"
    "v_perm_b32 v128, v113, v112, s58\n"
    "v_perm_b32 v129, v115, v114, s58\n"
    "v_perm_b32 v130, v117, v116, s58\n"
    "v_perm_b32 v131, v119, v118, s58\n"
    "v_perm_b32 v132, v121, v120, s58\n"
    "v_perm_b32 v133, v123, v122, s58\n"
    "v_perm_b32 v134, v125, v124, s58\n"
    "v_perm_b32 v135, v127, v126, s58\n"
    "s_nop 1\n"
    "v_mfma_f32_16x16x32_bf16 v[236:239], v[128:131], v[196:199], v[236:239]\n"
    "s_nop 1\n"
    "v_mfma_f32_16x16x32_bf16 v[240:243], v[128:131], v[228:231], v[240:243]\n"
    "s_nop 1\n"
    "v_mfma_f32_16x16x32_bf16 v[236:239], v[132:135], v[200:203], v[236:239]\n"
    "s_nop 1\n"
    "v_mfma_f32_16x16x32_bf16 v[240:243], v[132:135], v[232:235], v[240:243]\n"
    "s_setprio 0\n"
    "s_setprio 3\n"
    "s_nop 1\n"
    "v_mfma_f32_32x32x16_bf16 v[64:79], v[32:35], v[136:139], 0\n"
    "s_nop 1\n"
    "v_mfma_f32_32x32x16_bf16 v[80:95], v[40:43], v[136:139], 0\n"
    "s_nop 1\n"
    "v_mfma_f32_32x32x16_bf16 v[96:111], v[48:51], v[136:139], 0\n"
    "s_nop 1\n"
    "v_mfma_f32_32x32x16_bf16 v[112:127], v[56:59], v[136:139], 0\n"
    "s_nop 11\n"
    "v_perm_b32 v128, v65, v64, s58\n"
    "v_perm_b32 v129, v67, v66, s58\n"
    "v_perm_b32 v130, v69, v68, s58\n"
    "v_perm_b32 v131, v71, v70, s58\n"
    "v_perm_b32 v132, v73, v72, s58\n"
    "v_perm_b32 v133, v75, v74, s58\n"
    "v_perm_b32 v134, v77, v76, s58\n"
    "v_perm_b32 v135, v79, v78, s58\n"
    "s_nop 1\n"
    "v_mfma_f32_16x16x32_bf16 v[244:247], v[128:131], v[172:175], 0\n"
    "s_nop 1\n"
    "v_mfma_f32_16x16x32_bf16 v[248:251], v[128:131], v[204:207], 0\n"
    "s_nop 1\n"
    "v_mfma_f32_16x16x32_bf16 v[244:247], v[132:135], v[176:179], v[244:247]\n"
    "s_nop 1\n"
    "v_mfma_f32_16x16x32_bf16 v[248:251], v[132:135], v[208:211], v[248:251]\n"
    "v_perm_b32 v128, v81, v80, s58\n"
    "v_perm_b32 v129, v83, v82, s58\n"
    "v_perm_b32 v130, v85, v84, s58\n"
    "v_perm_b32 v131, v87, v86, s58\n"
    "v_perm_b32 v132, v89, v88, s58\n"
    "v_perm_b32 v133, v91, v90, s58\n"
    "v_perm_b32 v134, v93, v92, s58\n"
    "v_perm_b32 v135, v95, v94, s58\n"
    "s_nop 1\n"
    "v_mfma_f32_16x16x32_bf16 v[244:247], v[128:131], v[180:183], v[244:247]\n"
    "s_nop 1\n"
    "v_mfma_f32_16x16x32_bf16 v[248:251], v[128:131], v[212:215], v[248:251]\n"
    "s_nop 1\n"
    "v_mfma_f32_16x16x32_bf16 v[244:247], v[132:135], v[184:187], v[244:247]\n"
    "s_nop 1\n"
    "v_mfma_f32_16x16x32_bf16 v[248:251], v[132:135], v[216:219], v[248:251]\n"
    "v_perm_b32 v128, v97, v96, s58\n"
    "v_perm_b32 v129, v99, v98, s58\n"
    "v_perm_b32 v130, v101, v100, s58\n"
    "v_perm_b32 v131, v103, v102, s58\n"
    "v_perm_b32 v132, v105, v104, s58\n"
    "v_perm_b32 v133, v107, v106, s58\n"
    "v_perm_b32 v134, v109, v108, s58\n"
    "v_perm_b32 v135, v111, v110, s58\n"
    "s_nop 1\n"
    "v_mfma_f32_16x16x32_bf16 v[244:247], v[128:131], v[188:191], v[244:247]\n"
    "s_nop 1\n"
    "v_mfma_f32_16x16x32_bf16 v[248:251], v[128:131], v[220:223], v[248:251]\n"
    "s_nop 1\n"
    "v_mfma_f32_16x16x32_bf16 v[244:247], v[132:135], v[192:195], v[244:247]\n"
    "s_nop 1\n"
    "v_mfma_f32_16x16x32_bf16 v[248:251], v[132:135], v[224:227], v[248:251]\n"
    "v_perm_b32 v128, v113, v112, s58\n"
    "v_perm_b32 v129, v115, v114, s58\n"
    "v_perm_b32 v130, v117, v116, s58\n"
    "v_perm_b32 v131, v119, v118, s58\n"
    "v_perm_b32 v132, v121, v120, s58\n"
    "v_perm_b32 v133, v123, v122, s58\n"
    "v_perm_b32 v134, v125, v124, s58\n"
    "v_perm_b32 v135, v127, v126, s58\n"
    "s_nop 1\n"
    "v_mfma_f32_16x16x32_bf16 v[244:247], v[128:131], v[196:199], v[244:247]\n"
    "s_nop 1\n"
    "v_mfma_f32_16x16x32_bf16 v[248:251], v[128:131], v[228:231], v[248:251]\n"
    "s_nop 1\n"
    "v_mfma_f32_16x16x32_bf16 v[244:247], v[132:135], v[200:203], v[244:247]\n"
    "s_nop 1\n"
    "v_mfma_f32_16x16x32_bf16 v[248:251], v[132:135], v[232:235], v[248:251]\n"
    "s_mul_i32 s76, s4, 3072\n"
    "s_add_u32 s76, s76, 139264\n"
    "v_and_b32 v1, 15, v254\n"
    "v_mul_u32_u24 v10, 96, v1\n"
    "v_and_b32 v2, 48, v254\n"
    "v_add3_u32 v10, v10, v2, s76\n"
    "s_mov_b32 s30, 12058624\n"
    "s_mov_b32 s31, 0x20000\n"
    "v_min_u32 v6, 31, v254\n"
    "v_lshlrev_b32 v6, 7, v6\n"
    "s_mov_b32 s62, 4096\n"
    "s_waitcnt vmcnt(0)\n"
    "buffer_load_dwordx2 v[4:5], v6, s[28:31], s62 offen sc1\n"
    "ds_write_b128 v10, v[236:239] offset:0\n"
    "ds_write_b128 v10, v[240:243] offset:1536\n"
    "s_mov_b32 exec_hi, 0\n"
    "ds_write_b128 v10, v[244:247] offset:64\n"
    "ds_write_b128 v10, v[248:251] offset:1600\n"
    "s_mov_b32 exec_hi, -1\n"
    "s_sub_u32 s5, s4, 4\n"
    "s_lshr_b32 s50, s5, 1\n"
    "s_and_b32 s51, s5, 1\n"
    "s_mul_i32 s52, s50, 6144\n"
    "s_add_u32 s52, s52, 151552\n"
    "s_add_u32 s32, s32, 1\n"
    "s_addc_u32 s33, s33, 0\n"
    "s_mul_i32 s53, s50, 16640\n"
    "s_add_u32 s53, s53, 114688\n"
    "s_mul_i32 s54, s50, 5120\n"
    "s_add_u32 s54, s54, 151552\n"
    "v_and_b32 v24, 31, v254\n"
    "v_add_u32 v25, 1, v24\n"
    "v_and_b32 v25, 31, v25\n"
    "v_lshrrev_b32 v26, 5, v254\n"
    "s_cmp_eq_u32 s51, 0\n"
    "s_cbranch_scc0 .La2_%=\n"
    "v_mov_b32 v15, 0x1000302\n"
    "v_mov_b32 v14, 0x5040706\n"
    "v_mov_b32 v13, 0x1000302\n"
    "v_mov_b32 v12, 0x5040706\n"
    "v_mov_b32 v17, 0xffffffff\n"
    "v_mov_b32 v16, 0xffffffff\n"
    "v_mov_b32 v18, 0x0\n"
    "v_mov_b32 v19, 0x1\n"
    "v_mov_b32 v27, v24\n"
    "s_mov_b32 exec_lo, 0\n"
    "v_mov_b32 v15, 0x1000100\n"
    "v_mov_b32 v14, 0x3020504\n"
    "v_mov_b32 v13, 0x5040504\n"
    "v_mov_b32 v12, 0x7060100\n"
    "v_mov_b32 v17, 0x0\n"
    "v_mov_b32 v16, 0xffff\n"
    "v_mov_b32 v18, 0x2\n"
    "v_mov_b32 v19, 0x2\n"
    "v_mov_b32 v27, v25\n"
    "s_mov_b32 exec_hi, 0x80000000\n"
    "v_mov_b32 v19, 0x3\n"
    "s_branch .La3_%=\n"
    ".La2_%=:\n"
    "v_mov_b32 v15, 0x1000100\n"
    "v_mov_b32 v14, 0x1000302\n"
    "v_mov_b32 v13, 0x3020302\n"
    "v_mov_b32 v12, 0x1000100\n"
    "v_mov_b32 v17, 0x0\n"
    "v_mov_b32 v16, 0xffffffff\n"
    "v_mov_b32 v18, 0x3\n"
    "v_mov_b32 v19, 0x3\n"
    "v_mov_b32 v27, v25\n"
    "s_mov_b32 exec_lo, 0\n"
    "v_mov_b32 v15, 0x5040504\n"
    "v_mov_b32 v14, 0x5040706\n"
    "v_mov_b32 v13, 0x7060706\n"
    "s_mov_b32 exec_hi, 0x80000000\n"
    "v_mov_b32 v19, 0x4\n"
    "s_mov_b32 exec_lo, 0x80000000\n"
    "s_mov_b32 exec_hi, 0x7fffffff\n"
    "v_mov_b32 v12, 0x5040504\n"
    ".La3_%=:\n"
    "s_mov_b64 exec, -1\n"
    "v_mul_u32_u24 v28, 96, v24\n"
    "v_lshl_add_u32 v18, v18, 4, s52\n"
    "v_add_u32 v18, v18, v28\n"
    "v_mul_u32_u24 v28, 96, v27\n"
    "v_lshl_add_u32 v19, v19, 4, s52\n"
    "v_add_u32 v19, v19, v28\n"
    "v_lshl_add_u32 v29, s51, 1, v26\n"
    "v_cmp_eq_u32 vcc, 31, v24\n"
    "v_mov_b32 v20, 4\n"
    "v_bfe_i32 v22, v254, 0, 1\n"
    "v_addc_co_u32 v30, vcc, 16, v29, vcc\n"
    "v_or_b32 v22, 0xffff, v22\n"
    "v_lshrrev_b32 v21, 2, v30\n"
    "v_and_b32 v30, 3, v30\n"
    "v_mul_u32_u24 v28, 96, v24\n"
    "v_lshl_add_u32 v20, v20, 4, s52\n"
    "v_add_u32 v20, v20, v28\n"
    "v_mul_u32_u24 v28, 96, v25\n"
    "v_lshl_add_u32 v21, v21, 4, s52\n"
    "v_add_u32 v21, v21, v28\n"
    "v_lshl_add_u32 v20, v29, 2, v20\n"
    "v_lshl_add_u32 v21, v30, 2, v21\n"
    "s_mov_b32 s57, 0x05040100\n"
    "s_lshl2_add_u32 s76, s5, 151296\n"
    "v_mov_b32 v25, s76\n"
    "v_and_b32 v28, 3, v254\n"
    "v_mul_u32_u24 v27, 320, v28\n"
    "v_add_u32 v27, 150016, v27\n"
    "v_lshlrev_b32 v26, 2, v28\n"
    "v_add_u32 v26, 151296, v26\n"
    "s_mov_b32 s14, 1835008\n"
    "s_mov_b32 s15, 0x20000\n"
    "s_mov_b32 s22, 1835008\n"
    "s_mov_b32 s23, 0x20000\n"
    "s_lshl_b32 s76, s49, 16\n"
    "s_and_b32 s77, s49, 0xffff0000\n"
    "s_cmp_eq_u32 s50, 0\n"
    "s_cselect_b32 s76, s76, s77\n"
    "v_and_b32 v30, 127, v3\n"
    "v_cmp_eq_u32 vcc, 0, v30\n"
    "v_mov_b32 v23, s76\n"
    "v_lshlrev_b32 v24, 4, v30\n"
    "s_add_u32 s76, s53, 2032\n"
    "v_cndmask_b32 v23, 0, v23, vcc\n"
    "v_sub_u32 v24, s76, v24\n"
    "v_and_b32 v1, 15, v254\n"
    "v_lshrrev_b32 v2, 4, v254\n"
    "v_add_u32 v28, 1, v1\n"
    "v_and_b32 v28, 3, v28\n"
    "v_mul_u32_u24 v28, 4162, v28\n"
    "v_lshl_add_u32 v28, v2, 4, v28\n"
    "v_lshlrev_b32 v29, 1, v1\n"
    "v_sub_u32 v28, v28, v29\n"
    "s_add_u32 s77, s53, 30\n"
    "v_add_u32 v8, s77, v28\n"
    "s_lshl_b32 s76, s51, 9\n"
    "s_sub_u32 s77, s54, s76\n"
    "s_add_u32 s77, s77, 544\n"
    "v_lshl_add_u32 v28, v1, 5, s77\n"
    "v_lshl_add_u32 v9, v2, 4, v28\n"
    "v_mov_b32 v31, 152576\n"
    "v_lshl_add_u32 v31, v3, 3, v31\n"
    "v_lshlrev_b32 v7, 4, v3\n"
    "s_lshl_b32 s55, s75, 13\n"
    "s_add_u32 s56, s55, 1048576\n"
    "s_add_u32 s61, s56, 4096\n"
    "v_mov_b32 v28, 1\n"
    "ds_write_b32 v25, v28\n"
    ".La4_%=:\n"
    "ds_read_b32 v28, v26\n"
    "s_sub_u32 s65, s65, 1\n"
    "s_cmp_lt_i32 s65, 0\n"
    "s_waitcnt lgkmcnt(0)\n"
    "v_cmp_gt_u32 vcc, 1, v28\n"
    "s_cbranch_scc1 .La5_%=\n"
    "s_cbranch_vccnz .La4_%=\n"
    ".La5_%=:\n"
    "ds_read_b128 v[48:51], v18 offset:0\n"
    "ds_read_b128 v[52:55], v18 offset:3072\n"
    "ds_read_b128 v[56:59], v19 offset:0\n"
    "ds_read_b128 v[60:63], v19 offset:3072\n"
    "ds_read_b32 v64, v20 offset:0\n"
    "ds_read_b32 v65, v20 offset:3072\n"
    "ds_read_b32 v66, v21 offset:0\n"
    "ds_read_b32 v67, v21 offset:3072\n"
    "v_mov_b32 v28, 2\n"
    "ds_write_b32 v25, v28\n"
    "s_waitcnt lgkmcnt(7)\n"
    "v_add_f32 v48, v48, v52\n"
    "v_add_f32 v49, v49, v53\n"
    "v_add_f32 v50, v50, v54\n"
    "v_add_f32 v51, v51, v55\n"
    "v_add_f32 v48, v48, v23\n"
    "s_waitcnt lgkmcnt(5)\n"
    "v_add_f32 v56, v56, v60\n"
    "v_add_f32 v57, v57, v61\n"
    "v_add_f32 v58, v58, v62\n"
    "v_add_f32 v59, v59, v63\n"
    "v_cvt_pk_bf16_f32 v48, v48, v49\n"
    "v_cvt_pk_bf16_f32 v49, v50, v51\n"
    "v_cvt_pk_bf16_f32 v50, v56, v57\n"
    "v_cvt_pk_bf16_f32 v51, v58, v59\n"
    "s_waitcnt lgkmcnt(1)\n"
    "v_add_f32 v64, v64, v65\n"
    "v_add_f32 v66, v66, v67\n"
    "v_perm_b32 v43, v49, v48, v15\n"
    "v_perm_b32 v42, v49, v48, v14\n"
    "v_perm_b32 v41, v49, v48, v13\n"
    "v_perm_b32 v52, v51, v50, v13\n"
    "v_perm_b32 v40, v49, v48, v12\n"
    "v_perm_b32 v53, v51, v50, v12\n"
    "v_bfi_b32 v41, v17, v52, v41\n"
    "v_bfi_b32 v40, v16, v53, v40\n"
    "v_cvt_pk_bf16_f32 v47, v64, v64\n"
    "v_cvt_pk_bf16_f32 v44, v66, v66\n"
    "v_cvt_pk_bf16_f32 v46, v64, v64\n"
    "v_bfi_b32 v45, v22, v44, v47\n"
    "v_mov_b32 v29, 0x01000100\n"
    "v_perm_b32 v39, v40, v40, v29\n"
    "v_perm_b32 v38, v40, v40, v29\n"
    "v_mov_b32 v60, v44\n"
    "v_mov_b32 v61, v44\n"
    "v_mov_b32_dpp v39, v43 wave_shl:1 row_mask:0xf bank_mask:0xf\n"
    "v_mov_b32_dpp v38, v42 wave_shl:1 row_mask:0xf bank_mask:0xf\n"
    "v_alignbit_b32 v48, v40, v39, 16\n"
    "v_alignbit_b32 v49, v41, v40, 16\n"
    "v_alignbit_b32 v50, v42, v41, 16\n"
    "v_alignbit_b32 v51, v43, v42, 16\n"
    "v_alignbit_b32 v52, v39, v38, 16\n"
    "v_alignbit_b32 v53, v40, v39, 16\n"
    "v_alignbit_b32 v54, v41, v40, 16\n"
    "v_alignbit_b32 v55, v42, v41, 16\n"
    "v_mov_b32 v56, v39\n"
    "v_mov_b32 v57, v40\n"
    "v_mov_b32 v58, v41\n"
    "v_mov_b32 v59, v42\n"
    "v_mov_b32 v62, v44\n"
    "v_alignbit_b32 v63, v46, v45, 16\n"
    "v_mov_b32 v64, v44\n"
    "v_mov_b32 v65, v44\n"
    "v_mov_b32 v66, v63\n"
    "v_mov_b32 v67, v46\n"
    "v_mov_b32 v68, v44\n"
    "v_mov_b32 v69, v44\n"
    "v_mov_b32 v70, v45\n"
    "v_mov_b32 v71, v46\n"
    "s_waitcnt vmcnt(0)\n"
    "v_cmp_ne_u64 vcc, s[32:33], v[4:5]\n"
    "s_cbranch_vccnz .La6_%=\n"
    ".La7_%=:\n"
    "buffer_load_dwordx4 v[208:211], v7, s[28:31], s56 offen sc1\n"
    "buffer_load_dwordx4 v[212:215], v7, s[28:31], s61 offen sc1\n"
    ".La8_%=:\n"
    "ds_read_b32 v28, v27 offset:24\n"
    "s_sub_u32 s65, s65, 1\n"
    "s_cmp_lt_i32 s65, 0\n"
    "s_waitcnt lgkmcnt(0)\n"
    "v_cmp_ne_u32 vcc, 0, v28\n"
    "s_cbranch_scc1 .La9_%=\n"
    "s_cbranch_vccz .La9_%=\n"
    "s_sleep 2\n"
    "s_branch .La8_%=\n"
    ".La9_%=:\n"
    "s_setprio 3\n"
    "ds_write_b128 v24, v[40:43] offset:2048\n"
    "ds_write_b128 v24, v[48:51] offset:6208\n"
    "ds_write_b128 v24, v[56:59] offset:10368\n"
    "ds_write_b128 v24, v[52:55] offset:14528\n"
    "ds_write_b128 v24, v[44:47] offset:0\n"
    "ds_write_b128 v24, v[64:67] offset:4160\n"
    "ds_write_b128 v24, v[68:71] offset:8320\n"
    "ds_write_b128 v24, v[60:63] offset:12480\n"
    ".La11_%=:\n"
    "ds_read_b32 v28, v26\n"
    "s_sub_u32 s65, s65, 1\n"
    "s_cmp_lt_i32 s65, 0\n"
    "s_waitcnt lgkmcnt(0)\n"
    "v_cmp_gt_u32 vcc, 2, v28\n"
    "s_cbranch_scc1 .La12_%=\n"
    "s_cbranch_vccnz .La11_%=\n"
    ".La12_%=:\n"
    "v_mov_b32 v60, 0\n"
    "v_mov_b32 v61, 0\n"
    "v_mov_b32 v62, 0\n"
    "v_mov_b32 v63, 0\n"
    "v_and_b32 v28, 127, v3\n"
    "v_lshl_add_u32 v28, v28, 3, s54\n"
    "ds_write_b64 v28, v[60:61]\n"
    "s_cmp_eq_u32 s51, 0\n"
    "s_cbranch_scc0 .La10_%=\n"
    "v_lshrrev_b32 v28, 2, v254\n"
    "v_mul_u32_u24 v28, 4096, v28\n"
    "v_lshl_add_u32 v28, v254, 4, v28\n"
    "v_add_u32 v28, s53, v28\n"
    "v_lshrrev_b32 v53, 16, v43\n"
    "v_mov_b32 v52, v51\n"
    "s_mov_b64 exec, 0xffff\n"
    "ds_write_b128 v28, v[60:63] offset:4096\n"
    "s_mov_b64 exec, 1\n"
    "ds_write_b32 v28, v53 offset:8256\n"
    "ds_write_b32 v28, v43 offset:12416\n"
    "ds_write_b64 v28, v[52:53] offset:16576\n"
    "s_mov_b64 exec, -1\n"
    ".La10_%=:\n"
    "s_waitcnt vmcnt(0)\n"
    "v_perm_b32 v32, v209, v208, s57\n"
    "v_perm_b32 v33, v211, v210, s57\n"
    "v_perm_b32 v34, v209, v208, s58\n"
    "v_perm_b32 v35, v211, v210, s58\n"
    "ds_write_b64 v31, v[32:33] offset:0\n"
    "ds_write_b64 v31, v[34:35] offset:5120\n"
    "v_perm_b32 v32, v213, v212, s57\n"
    "v_perm_b32 v33, v215, v214, s57\n"
    "v_perm_b32 v34, v213, v212, s58\n"
    "v_perm_b32 v35, v215, v214, s58\n"
    "ds_write_b64 v31, v[32:33] offset:2048\n"
    "ds_write_b64 v31, v[34:35] offset:7168\n"
    "v_mov_b32 v28, 3\n"
    "ds_write_b32 v25, v28\n"
    ".La13_%=:\n"
    "ds_read_b32 v28, v26\n"
    "s_sub_u32 s65, s65, 1\n"
    "s_cmp_lt_i32 s65, 0\n"
    "s_waitcnt lgkmcnt(0)\n"
    "v_cmp_gt_u32 vcc, 3, v28\n"
    "s_cbranch_scc1 .La14_%=\n"
    "s_cbranch_vccnz .La13_%=\n"
    ".La14_%=:\n"
    "ds_read_b64 v[64:65], v8 offset:4032\n"
    "ds_read_b64 v[66:67], v8 offset:4040\n"
    "ds_read_b128 v[124:127], v9 offset:4032\n"
    "ds_read_b128 v[116:119], v9 offset:3008\n"
    "ds_read_b128 v[108:111], v9 offset:1984\n"
    "ds_read_b128 v[100:103], v9 offset:960\n"
    "ds_read_b64 v[68:69], v8 offset:3520\n"
    "ds_read_b64 v[70:71], v8 offset:3528\n"
    "ds_read_b128 v[120:123], v9 offset:3520\n"
    "ds_read_b128 v[112:115], v9 offset:2496\n"
    "ds_read_b128 v[104:107], v9 offset:1472\n"
    "ds_read_b128 v[96:99], v9 offset:448\n"
    "ds_read_b64 v[72:73], v8 offset:3008\n"
    "ds_read_b64 v[74:75], v8 offset:3016\n"
    ".La15_%=:\n"
    "ds_read_b32 v28, v27 offset:16\n"
    "s_sub_u32 s65, s65, 1\n"
    "s_cmp_lt_i32 s65, 0\n"
    "s_waitcnt lgkmcnt(0)\n"
    "v_cmp_ne_u32 vcc, 0, v28\n"
    "s_cbranch_scc1 .La16_%=\n"
    "s_cbranch_vccz .La16_%=\n"
    "s_sleep 2\n"
    "s_branch .La15_%=\n"
    ".La16_%=:\n"
    "s_setprio 1\n"
    "v_mfma_f32_16x16x32_bf16 v[44:47], v[64:67], v[124:127], 0\n"
    "ds_read_b64 v[76:77], v8 offset:2496\n"
    "v_mfma_f32_16x16x32_bf16 v[40:43], v[64:67], v[116:119], 0\n"
    "ds_read_b64 v[78:79], v8 offset:2504\n"
    "v_mfma_f32_16x16x32_bf16 v[36:39], v[64:67], v[108:111], 0\n"
    "ds_read_b64 v[80:81], v8 offset:1984\n"
    "v_mfma_f32_16x16x32_bf16 v[32:35], v[64:67], v[100:103], 0\n"
    "ds_read_b64 v[82:83], v8 offset:1992\n"
    "v_mfma_f32_16x16x32_bf16 v[44:47], v[68:71], v[120:123], v[44:47]\n"
    "ds_read_b64 v[84:85], v8 offset:1472\n"
    "ds_read_b64 v[86:87], v8 offset:1480\n"
    "v_mfma_f32_16x16x32_bf16 v[40:43], v[68:71], v[112:115], v[40:43]\n"
    "ds_read_b64 v[88:89], v8 offset:960\n"
    "v_mfma_f32_16x16x32_bf16 v[36:39], v[68:71], v[104:107], v[36:39]\n"
    "ds_read_b64 v[90:91], v8 offset:968\n"
    "v_mfma_f32_16x16x32_bf16 v[32:35], v[68:71], v[96:99], v[32:35]\n"
    "ds_read_b64 v[92:93], v8 offset:448\n"
    "v_mfma_f32_16x16x32_bf16 v[44:47], v[72:75], v[116:119], v[44:47]\n"
    "ds_read_b64 v[94:95], v8 offset:456\n"
    "v_mfma_f32_16x16x32_bf16 v[40:43], v[72:75], v[108:111], v[40:43]\n"
    "ds_read_b64 v[128:129], v8 offset:3968\n"
    "ds_read_b64 v[130:131], v8 offset:3976\n"
    "v_mfma_f32_16x16x32_bf16 v[36:39], v[72:75], v[100:103], v[36:39]\n"
    "ds_read_b128 v[188:191], v9 offset:3968\n"
    "s_waitcnt lgkmcnt(11)\n"
    "v_mfma_f32_16x16x32_bf16 v[44:47], v[76:79], v[112:115], v[44:47]\n"
    "ds_read_b128 v[180:183], v9 offset:2944\n"
    "v_mfma_f32_16x16x32_bf16 v[40:43], v[76:79], v[104:107], v[40:43]\n"
    "ds_read_b128 v[172:175], v9 offset:1920\n"
    "v_mfma_f32_16x16x32_bf16 v[36:39], v[76:79], v[96:99], v[36:39]\n"
    "ds_read_b128 v[164:167], v9 offset:896\n"
    "s_waitcnt lgkmcnt(12)\n"
    "v_mfma_f32_16x16x32_bf16 v[44:47], v[80:83], v[108:111], v[44:47]\n"
    "ds_read_b64 v[132:133], v8 offset:3456\n"
    "ds_read_b64 v[134:135], v8 offset:3464\n"
    "v_mfma_f32_16x16x32_bf16 v[40:43], v[80:83], v[100:103], v[40:43]\n"
    "ds_read_b128 v[184:187], v9 offset:3456\n"
    "s_waitcnt lgkmcnt(13)\n"
    "v_mfma_f32_16x16x32_bf16 v[44:47], v[84:87], v[104:107], v[44:47]\n"
    "ds_read_b128 v[176:179], v9 offset:2432\n"
    "v_mfma_f32_16x16x32_bf16 v[40:43], v[84:87], v[96:99], v[40:43]\n"
    "ds_read_b128 v[168:171], v9 offset:1408\n"
    "s_waitcnt lgkmcnt(13)\n"
    "v_mfma_f32_16x16x32_bf16 v[44:47], v[88:91], v[100:103], v[44:47]\n"
    "ds_read_b128 v[160:163], v9 offset:384\n"
    "s_waitcnt lgkmcnt(12)\n"
    "v_mfma_f32_16x16x32_bf16 v[44:47], v[92:95], v[96:99], v[44:47]\n"
    "ds_read_b64 v[136:137], v8 offset:2944\n"
    "ds_read_b64 v[138:139], v8 offset:2952\n"
    "s_waitcnt lgkmcnt(11)\n"
    "v_mfma_f32_16x16x32_bf16 v[44:47], v[128:131], v[188:191], v[44:47]\n"
    "ds_read_b64 v[140:141], v8 offset:2432\n"
    "s_waitcnt lgkmcnt(11)\n"
    "v_mfma_f32_16x16x32_bf16 v[40:43], v[128:131], v[180:183], v[40:43]\n"
    "ds_read_b64 v[142:143], v8 offset:2440\n"
    "s_waitcnt lgkmcnt(11)\n"
    "v_mfma_f32_16x16x32_bf16 v[36:39], v[128:131], v[172:175], v[36:39]\n"
    "ds_read_b64 v[144:145], v8 offset:1920\n"
    "s_waitcnt lgkmcnt(11)\n"
    "v_mfma_f32_16x16x32_bf16 v[32:35], v[128:131], v[164:167], v[32:35]\n"
    "ds_read_b64 v[146:147], v8 offset:1928\n"
    "s_waitcnt lgkmcnt(9)\n"
    "v_mfma_f32_16x16x32_bf16 v[44:47], v[132:135], v[184:187], v[44:47]\n"
    "ds_read_b64 v[148:149], v8 offset:1408\n"
    "ds_read_b64 v[150:151], v8 offset:1416\n"
    "s_waitcnt lgkmcnt(10)\n"
    "v_mfma_f32_16x16x32_bf16 v[40:43], v[132:135], v[176:179], v[40:43]\n"
    "ds_read_b64 v[152:153], v8 offset:896\n"
    "s_waitcnt lgkmcnt(10)\n"
    "v_mfma_f32_16x16x32_bf16 v[36:39], v[132:135], v[168:171], v[36:39]\n"
    "ds_read_b64 v[154:155], v8 offset:904\n"
    "s_waitcnt lgkmcnt(10)\n"
    "v_mfma_f32_16x16x32_bf16 v[32:35], v[132:135], v[160:163], v[32:35]\n"
    "ds_read_b64 v[156:157], v8 offset:384\n"
    "s_waitcnt lgkmcnt(9)\n"
    "v_mfma_f32_16x16x32_bf16 v[44:47], v[136:139], v[180:183], v[44:47]\n"
    "ds_read_b64 v[158:159], v8 offset:392\n"
    "v_mfma_f32_16x16x32_bf16 v[40:43], v[136:139], v[172:175], v[40:43]\n"
    "ds_read_b64 v[64:65], v8 offset:3904\n"
    "ds_read_b64 v[66:67], v8 offset:3912\n"
    "v_mfma_f32_16x16x32_bf16 v[36:39], v[136:139], v[164:167], v[36:39]\n"
    "ds_read_b128 v[124:127], v9 offset:3904\n"
    "s_waitcnt lgkmcnt(11)\n"
    "v_mfma_f32_16x16x32_bf16 v[44:47], v[140:143], v[176:179], v[44:47]\n"
    "ds_read_b128 v[116:119], v9 offset:2880\n"
    "v_mfma_f32_16x16x32_bf16 v[40:43], v[140:143], v[168:171], v[40:43]\n"
    "ds_read_b128 v[108:111], v9 offset:1856\n"
    "v_mfma_f32_16x16x32_bf16 v[36:39], v[140:143], v[160:163], v[36:39]\n"
    "ds_read_b128 v[100:103], v9 offset:832\n"
    "s_waitcnt lgkmcnt(12)\n"
    "v_mfma_f32_16x16x32_bf16 v[44:47], v[144:147], v[172:175], v[44:47]\n"
    "ds_read_b64 v[68:69], v8 offset:3392\n"
    "ds_read_b64 v[70:71], v8 offset:3400\n"
    "v_mfma_f32_16x16x32_bf16 v[40:43], v[144:147], v[164:167], v[40:43]\n"
    "ds_read_b128 v[120:123], v9 offset:3392\n"
    "s_waitcnt lgkmcnt(13)\n"
    "v_mfma_f32_16x16x32_bf16 v[44:47], v[148:151], v[168:171], v[44:47]\n"
    "ds_read_b128 v[112:115], v9 offset:2368\n"
    "v_mfma_f32_16x16x32_bf16 v[40:43], v[148:151], v[160:163], v[40:43]\n"
    "ds_read_b128 v[104:107], v9 offset:1344\n"
    "s_waitcnt lgkmcnt(13)\n"
    "v_mfma_f32_16x16x32_bf16 v[44:47], v[152:155], v[164:167], v[44:47]\n"
    "ds_read_b128 v[96:99], v9 offset:320\n"
    "s_waitcnt lgkmcnt(12)\n"
    "v_mfma_f32_16x16x32_bf16 v[44:47], v[156:159], v[160:163], v[44:47]\n"
    "ds_read_b64 v[72:73], v8 offset:2880\n"
    "ds_read_b64 v[74:75], v8 offset:2888\n"
    "s_waitcnt lgkmcnt(11)\n"
    "v_mfma_f32_16x16x32_bf16 v[44:47], v[64:67], v[124:127], v[44:47]\n"
    "ds_read_b64 v[76:77], v8 offset:2368\n"
    "s_waitcnt lgkmcnt(11)\n"
    "v_mfma_f32_16x16x32_bf16 v[40:43], v[64:67], v[116:119], v[40:43]\n"
    "ds_read_b64 v[78:79], v8 offset:2376\n"
    "s_waitcnt lgkmcnt(11)\n"
    "v_mfma_f32_16x16x32_bf16 v[36:39], v[64:67], v[108:111], v[36:39]\n"
    "ds_read_b64 v[80:81], v8 offset:1856\n"
    "s_waitcnt lgkmcnt(11)\n"
    "v_mfma_f32_16x16x32_bf16 v[32:35], v[64:67], v[100:103], v[32:35]\n"
    "ds_read_b64 v[82:83], v8 offset:1864\n"
    "s_waitcnt lgkmcnt(9)\n"
    "v_mfma_f32_16x16x32_bf16 v[44:47], v[68:71], v[120:123], v[44:47]\n"
    "ds_read_b64 v[84:85], v8 offset:1344\n"
    "ds_read_b64 v[86:87], v8 offset:1352\n"
    "s_waitcnt lgkmcnt(10)\n"
    "v_mfma_f32_16x16x32_bf16 v[40:43], v[68:71], v[112:115], v[40:43]\n"
    "ds_read_b64 v[88:89], v8 offset:832\n"
    "s_waitcnt lgkmcnt(10)\n"
    "v_mfma_f32_16x16x32_bf16 v[36:39], v[68:71], v[104:107], v[36:39]\n"
    "ds_read_b64 v[90:91], v8 offset:840\n"
    "s_waitcnt lgkmcnt(10)\n"
    "v_mfma_f32_16x16x32_bf16 v[32:35], v[68:71], v[96:99], v[32:35]\n"
    "ds_read_b64 v[92:93], v8 offset:320\n"
    "s_waitcnt lgkmcnt(9)\n"
    "v_mfma_f32_16x16x32_bf16 v[44:47], v[72:75], v[116:119], v[44:47]\n"
    "ds_read_b64 v[94:95], v8 offset:328\n"
    "v_mfma_f32_16x16x32_bf16 v[40:43], v[72:75], v[108:111], v[40:43]\n"
    "ds_read_b64 v[128:129], v8 offset:3840\n"
    "ds_read_b64 v[130:131], v8 offset:3848\n"
    "v_mfma_f32_16x16x32_bf16 v[36:39], v[72:75], v[100:103], v[36:39]\n"
    "ds_read_b128 v[188:191], v9 offset:3840\n"
    "s_waitcnt lgkmcnt(11)\n"
    "v_mfma_f32_16x16x32_bf16 v[44:47], v[76:79], v[112:115], v[44:47]\n"
    "ds_read_b128 v[180:183], v9 offset:2816\n"
    "v_mfma_f32_16x16x32_bf16 v[40:43], v[76:79], v[104:107], v[40:43]\n"
    "ds_read_b128 v[172:175], v9 offset:1792\n"
    "v_mfma_f32_16x16x32_bf16 v[36:39], v[76:79], v[96:99], v[36:39]\n"
    "ds_read_b128 v[164:167], v9 offset:768\n"
    "s_waitcnt lgkmcnt(12)\n"
    "v_mfma_f32_16x16x32_bf16 v[44:47], v[80:83], v[108:111], v[44:47]\n"
    "ds_read_b64 v[132:133], v8 offset:3328\n"
    "ds_read_b64 v[134:135], v8 offset:3336\n"
    "v_mfma_f32_16x16x32_bf16 v[40:43], v[80:83], v[100:103], v[40:43]\n"
    "ds_read_b128 v[184:187], v9 offset:3328\n"
    "s_waitcnt lgkmcnt(13)\n"
    "v_mfma_f32_16x16x32_bf16 v[44:47], v[84:87], v[104:107], v[44:47]\n"
    "ds_read_b128 v[176:179], v9 offset:2304\n"
    "v_mfma_f32_16x16x32_bf16 v[40:43], v[84:87], v[96:99], v[40:43]\n"
    "ds_read_b128 v[168:171], v9 offset:1280\n"
    "s_waitcnt lgkmcnt(13)\n"
    "v_mfma_f32_16x16x32_bf16 v[44:47], v[88:91], v[100:103], v[44:47]\n"
    "ds_read_b128 v[160:163], v9 offset:256\n"
    "s_waitcnt lgkmcnt(12)\n"
    "v_mfma_f32_16x16x32_bf16 v[44:47], v[92:95], v[96:99], v[44:47]\n"
    "ds_read_b64 v[136:137], v8 offset:2816\n"
    "ds_read_b64 v[138:139], v8 offset:2824\n"
    "s_waitcnt lgkmcnt(11)\n"
    "v_mfma_f32_16x16x32_bf16 v[44:47], v[128:131], v[188:191], v[44:47]\n"
    "ds_read_b64 v[140:141], v8 offset:2304\n"
    "s_waitcnt lgkmcnt(11)\n"
    "v_mfma_f32_16x16x32_bf16 v[40:43], v[128:131], v[180:183], v[40:43]\n"
    "ds_read_b64 v[142:143], v8 offset:2312\n"
    "s_waitcnt lgkmcnt(11)\n"
    "v_mfma_f32_16x16x32_bf16 v[36:39], v[128:131], v[172:175], v[36:39]\n"
    "ds_read_b64 v[144:145], v8 offset:1792\n"
    "s_waitcnt lgkmcnt(11)\n"
    "v_mfma_f32_16x16x32_bf16 v[32:35], v[128:131], v[164:167], v[32:35]\n"
    "ds_read_b64 v[146:147], v8 offset:1800\n"
    "s_waitcnt lgkmcnt(9)\n"
    "v_mfma_f32_16x16x32_bf16 v[44:47], v[132:135], v[184:187], v[44:47]\n"
    "ds_read_b64 v[148:149], v8 offset:1280\n"
    "ds_read_b64 v[150:151], v8 offset:1288\n"
    "s_waitcnt lgkmcnt(10)\n"
    "v_mfma_f32_16x16x32_bf16 v[40:43], v[132:135], v[176:179], v[40:43]\n"
    "ds_read_b64 v[152:153], v8 offset:768\n"
    "s_waitcnt lgkmcnt(10)\n"
    "v_mfma_f32_16x16x32_bf16 v[36:39], v[132:135], v[168:171], v[36:39]\n"
    "ds_read_b64 v[154:155], v8 offset:776\n"
    "s_waitcnt lgkmcnt(10)\n"
    "v_mfma_f32_16x16x32_bf16 v[32:35], v[132:135], v[160:163], v[32:35]\n"
    "ds_read_b64 v[156:157], v8 offset:256\n"
    "s_waitcnt lgkmcnt(9)\n"
    "v_mfma_f32_16x16x32_bf16 v[44:47], v[136:139], v[180:183], v[44:47]\n"
    "ds_read_b64 v[158:159], v8 offset:264\n"
    "v_mfma_f32_16x16x32_bf16 v[40:43], v[136:139], v[172:175], v[40:43]\n"
    "ds_read_b64 v[64:65], v8 offset:3776\n"
    "ds_read_b64 v[66:67], v8 offset:3784\n"
    "v_mfma_f32_16x16x32_bf16 v[36:39], v[136:139], v[164:167], v[36:39]\n"
    "ds_read_b128 v[124:127], v9 offset:3776\n"
    "s_waitcnt lgkmcnt(11)\n"
    "v_mfma_f32_16x16x32_bf16 v[44:47], v[140:143], v[176:179], v[44:47]\n"
    "ds_read_b128 v[116:119], v9 offset:2752\n"
    "v_mfma_f32_16x16x32_bf16 v[40:43], v[140:143], v[168:171], v[40:43]\n"
    "ds_read_b128 v[108:111], v9 offset:1728\n"
    "v_mfma_f32_16x16x32_bf16 v[36:39], v[140:143], v[160:163], v[36:39]\n"
    "ds_read_b128 v[100:103], v9 offset:704\n"
    "s_waitcnt lgkmcnt(12)\n"
    "v_mfma_f32_16x16x32_bf16 v[44:47], v[144:147], v[172:175], v[44:47]\n"
    "ds_read_b64 v[68:69], v8 offset:3264\n"
    "ds_read_b64 v[70:71], v8 offset:3272\n"
    "v_mfma_f32_16x16x32_bf16 v[40:43], v[144:147], v[164:167], v[40:43]\n"
    "ds_read_b128 v[120:123], v9 offset:3264\n"
    "s_waitcnt lgkmcnt(13)\n"
    "v_mfma_f32_16x16x32_bf16 v[44:47], v[148:151], v[168:171], v[44:47]\n"
    "ds_read_b128 v[112:115], v9 offset:2240\n"
    "v_mfma_f32_16x16x32_bf16 v[40:43], v[148:151], v[160:163], v[40:43]\n"
    "ds_read_b128 v[104:107], v9 offset:1216\n"
    "s_waitcnt lgkmcnt(13)\n"
    "v_mfma_f32_16x16x32_bf16 v[44:47], v[152:155], v[164:167], v[44:47]\n"
    "ds_read_b128 v[96:99], v9 offset:192\n"
    "s_waitcnt lgkmcnt(12)\n"
    "v_mfma_f32_16x16x32_bf16 v[44:47], v[156:159], v[160:163], v[44:47]\n"
    "ds_read_b64 v[72:73], v8 offset:2752\n"
    "ds_read_b64 v[74:75], v8 offset:2760\n"
    "s_waitcnt lgkmcnt(11)\n"
    "v_mfma_f32_16x16x32_bf16 v[44:47], v[64:67], v[124:127], v[44:47]\n"
    "ds_read_b64 v[76:77], v8 offset:2240\n"
    "s_waitcnt lgkmcnt(11)\n"
    "v_mfma_f32_16x16x32_bf16 v[40:43], v[64:67], v[116:119], v[40:43]\n"
    "ds_read_b64 v[78:79], v8 offset:2248\n"
    "s_waitcnt lgkmcnt(11)\n"
    "v_mfma_f32_16x16x32_bf16 v[36:39], v[64:67], v[108:111], v[36:39]\n"
    "ds_read_b64 v[80:81], v8 offset:1728\n"
    "s_waitcnt lgkmcnt(11)\n"
    "v_mfma_f32_16x16x32_bf16 v[32:35], v[64:67], v[100:103], v[32:35]\n"
    "ds_read_b64 v[82:83], v8 offset:1736\n"
    "s_waitcnt lgkmcnt(9)\n"
    "v_mfma_f32_16x16x32_bf16 v[44:47], v[68:71], v[120:123], v[44:47]\n"
    "ds_read_b64 v[84:85], v8 offset:1216\n"
    "ds_read_b64 v[86:87], v8 offset:1224\n"
    "s_waitcnt lgkmcnt(10)\n"
    "v_mfma_f32_16x16x32_bf16 v[40:43], v[68:71], v[112:115], v[40:43]\n"
    "ds_read_b64 v[88:89], v8 offset:704\n"
    "s_waitcnt lgkmcnt(10)\n"
    "v_mfma_f32_16x16x32_bf16 v[36:39], v[68:71], v[104:107], v[36:39]\n"
    "ds_read_b64 v[90:91], v8 offset:712\n"
    "s_waitcnt lgkmcnt(10)\n"
    "v_mfma_f32_16x16x32_bf16 v[32:35], v[68:71], v[96:99], v[32:35]\n"
    "ds_read_b64 v[92:93], v8 offset:192\n"
    "s_waitcnt lgkmcnt(9)\n"
    "v_mfma_f32_16x16x32_bf16 v[44:47], v[72:75], v[116:119], v[44:47]\n"
    "ds_read_b64 v[94:95], v8 offset:200\n"
    "v_mfma_f32_16x16x32_bf16 v[40:43], v[72:75], v[108:111], v[40:43]\n"
    "ds_read_b64 v[128:129], v8 offset:3712\n"
    "ds_read_b64 v[130:131], v8 offset:3720\n"
    "v_mfma_f32_16x16x32_bf16 v[36:39], v[72:75], v[100:103], v[36:39]\n"
    "ds_read_b128 v[188:191], v9 offset:3712\n"
    "s_waitcnt lgkmcnt(11)\n"
    "v_mfma_f32_16x16x32_bf16 v[44:47], v[76:79], v[112:115], v[44:47]\n"
    "ds_read_b128 v[180:183], v9 offset:2688\n"
    "v_mfma_f32_16x16x32_bf16 v[40:43], v[76:79], v[104:107], v[40:43]\n"
    "ds_read_b128 v[172:175], v9 offset:1664\n"
    "v_mfma_f32_16x16x32_bf16 v[36:39], v[76:79], v[96:99], v[36:39]\n"
    "ds_read_b128 v[164:167], v9 offset:640\n"
    "s_waitcnt lgkmcnt(12)\n"
    "v_mfma_f32_16x16x32_bf16 v[44:47], v[80:83], v[108:111], v[44:47]\n"
    "ds_read_b64 v[132:133], v8 offset:3200\n"
    "ds_read_b64 v[134:135], v8 offset:3208\n"
    "v_mfma_f32_16x16x32_bf16 v[40:43], v[80:83], v[100:103], v[40:43]\n"
    "ds_read_b128 v[184:187], v9 offset:3200\n"
    "s_waitcnt lgkmcnt(13)\n"
    "v_mfma_f32_16x16x32_bf16 v[44:47], v[84:87], v[104:107], v[44:47]\n"
    "ds_read_b128 v[176:179], v9 offset:2176\n"
    "v_mfma_f32_16x16x32_bf16 v[40:43], v[84:87], v[96:99], v[40:43]\n"
    "ds_read_b128 v[168:171], v9 offset:1152\n"
    "s_waitcnt lgkmcnt(13)\n"
    "v_mfma_f32_16x16x32_bf16 v[44:47], v[88:91], v[100:103], v[44:47]\n"
    "ds_read_b128 v[160:163], v9 offset:128\n"
    "s_waitcnt lgkmcnt(12)\n"
    "v_mfma_f32_16x16x32_bf16 v[44:47], v[92:95], v[96:99], v[44:47]\n"
    "ds_read_b64 v[136:137], v8 offset:2688\n"
    "ds_read_b64 v[138:139], v8 offset:2696\n"
    "s_waitcnt lgkmcnt(11)\n"
    "v_mfma_f32_16x16x32_bf16 v[44:47], v[128:131], v[188:191], v[44:47]\n"
    "ds_read_b64 v[140:141], v8 offset:2176\n"
    "s_waitcnt lgkmcnt(11)\n"
    "v_mfma_f32_16x16x32_bf16 v[40:43], v[128:131], v[180:183], v[40:43]\n"
    "ds_read_b64 v[142:143], v8 offset:2184\n"
    "s_waitcnt lgkmcnt(11)\n"
    "v_mfma_f32_16x16x32_bf16 v[36:39], v[128:131], v[172:175], v[36:39]\n"
    "ds_read_b64 v[144:145], v8 offset:1664\n"
    "s_waitcnt lgkmcnt(11)\n"
    "v_mfma_f32_16x16x32_bf16 v[32:35], v[128:131], v[164:167], v[32:35]\n"
    "ds_read_b64 v[146:147], v8 offset:1672\n"
    "s_waitcnt lgkmcnt(9)\n"
    "v_mfma_f32_16x16x32_bf16 v[44:47], v[132:135], v[184:187], v[44:47]\n"
    "ds_read_b64 v[148:149], v8 offset:1152\n"
    "ds_read_b64 v[150:151], v8 offset:1160\n"
    "s_waitcnt lgkmcnt(10)\n"
    "v_mfma_f32_16x16x32_bf16 v[40:43], v[132:135], v[176:179], v[40:43]\n"
    "ds_read_b64 v[152:153], v8 offset:640\n"
    "s_waitcnt lgkmcnt(10)\n"
    "v_mfma_f32_16x16x32_bf16 v[36:39], v[132:135], v[168:171], v[36:39]\n"
    "ds_read_b64 v[154:155], v8 offset:648\n"
    "s_waitcnt lgkmcnt(10)\n"
    "v_mfma_f32_16x16x32_bf16 v[32:35], v[132:135], v[160:163], v[32:35]\n"
    "ds_read_b64 v[156:157], v8 offset:128\n"
    "s_waitcnt lgkmcnt(9)\n"
    "v_mfma_f32_16x16x32_bf16 v[44:47], v[136:139], v[180:183], v[44:47]\n"
    "ds_read_b64 v[158:159], v8 offset:136\n"
    "v_mfma_f32_16x16x32_bf16 v[40:43], v[136:139], v[172:175], v[40:43]\n"
    "ds_read_b64 v[64:65], v8 offset:3648\n"
    "ds_read_b64 v[66:67], v8 offset:3656\n"
    "v_mfma_f32_16x16x32_bf16 v[36:39], v[136:139], v[164:167], v[36:39]\n"
    "ds_read_b128 v[124:127], v9 offset:3648\n"
    "s_waitcnt lgkmcnt(11)\n"
    "v_mfma_f32_16x16x32_bf16 v[44:47], v[140:143], v[176:179], v[44:47]\n"
    "ds_read_b128 v[116:119], v9 offset:2624\n"
    "v_mfma_f32_16x16x32_bf16 v[40:43], v[140:143], v[168:171], v[40:43]\n"
    "ds_read_b128 v[108:111], v9 offset:1600\n"
    "v_mfma_f32_16x16x32_bf16 v[36:39], v[140:143], v[160:163], v[36:39]\n"
    "ds_read_b128 v[100:103], v9 offset:576\n"
    "s_waitcnt lgkmcnt(12)\n"
    "v_mfma_f32_16x16x32_bf16 v[44:47], v[144:147], v[172:175], v[44:47]\n"
    "ds_read_b64 v[68:69], v8 offset:3136\n"
    "ds_read_b64 v[70:71], v8 offset:3144\n"
    "v_mfma_f32_16x16x32_bf16 v[40:43], v[144:147], v[164:167], v[40:43]\n"
    "ds_read_b128 v[120:123], v9 offset:3136\n"
    "s_waitcnt lgkmcnt(13)\n"
    "v_mfma_f32_16x16x32_bf16 v[44:47], v[148:151], v[168:171], v[44:47]\n"
    "ds_read_b128 v[112:115], v9 offset:2112\n"
    "v_mfma_f32_16x16x32_bf16 v[40:43], v[148:151], v[160:163], v[40:43]\n"
    "ds_read_b128 v[104:107], v9 offset:1088\n"
    "s_waitcnt lgkmcnt(13)\n"
    "v_mfma_f32_16x16x32_bf16 v[44:47], v[152:155], v[164:167], v[44:47]\n"
    "ds_read_b128 v[96:99], v9 offset:64\n"
    "s_waitcnt lgkmcnt(12)\n"
    "v_mfma_f32_16x16x32_bf16 v[44:47], v[156:159], v[160:163], v[44:47]\n"
    "ds_read_b64 v[72:73], v8 offset:2624\n"
    "ds_read_b64 v[74:75], v8 offset:2632\n"
    "s_waitcnt lgkmcnt(11)\n"
    "v_mfma_f32_16x16x32_bf16 v[44:47], v[64:67], v[124:127], v[44:47]\n"
    "ds_read_b64 v[76:77], v8 offset:2112\n"
    "s_waitcnt lgkmcnt(11)\n"
    "v_mfma_f32_16x16x32_bf16 v[40:43], v[64:67], v[116:119], v[40:43]\n"
    "ds_read_b64 v[78:79], v8 offset:2120\n"
    "s_waitcnt lgkmcnt(11)\n"
    "v_mfma_f32_16x16x32_bf16 v[36:39], v[64:67], v[108:111], v[36:39]\n"
    "ds_read_b64 v[80:81], v8 offset:1600\n"
    "s_waitcnt lgkmcnt(11)\n"
    "v_mfma_f32_16x16x32_bf16 v[32:35], v[64:67], v[100:103], v[32:35]\n"
    "ds_read_b64 v[82:83], v8 offset:1608\n"
    "s_waitcnt lgkmcnt(9)\n"
    "v_mfma_f32_16x16x32_bf16 v[44:47], v[68:71], v[120:123], v[44:47]\n"
    "ds_read_b64 v[84:85], v8 offset:1088\n"
    "ds_read_b64 v[86:87], v8 offset:1096\n"
    "s_waitcnt lgkmcnt(10)\n"
    "v_mfma_f32_16x16x32_bf16 v[40:43], v[68:71], v[112:115], v[40:43]\n"
    "ds_read_b64 v[88:89], v8 offset:576\n"
    "s_waitcnt lgkmcnt(10)\n"
    "v_mfma_f32_16x16x32_bf16 v[36:39], v[68:71], v[104:107], v[36:39]\n"
    "ds_read_b64 v[90:91], v8 offset:584\n"
    "s_waitcnt lgkmcnt(10)\n"
    "v_mfma_f32_16x16x32_bf16 v[32:35], v[68:71], v[96:99], v[32:35]\n"
    "ds_read_b64 v[92:93], v8 offset:64\n"
    "s_waitcnt lgkmcnt(9)\n"
    "v_mfma_f32_16x16x32_bf16 v[44:47], v[72:75], v[116:119], v[44:47]\n"
    "ds_read_b64 v[94:95], v8 offset:72\n"
    "v_mfma_f32_16x16x32_bf16 v[40:43], v[72:75], v[108:111], v[40:43]\n"
    "ds_read_b64 v[128:129], v8 offset:3584\n"
    "ds_read_b64 v[130:131], v8 offset:3592\n"
    "v_mfma_f32_16x16x32_bf16 v[36:39], v[72:75], v[100:103], v[36:39]\n"
    "ds_read_b128 v[188:191], v9 offset:3584\n"
    "s_waitcnt lgkmcnt(11)\n"
    "v_mfma_f32_16x16x32_bf16 v[44:47], v[76:79], v[112:115], v[44:47]\n"
    "ds_read_b128 v[180:183], v9 offset:2560\n"
    "v_mfma_f32_16x16x32_bf16 v[40:43], v[76:79], v[104:107], v[40:43]\n"
    "ds_read_b128 v[172:175], v9 offset:1536\n"
    "v_mfma_f32_16x16x32_bf16 v[36:39], v[76:79], v[96:99], v[36:39]\n"
    "ds_read_b128 v[164:167], v9 offset:512\n"
    "s_waitcnt lgkmcnt(12)\n"
    "v_mfma_f32_16x16x32_bf16 v[44:47], v[80:83], v[108:111], v[44:47]\n"
    "ds_read_b64 v[132:133], v8 offset:3072\n"
    "ds_read_b64 v[134:135], v8 offset:3080\n"
    "v_mfma_f32_16x16x32_bf16 v[40:43], v[80:83], v[100:103], v[40:43]\n"
    "ds_read_b128 v[184:187], v9 offset:3072\n"
    "s_waitcnt lgkmcnt(13)\n"
    "v_mfma_f32_16x16x32_bf16 v[44:47], v[84:87], v[104:107], v[44:47]\n"
    "ds_read_b128 v[176:179], v9 offset:2048\n"
    "v_mfma_f32_16x16x32_bf16 v[40:43], v[84:87], v[96:99], v[40:43]\n"
    "ds_read_b128 v[168:171], v9 offset:1024\n"
    "s_waitcnt lgkmcnt(13)\n"
    "v_mfma_f32_16x16x32_bf16 v[44:47], v[88:91], v[100:103], v[44:47]\n"
    "ds_read_b128 v[160:163], v9 offset:0\n"
    "s_waitcnt lgkmcnt(12)\n"
    "v_mfma_f32_16x16x32_bf16 v[44:47], v[92:95], v[96:99], v[44:47]\n"
    "ds_read_b64 v[136:137], v8 offset:2560\n"
    "ds_read_b64 v[138:139], v8 offset:2568\n"
    "s_waitcnt lgkmcnt(11)\n"
    "v_mfma_f32_16x16x32_bf16 v[44:47], v[128:131], v[188:191], v[44:47]\n"
    "ds_read_b64 v[140:141], v8 offset:2048\n"
    "s_waitcnt lgkmcnt(11)\n"
    "v_mfma_f32_16x16x32_bf16 v[40:43], v[128:131], v[180:183], v[40:43]\n"
    "ds_read_b64 v[142:143], v8 offset:2056\n"
    "s_waitcnt lgkmcnt(11)\n"
    "v_mfma_f32_16x16x32_bf16 v[36:39], v[128:131], v[172:175], v[36:39]\n"
    "ds_read_b64 v[144:145], v8 offset:1536\n"
    "s_waitcnt lgkmcnt(11)\n"
    "v_mfma_f32_16x16x32_bf16 v[32:35], v[128:131], v[164:167], v[32:35]\n"
    "ds_read_b64 v[146:147], v8 offset:1544\n"
    "s_waitcnt lgkmcnt(9)\n"
    "v_mfma_f32_16x16x32_bf16 v[44:47], v[132:135], v[184:187], v[44:47]\n"
    "ds_read_b64 v[148:149], v8 offset:1024\n"
    "ds_read_b64 v[150:151], v8 offset:1032\n"
    "s_waitcnt lgkmcnt(10)\n"
    "v_mfma_f32_16x16x32_bf16 v[40:43], v[132:135], v[176:179], v[40:43]\n"
    "ds_read_b64 v[152:153], v8 offset:512\n"
    "s_waitcnt lgkmcnt(10)\n"
    "v_mfma_f32_16x16x32_bf16 v[36:39], v[132:135], v[168:171], v[36:39]\n"
    "ds_read_b64 v[154:155], v8 offset:520\n"
    "s_waitcnt lgkmcnt(10)\n"
    "v_mfma_f32_16x16x32_bf16 v[32:35], v[132:135], v[160:163], v[32:35]\n"
    "ds_read_b64 v[156:157], v8 offset:0\n"
    "s_waitcnt lgkmcnt(9)\n"
    "v_mfma_f32_16x16x32_bf16 v[44:47], v[136:139], v[180:183], v[44:47]\n"
    "ds_read_b64 v[158:159], v8 offset:8\n"
    "v_mfma_f32_16x16x32_bf16 v[40:43], v[136:139], v[172:175], v[40:43]\n"
    "v_mfma_f32_16x16x32_bf16 v[36:39], v[136:139], v[164:167], v[36:39]\n"
    "s_waitcnt lgkmcnt(8)\n"
    "v_mfma_f32_16x16x32_bf16 v[44:47], v[140:143], v[176:179], v[44:47]\n"
    "v_mfma_f32_16x16x32_bf16 v[40:43], v[140:143], v[168:171], v[40:43]\n"
    "v_mfma_f32_16x16x32_bf16 v[36:39], v[140:143], v[160:163], v[36:39]\n"
    "s_waitcnt lgkmcnt(6)\n"
    "v_mfma_f32_16x16x32_bf16 v[44:47], v[144:147], v[172:175], v[44:47]\n"
    "v_mfma_f32_16x16x32_bf16 v[40:43], v[144:147], v[164:167], v[40:43]\n"
    "s_waitcnt lgkmcnt(4)\n"
    "v_mfma_f32_16x16x32_bf16 v[44:47], v[148:151], v[168:171], v[44:47]\n"
    "v_mfma_f32_16x16x32_bf16 v[40:43], v[148:151], v[160:163], v[40:43]\n"
    "s_waitcnt lgkmcnt(2)\n"
    "v_mfma_f32_16x16x32_bf16 v[44:47], v[152:155], v[164:167], v[44:47]\n"
    "s_waitcnt lgkmcnt(0)\n"
    "v_mfma_f32_16x16x32_bf16 v[44:47], v[156:159], v[160:163], v[44:47]\n"
    "s_setprio 3\n"
    "v_lshlrev_b32 v28, 5, v1\n"
    "v_lshl_add_u32 v28, v2, 3, v28\n"
    "s_lshl_b32 s76, s50, 12\n"
    "s_add_u32 s56, s55, s76\n"
    "s_xor_b32 s76, s51, 1\n"
    "s_lshl_b32 s76, s76, 9\n"
    "s_add_u32 s56, s56, s76\n"
    "s_add_u32 s56, s56, 2883584\n"
    "v_cvt_pk_bf16_f32 v208, v32, v33\n"
    "v_cvt_pk_bf16_f32 v209, v34, v35\n"
    "buffer_store_dwordx2 v[208:209], v28, s[28:31], s56 offen offset:0 sc1\n"
    "v_cvt_pk_bf16_f32 v210, v36, v37\n"
    "v_cvt_pk_bf16_f32 v211, v38, v39\n"
    "buffer_store_dwordx2 v[210:211], v28, s[28:31], s56 offen offset:1024 sc1\n"
    "v_cvt_pk_bf16_f32 v212, v40, v41\n"
    "v_cvt_pk_bf16_f32 v213, v42, v43\n"
    "buffer_store_dwordx2 v[212:213], v28, s[28:31], s56 offen offset:2048 sc1\n"
    "v_cvt_pk_bf16_f32 v214, v44, v45\n"
    "v_cvt_pk_bf16_f32 v215, v46, v47\n"
    "buffer_store_dwordx2 v[214:215], v28, s[28:31], s56 offen offset:3072 sc1\n"
    "v_lshrrev_b32 v12, 3, v3\n"
    "v_and_b32 v13, 7, v3\n"
    "v_mul_u32_u24 v14, 896, v12\n"
    "v_lshl_add_u32 v14, v13, 4, v14\n"
    "s_mul_i32 s59, s79, 57344\n"
    "s_lshl_b32 s76, s74, 7\n"
    "s_add_u32 s59, s59, s76\n"
    "s_add_u32 s60, s59, 28672\n"
    "v_lshl_add_u32 v15, v13, 4, s76\n"
    "buffer_load_dwordx4 v[240:243], v14, s[12:15], s59 offen\n"
    "buffer_load_dwordx4 v[244:247], v14, s[12:15], s60 offen\n"
    "global_load_dwordx4 v[248:251], v15, s[24:25]\n"
    "v_lshlrev_b32 v16, 4, v13\n"
    "v_lshl_add_u32 v16, v12, 12, v16\n"
    "s_lshl_b32 s56, s74, 18\n"
    "s_lshl_b32 s76, s79, 7\n"
    "s_add_u32 s56, s56, s76\n"
    "s_add_u32 s56, s56, 2883584\n"
    "s_add_u32 s61, s56, 131072\n"
    "s_lshl_b32 s62, s74, 12\n"
    "s_add_u32 s62, s62, 8192\n"
    "v_xor_b32 v17, s5, v13\n"
    "v_lshlrev_b32 v17, 4, v17\n"
    "v_lshl_add_u32 v17, v12, 7, v17\n"
    "v_add_u32 v17, 151552, v17\n"
    "v_xor_b32 v22, 64, v17\n"
    "v_xor_b32 v18, s5, v13\n"
    "v_lshlrev_b32 v18, 4, v18\n"
    "v_lshl_add_u32 v18, v13, 10, v18\n"
    "v_and_b32 v28, 7, v12\n"
    "v_lshl_add_u32 v18, v28, 1, v18\n"
    "v_add_u32 v18, 151552, v18\n"
    "v_xor_b32 v21, 64, v18\n"
    "v_and_b32 v28, 7, v12\n"
    "v_lshlrev_b32 v19, 1, v13\n"
    "v_xor_b32 v19, v19, v28\n"
    "v_lshlrev_b32 v19, 4, v19\n"
    "v_lshl_add_u32 v19, v28, 8, v19\n"
    "s_lshl_b32 s76, s50, 14\n"
    "s_lshl_b32 s77, s51, 11\n"
    "s_add_u32 s76, s76, s77\n"
    "v_add_u32 v19, s76, v19\n"
    "v_xor_b32 v20, 16, v19\n"
    "s_mov_b32 s72, 0xbfb8aa3b\n"
    "s_mov_b32 s73, 0xbfb8aa3b\n"
    "v_mov_b32 v32, s32\n"
    "v_mov_b32 v33, s33\n"
    "v_mov_b32 v34, s33\n"
    "v_mov_b32 v35, s32\n"
    "v_lshrrev_b32 v36, 3, v3\n"
    "v_min_u32 v36, 1, v36\n"
    "v_lshl_add_u32 v36, v36, 28, v7\n"
    "s_lshl_b32 s76, s75, 7\n"
    "s_add_u32 s76, s76, 8192\n"
    "s_waitcnt vmcnt(3)\n"
    "v_mov_b32 v28, 4\n"
    "ds_write_b32 v25, v28\n"
    ".La17_%=:\n"
    "ds_read_b32 v28, v26\n"
    "s_sub_u32 s65, s65, 1\n"
    "s_cmp_lt_i32 s65, 0\n"
    "s_waitcnt lgkmcnt(0)\n"
    "v_cmp_gt_u32 vcc, 4, v28\n"
    "s_cbranch_scc1 .La18_%=\n"
    "s_cbranch_vccnz .La17_%=\n"
    ".La18_%=:\n"
    "s_cmp_eq_u32 s5, 0\n"
    "s_cbranch_scc0 .La19_%=\n"
    "buffer_store_dwordx4 v[32:35], v36, s[28:31], s76 offen sc1 ; COND\n"
    ".La19_%=:\n"
    "s_waitcnt vmcnt(0)\n"
    "v_lshlrev_b32 v80, 16, v240\n"
    "v_and_b32 v81, 0xffff0000, v240\n"
    "v_lshlrev_b32 v82, 16, v241\n"
    "v_and_b32 v83, 0xffff0000, v241\n"
    "v_lshlrev_b32 v84, 16, v242\n"
    "v_and_b32 v85, 0xffff0000, v242\n"
    "v_lshlrev_b32 v86, 16, v243\n"
    "v_and_b32 v87, 0xffff0000, v243\n"
    "v_lshlrev_b32 v88, 16, v244\n"
    "v_and_b32 v89, 0xffff0000, v244\n"
    "v_lshlrev_b32 v90, 16, v245\n"
    "v_and_b32 v91, 0xffff0000, v245\n"
    "v_lshlrev_b32 v92, 16, v246\n"
    "v_and_b32 v93, 0xffff0000, v246\n"
    "v_lshlrev_b32 v94, 16, v247\n"
    "v_and_b32 v95, 0xffff0000, v247\n"
    "v_lshlrev_b32 v96, 16, v248\n"
    "v_and_b32 v97, 0xffff0000, v248\n"
    "v_lshlrev_b32 v98, 16, v249\n"
    "v_and_b32 v99, 0xffff0000, v249\n"
    "v_lshlrev_b32 v100, 16, v250\n"
    "v_and_b32 v101, 0xffff0000, v250\n"
    "v_lshlrev_b32 v102, 16, v251\n"
    "v_and_b32 v103, 0xffff0000, v251\n"
    "v_mov_b32 v64, 0\n"
    "v_mov_b32 v65, 0\n"
    "v_mov_b32 v66, 0\n"
    "v_mov_b32 v67, 0\n"
    "v_mov_b32 v68, 0\n"
    "v_mov_b32 v69, 0\n"
    "v_mov_b32 v70, 0\n"
    "v_mov_b32 v71, 0\n"
    "v_mov_b32 v72, 0\n"
    "v_mov_b32 v73, 0\n"
    "v_mov_b32 v74, 0\n"
    "v_mov_b32 v75, 0\n"
    "v_mov_b32 v76, 0\n"
    "v_mov_b32 v77, 0\n"
    "v_mov_b32 v78, 0\n"
    "v_mov_b32 v79, 0\n"
    ".La20_%=:\n"
    "ds_read_b32 v28, v27 offset:12\n"
    "s_sub_u32 s65, s65, 1\n"
    "s_cmp_lt_i32 s65, 0\n"
    "s_waitcnt lgkmcnt(0)\n"
    "v_cmp_ne_u32 vcc, 0, v28\n"
    "s_cbranch_scc1 .La21_%=\n"
    "s_cbranch_vccz .La21_%=\n"
    "s_sleep 2\n"
    "s_branch .La20_%=\n"
    ".La21_%=:\n"
    "buffer_load_dwordx2 v[4:5], v6, s[28:31], s62 offen sc1\n"
    "s_waitcnt vmcnt(0)\n"
    "v_cmp_ne_u64 vcc, s[32:33], v[4:5]\n"
    "s_cbranch_vccnz .La22_%=\n"
    ".La23_%=:\n"
    "buffer_load_dwordx4 v[40:43], v16, s[28:31], s56 offen sc1\n"
    "buffer_load_dwordx4 v[44:47], v16, s[28:31], s61 offen sc1\n"
    "s_waitcnt vmcnt(0)\n"
    "ds_write_b128 v17, v[40:43] offset:0\n"
    "ds_write_b128 v22, v[44:47] offset:4096\n"
    "v_mov_b32 v28, 6\n"
    "ds_write_b32 v25, v28\n"
    ".La24_%=:\n"
    "ds_read_b32 v28, v26\n"
    "s_sub_u32 s65, s65, 1\n"
    "s_cmp_lt_i32 s65, 0\n"
    "s_waitcnt lgkmcnt(0)\n"
    "v_cmp_gt_u32 vcc, 6, v28\n"
    "s_cbranch_scc1 .La25_%=\n"
    "s_cbranch_vccnz .La24_%=\n"
    ".La25_%=:\n"
    "ds_read_u16_d16_hi v64, v18 offset:0\n"
    "ds_read_u16_d16_hi v65, v18 offset:128\n"
    "ds_read_u16_d16_hi v66, v18 offset:256\n"
    "ds_read_u16_d16_hi v67, v18 offset:384\n"
    "ds_read_u16_d16_hi v68, v18 offset:512\n"
    "ds_read_u16_d16_hi v69, v18 offset:640\n"
    "ds_read_u16_d16_hi v70, v18 offset:768\n"
    "ds_read_u16_d16_hi v71, v18 offset:896\n"
    "ds_read_u16_d16_hi v72, v21 offset:0\n"
    "ds_read_u16_d16_hi v73, v21 offset:128\n"
    "ds_read_u16_d16_hi v74, v21 offset:256\n"
    "ds_read_u16_d16_hi v75, v21 offset:384\n"
    "ds_read_u16_d16_hi v76, v21 offset:512\n"
    "ds_read_u16_d16_hi v77, v21 offset:640\n"
    "ds_read_u16_d16_hi v78, v21 offset:768\n"
    "ds_read_u16_d16_hi v79, v21 offset:896\n"
    "s_waitcnt lgkmcnt(0)\n"
    "s_barrier\n"
    "ds_read_b128 v[112:115], v19 offset:0\n"
    "ds_read_b128 v[116:119], v20 offset:0\n"
    "ds_read_b128 v[120:123], v19 offset:4096\n"
    "ds_read_b128 v[124:127], v20 offset:4096\n"
    "ds_read_b128 v[128:131], v19 offset:8192\n"
    "ds_read_b128 v[132:135], v20 offset:8192\n"
    "ds_read_b128 v[136:139], v19 offset:12288\n"
    "ds_read_b128 v[140:143], v20 offset:12288\n"
    "s_waitcnt lgkmcnt(6)\n"
    "v_pk_add_f32 v[112:113], v[112:113], v[96:97]\n"
    "v_pk_add_f32 v[114:115], v[114:115], v[98:99]\n"
    "v_pk_add_f32 v[116:117], v[116:117], v[100:101]\n"
    "v_pk_add_f32 v[118:119], v[118:119], v[102:103]\n"
    "s_waitcnt lgkmcnt(4)\n"
    "v_pk_add_f32 v[112:113], v[112:113], v[120:121]\n"
    "v_pk_add_f32 v[114:115], v[114:115], v[122:123]\n"
    "v_pk_add_f32 v[116:117], v[116:117], v[124:125]\n"
    "v_pk_add_f32 v[118:119], v[118:119], v[126:127]\n"
    "s_waitcnt lgkmcnt(2)\n"
    "v_pk_add_f32 v[112:113], v[112:113], v[128:129]\n"
    "v_pk_add_f32 v[114:115], v[114:115], v[130:131]\n"
    "v_pk_add_f32 v[116:117], v[116:117], v[132:133]\n"
    "v_pk_add_f32 v[118:119], v[118:119], v[134:135]\n"
    "s_waitcnt lgkmcnt(0)\n"
    "v_pk_add_f32 v[112:113], v[112:113], v[136:137]\n"
    "v_pk_add_f32 v[114:115], v[114:115], v[138:139]\n"
    "v_pk_add_f32 v[116:117], v[116:117], v[140:141]\n"
    "v_pk_add_f32 v[118:119], v[118:119], v[142:143]\n"
    "v_pk_mul_f32 v[120:121], v[112:113], s[72:73]\n"
    "v_pk_mul_f32 v[122:123], v[114:115], s[72:73]\n"
    "v_pk_mul_f32 v[124:125], v[116:117], s[72:73]\n"
    "v_pk_mul_f32 v[126:127], v[118:119], s[72:73]\n"
    "v_exp_f32 v120, v120\n"
    "v_exp_f32 v121, v121\n"
    "v_exp_f32 v122, v122\n"
    "v_exp_f32 v123, v123\n"
    "v_exp_f32 v124, v124\n"
    "v_exp_f32 v125, v125\n"
    "v_exp_f32 v126, v126\n"
    "v_exp_f32 v127, v127\n"
    "v_pk_mul_f32 v[112:113], v[112:113], v[64:65]\n"
    "v_pk_mul_f32 v[114:115], v[114:115], v[66:67]\n"
    "v_pk_mul_f32 v[116:117], v[116:117], v[68:69]\n"
    "v_pk_mul_f32 v[118:119], v[118:119], v[70:71]\n"
    "v_pk_add_f32 v[120:121], v[120:121], 1.0 op_sel_hi:[1,0]\n"
    "v_pk_add_f32 v[122:123], v[122:123], 1.0 op_sel_hi:[1,0]\n"
    "v_pk_add_f32 v[124:125], v[124:125], 1.0 op_sel_hi:[1,0]\n"
    "v_pk_add_f32 v[126:127], v[126:127], 1.0 op_sel_hi:[1,0]\n"
    "v_rcp_f32 v120, v120\n"
    "v_rcp_f32 v121, v121\n"
    "v_rcp_f32 v122, v122\n"
    "v_rcp_f32 v123, v123\n"
    "v_rcp_f32 v124, v124\n"
    "v_rcp_f32 v125, v125\n"
    "v_rcp_f32 v126, v126\n"
    "v_rcp_f32 v127, v127\n"
    "v_pk_fma_f32 v[112:113], v[112:113], v[120:121], v[80:81]\n"
    "v_pk_fma_f32 v[114:115], v[114:115], v[122:123], v[82:83]\n"
    "v_pk_fma_f32 v[116:117], v[116:117], v[124:125], v[84:85]\n"
    "v_pk_fma_f32 v[118:119], v[118:119], v[126:127], v[86:87]\n"
    "v_cvt_pk_bf16_f32 v128, v112, v113\n"
    "v_cvt_pk_bf16_f32 v129, v114, v115\n"
    "v_cvt_pk_bf16_f32 v130, v116, v117\n"
    "v_cvt_pk_bf16_f32 v131, v118, v119\n"
    "buffer_store_dwordx4 v[128:131], v14, s[20:23], s59 offen sc1\n"
    "s_barrier\n"
    "ds_read_b128 v[144:147], v19 offset:32768\n"
    "ds_read_b128 v[148:151], v20 offset:32768\n"
    "ds_read_b128 v[152:155], v19 offset:36864\n"
    "ds_read_b128 v[156:159], v20 offset:36864\n"
    "ds_read_b128 v[160:163], v19 offset:40960\n"
    "ds_read_b128 v[164:167], v20 offset:40960\n"
    "ds_read_b128 v[168:171], v19 offset:45056\n"
    "ds_read_b128 v[172:175], v20 offset:45056\n"
    "s_waitcnt lgkmcnt(6)\n"
    "v_pk_add_f32 v[144:145], v[144:145], v[96:97]\n"
    "v_pk_add_f32 v[146:147], v[146:147], v[98:99]\n"
    "v_pk_add_f32 v[148:149], v[148:149], v[100:101]\n"
    "v_pk_add_f32 v[150:151], v[150:151], v[102:103]\n"
    "s_waitcnt lgkmcnt(4)\n"
    "v_pk_add_f32 v[144:145], v[144:145], v[152:153]\n"
    "v_pk_add_f32 v[146:147], v[146:147], v[154:155]\n"
    "v_pk_add_f32 v[148:149], v[148:149], v[156:157]\n"
    "v_pk_add_f32 v[150:151], v[150:151], v[158:159]\n"
    "s_waitcnt lgkmcnt(2)\n"
    "v_pk_add_f32 v[144:145], v[144:145], v[160:161]\n"
    "v_pk_add_f32 v[146:147], v[146:147], v[162:163]\n"
    "v_pk_add_f32 v[148:149], v[148:149], v[164:165]\n"
    "v_pk_add_f32 v[150:151], v[150:151], v[166:167]\n"
    "s_waitcnt lgkmcnt(0)\n"
    "v_pk_add_f32 v[144:145], v[144:145], v[168:169]\n"
    "v_pk_add_f32 v[146:147], v[146:147], v[170:171]\n"
    "v_pk_add_f32 v[148:149], v[148:149], v[172:173]\n"
    "v_pk_add_f32 v[150:151], v[150:151], v[174:175]\n"
    "v_pk_mul_f32 v[152:153], v[144:145], s[72:73]\n"
    "v_pk_mul_f32 v[154:155], v[146:147], s[72:73]\n"
    "v_pk_mul_f32 v[156:157], v[148:149], s[72:73]\n"
    "v_pk_mul_f32 v[158:159], v[150:151], s[72:73]\n"
    "v_exp_f32 v152, v152\n"
    "v_exp_f32 v153, v153\n"
    "v_exp_f32 v154, v154\n"
    "v_exp_f32 v155, v155\n"
    "v_exp_f32 v156, v156\n"
    "v_exp_f32 v157, v157\n"
    "v_exp_f32 v158, v158\n"
    "v_exp_f32 v159, v159\n"
    "v_pk_mul_f32 v[144:145], v[144:145], v[72:73]\n"
    "v_pk_mul_f32 v[146:147], v[146:147], v[74:75]\n"
    "v_pk_mul_f32 v[148:149], v[148:149], v[76:77]\n"
    "v_pk_mul_f32 v[150:151], v[150:151], v[78:79]\n"
    "v_pk_add_f32 v[152:153], v[152:153], 1.0 op_sel_hi:[1,0]\n"
    "v_pk_add_f32 v[154:155], v[154:155], 1.0 op_sel_hi:[1,0]\n"
    "v_pk_add_f32 v[156:157], v[156:157], 1.0 op_sel_hi:[1,0]\n"
    "v_pk_add_f32 v[158:159], v[158:159], 1.0 op_sel_hi:[1,0]\n"
    "v_rcp_f32 v152, v152\n"
    "v_rcp_f32 v153, v153\n"
    "v_rcp_f32 v154, v154\n"
    "v_rcp_f32 v155, v155\n"
    "v_rcp_f32 v156, v156\n"
    "v_rcp_f32 v157, v157\n"
    "v_rcp_f32 v158, v158\n"
    "v_rcp_f32 v159, v159\n"
    "v_pk_fma_f32 v[144:145], v[144:145], v[152:153], v[88:89]\n"
    "v_pk_fma_f32 v[146:147], v[146:147], v[154:155], v[90:91]\n"
    "v_pk_fma_f32 v[148:149], v[148:149], v[156:157], v[92:93]\n"
    "v_pk_fma_f32 v[150:151], v[150:151], v[158:159], v[94:95]\n"
    "v_cvt_pk_bf16_f32 v160, v144, v145\n"
    "v_cvt_pk_bf16_f32 v161, v146, v147\n"
    "v_cvt_pk_bf16_f32 v162, v148, v149\n"
    "v_cvt_pk_bf16_f32 v163, v150, v151\n"
    "buffer_store_dwordx4 v[160:163], v14, s[20:23], s60 offen sc1\n"
    "s_endpgm\n"
    "; RETRY_BEGIN\n"
    ".La6_%=:\n"
    "s_sleep 8\n"
    "buffer_load_dwordx2 v[4:5], v6, s[28:31], s62 offen sc1\n"
    "s_waitcnt vmcnt(0)\n"
    "v_cmp_ne_u64 vcc, s[32:33], v[4:5]\n"
    "s_cbranch_vccz .La7_%=\n"
    "s_sub_u32 s65, s65, 1\n"
    "s_cmp_lt_i32 s65, 0\n"
    "s_cbranch_scc0 .La6_%=\n"
    "s_branch .La7_%=\n"
    ".La22_%=:\n"
    "s_sleep 8\n"
    "buffer_load_dwordx2 v[4:5], v6, s[28:31], s62 offen sc1\n"
    "s_waitcnt vmcnt(0)\n"
    "v_cmp_ne_u64 vcc, s[32:33], v[4:5]\n"
    "s_cbranch_vccz .La23_%=\n"
    "s_sub_u32 s65, s65, 1\n"
    "s_cmp_lt_i32 s65, 0\n"
    "s_cbranch_scc0 .La22_%=\n"
    "s_branch .La23_%=\n"
    "; RETRY_END\n"
    ".LH_%=:\n"
    "s_load_dwordx2 s[12:13], %0, 0x0 ; ROLE_START\n"
    "s_load_dwordx2 s[28:29], %0, 0x20\n"
    "s_mov_b32 s30, 12058624\n"
    "s_mov_b32 s31, 0x20000\n"
    "s_cmp_lg_u32 s4, 7\n"
    "s_cbranch_scc1 .La51_%=\n"
    "s_and_b32 s76, %1, 0xf8\n"
    "v_add_lshl_u32 v1, s76, v254, 7\n"
    "v_min_u32 v1, .Lend_%=-.Lpc_%=-4, v1\n"
    "s_mov_b64 exec, 0xff\n"
    "global_load_dword v2, v1, s[6:7] ; COND\n"
    "s_mov_b64 exec, -1\n"
    ".La51_%=:\n"
    "s_and_b32 s71, %1, 7\n"
    "s_lshr_b32 s72, %1, 3\n"
    "s_lshl2_add_u32 s71, s71, s72\n"
    "s_lshl3_add_u32 s76, s71, s4\n"
    "s_mul_i32 s77, s76, 7168\n"
    "s_lshl_b32 s76, s76, 7\n"
    "s_add_u32 s69, s76, 491520\n"
    "v_lshrrev_b32 v4, 3, v254\n"
    "v_and_b32 v5, 7, v254\n"
    "v_mul_u32_u24 v6, 896, v4\n"
    "v_lshl_add_u32 v6, v5, 4, v6\n"
    "s_mov_b32 s14, 1835008\n"
    "s_mov_b32 s15, 0x20000\n"
    "s_waitcnt lgkmcnt(0)\n"
    "buffer_load_dwordx4 v[32:35], v6, s[12:15], s77 offen offset:0\n"
    "buffer_load_dwordx4 v[36:39], v6, s[12:15], s77 offen offset:128\n"
    "buffer_load_dwordx4 v[40:43], v6, s[12:15], s77 offen offset:256\n"
    "buffer_load_dwordx4 v[44:47], v6, s[12:15], s77 offen offset:384\n"
    "buffer_load_dwordx4 v[48:51], v6, s[12:15], s77 offen offset:512\n"
    "buffer_load_dwordx4 v[52:55], v6, s[12:15], s77 offen offset:640\n"
    "buffer_load_dwordx4 v[56:59], v6, s[12:15], s77 offen offset:768\n"
    "s_load_dwordx2 s[32:33], s[28:29], 0x0\n"
    "v_lshlrev_b32 v4, 4, v4\n"
    "v_lshl_add_u32 v4, v5, 28, v4\n"
    "v_mov_b32 v7, 0x3f803f80\n"
    "v_mov_b32 v8, 0\n"
    "v_mov_b32 v9, 0\n"
    "v_lshrrev_b32 v16, 3, %2\n"
    "v_bfe_u32 v17, v16, 2, 3\n"
    "v_xor_b32 v17, v17, v5\n"
    "v_mul_u32_u24 v18, 896, v16\n"
    "v_lshl_add_u32 v17, v17, 4, v18\n"
    "v_lshlrev_b32 v16, 3, v16\n"
    "s_waitcnt vmcnt(6)\n"
    "ds_write_b128 v17, v[32:35] offset:0\n"
    "v_dot2c_f32_bf16 v8, v7, v32\n"
    "v_dot2c_f32_bf16 v9, v32, v32\n"
    "v_dot2c_f32_bf16 v8, v7, v33\n"
    "v_dot2c_f32_bf16 v9, v33, v33\n"
    "v_dot2c_f32_bf16 v8, v7, v34\n"
    "v_dot2c_f32_bf16 v9, v34, v34\n"
    "v_dot2c_f32_bf16 v8, v7, v35\n"
    "v_dot2c_f32_bf16 v9, v35, v35\n"
    "s_waitcnt vmcnt(5)\n"
    "ds_write_b128 v17, v[36:39] offset:128\n"
    "v_dot2c_f32_bf16 v8, v7, v36\n"
    "v_dot2c_f32_bf16 v9, v36, v36\n"
    "v_dot2c_f32_bf16 v8, v7, v37\n"
    "v_dot2c_f32_bf16 v9, v37, v37\n"
    "v_dot2c_f32_bf16 v8, v7, v38\n"
    "v_dot2c_f32_bf16 v9, v38, v38\n"
    "v_dot2c_f32_bf16 v8, v7, v39\n"
    "v_dot2c_f32_bf16 v9, v39, v39\n"
    "s_waitcnt vmcnt(4)\n"
    "ds_write_b128 v17, v[40:43] offset:256\n"
    "v_dot2c_f32_bf16 v8, v7, v40\n"
    "v_dot2c_f32_bf16 v9, v40, v40\n"
    "v_dot2c_f32_bf16 v8, v7, v41\n"
    "v_dot2c_f32_bf16 v9, v41, v41\n"
    "v_dot2c_f32_bf16 v8, v7, v42\n"
    "v_dot2c_f32_bf16 v9, v42, v42\n"
    "v_dot2c_f32_bf16 v8, v7, v43\n"
    "v_dot2c_f32_bf16 v9, v43, v43\n"
    "s_waitcnt vmcnt(3)\n"
    "ds_write_b128 v17, v[44:47] offset:384\n"
    "v_dot2c_f32_bf16 v8, v7, v44\n"
    "v_dot2c_f32_bf16 v9, v44, v44\n"
    "v_dot2c_f32_bf16 v8, v7, v45\n"
    "v_dot2c_f32_bf16 v9, v45, v45\n"
    "v_dot2c_f32_bf16 v8, v7, v46\n"
    "v_dot2c_f32_bf16 v9, v46, v46\n"
    "v_dot2c_f32_bf16 v8, v7, v47\n"
    "v_dot2c_f32_bf16 v9, v47, v47\n"
    "s_waitcnt vmcnt(2)\n"
    "ds_write_b128 v17, v[48:51] offset:512\n"
    "v_dot2c_f32_bf16 v8, v7, v48\n"
    "v_dot2c_f32_bf16 v9, v48, v48\n"
    "v_dot2c_f32_bf16 v8, v7, v49\n"
    "v_dot2c_f32_bf16 v9, v49, v49\n"
    "v_dot2c_f32_bf16 v8, v7, v50\n"
    "v_dot2c_f32_bf16 v9, v50, v50\n"
    "v_dot2c_f32_bf16 v8, v7, v51\n"
    "v_dot2c_f32_bf16 v9, v51, v51\n"
    "s_waitcnt vmcnt(1)\n"
    "ds_write_b128 v17, v[52:55] offset:640\n"
    "v_dot2c_f32_bf16 v8, v7, v52\n"
    "v_dot2c_f32_bf16 v9, v52, v52\n"
    "v_dot2c_f32_bf16 v8, v7, v53\n"
    "v_dot2c_f32_bf16 v9, v53, v53\n"
    "v_dot2c_f32_bf16 v8, v7, v54\n"
    "v_dot2c_f32_bf16 v9, v54, v54\n"
    "v_dot2c_f32_bf16 v8, v7, v55\n"
    "v_dot2c_f32_bf16 v9, v55, v55\n"
    "s_waitcnt vmcnt(0)\n"
    "ds_write_b128 v17, v[56:59] offset:768\n"
    "v_dot2c_f32_bf16 v8, v7, v56\n"
    "v_dot2c_f32_bf16 v9, v56, v56\n"
    "v_dot2c_f32_bf16 v8, v7, v57\n"
    "v_dot2c_f32_bf16 v9, v57, v57\n"
    "v_dot2c_f32_bf16 v8, v7, v58\n"
    "v_dot2c_f32_bf16 v9, v58, v58\n"
    "v_dot2c_f32_bf16 v8, v7, v59\n"
    "v_dot2c_f32_bf16 v9, v59, v59\n"
    "s_waitcnt lgkmcnt(0)\n"
    "s_add_u32 s32, s32, 1\n"
    "s_addc_u32 s33, s33, 0\n"
    "v_add_f32_dpp v8, v8, v8 quad_perm:[1,0,3,2] row_mask:0xf bank_mask:0xf bound_ctrl:1\n"
    "v_add_f32_dpp v9, v9, v9 quad_perm:[1,0,3,2] row_mask:0xf bank_mask:0xf bound_ctrl:1\n"
    "s_nop 0\n"
    "v_add_f32_dpp v8, v8, v8 quad_perm:[2,3,0,1] row_mask:0xf bank_mask:0xf bound_ctrl:1\n"
    "v_add_f32_dpp v9, v9, v9 quad_perm:[2,3,0,1] row_mask:0xf bank_mask:0xf bound_ctrl:1\n"
    "s_nop 0\n"
    "v_add_f32_dpp v8, v8, v8 row_half_mirror row_mask:0xf bank_mask:0xf bound_ctrl:1\n"
    "v_add_f32_dpp v9, v9, v9 row_half_mirror row_mask:0xf bank_mask:0xf bound_ctrl:1\n"
    "s_nop 0\n"
    "v_mul_f32 v8, 0xbb124925, v8\n"
    "v_mul_f32 v9, 0x3b124925, v9\n"
    "v_fma_f32 v9, -v8, v8, v9\n"
    "v_max_f32 v9, 0, v9\n"
    "v_cvt_pk_bf16_f32 v10, v9, v9\n"
    "v_lshlrev_b32 v10, 16, v10\n"
    "v_add_f32 v10, 0x3727c5ac, v10\n"
    "v_cvt_pk_bf16_f32 v10, v10, v10\n"
    "v_lshlrev_b32 v10, 16, v10\n"
    "v_sqrt_f32 v10, v10\n"
    "v_mov_b32 v12, s32\n"
    "v_cvt_pk_bf16_f32 v10, v10, v10\n"
    "v_lshlrev_b32 v10, 16, v10\n"
    "v_rcp_f32 v11, v10\n"
    "v_cvt_pk_bf16_f32 v13, v8, v8\n"
    "v_lshlrev_b32 v13, 16, v13\n"
    "v_mov_b32 v18, v11\n"
    "v_mul_f32 v19, v13, v11\n"
    "ds_write_b64 v16, v[18:19] offset:57344\n"
    "v_sub_f32 v13, v8, v13\n"
    "v_cvt_pk_bf16_f32 v13, v8, v13\n"
    "v_mul_f32 v9, v9, v11\n"
    "v_mul_f32 v9, v9, v11\n"
    "v_cvt_pk_bf16_f32 v9, v9, v9\n"
    "v_lshlrev_b32 v9, 16, v9\n"
    "v_add_f32 v9, 0x3727c5ac, v9\n"
    "v_cvt_pk_bf16_f32 v9, v9, v9\n"
    "v_lshlrev_b32 v9, 16, v9\n"
    "v_sqrt_f32 v9, v9\n"
    "v_mov_b32 v14, s33\n"
    "v_cvt_pk_bf16_f32 v15, v10, v9\n"
    "buffer_store_dwordx4 v[12:15], v4, s[28:31], s69 offen sc1\n"
    "s_load_dwordx2 s[24:25], %0, 0x18\n"
    "v_and_b32 v4, 15, v254\n"
    "v_lshrrev_b32 v5, 4, v254\n"
    "v_and_b32 v6, 7, v254\n"
    "v_xor_b32 v6, v6, v5\n"
    "v_mul_u32_u24 v7, 3584, v4\n"
    "v_lshl_add_u32 v6, v6, 4, v7\n"
    "s_lshl_b32 s76, s4, 7\n"
    "v_add_u32 v6, s76, v6\n"
    "v_xor_b32 v20, 64, v6\n"
    "v_lshlrev_b32 v7, 4, v4\n"
    "v_lshl_add_u32 v5, v5, 15, v7\n"
    "v_lshlrev_b32 v7, 5, v4\n"
    "s_lshl_b32 s77, s4, 18\n"
    "s_lshl_b32 s76, s71, 8\n"
    "s_add_u32 s77, s77, s76\n"
    "s_add_u32 s77, s77, 1048576\n"
    "s_cmp_eq_u32 s4, 7\n"
    "s_waitcnt lgkmcnt(0)\n"
    "s_barrier\n"
    "s_cbranch_scc1 .La52_%=\n"
    "ds_read_b128 v[80:83], v7 offset:57344\n"
    "ds_read_b128 v[84:87], v7 offset:57360\n"
    "ds_read_b128 v[32:35], v6 offset:0\n"
    "ds_read_b128 v[36:39], v6 offset:896\n"
    "ds_read_b128 v[40:43], v6 offset:1792\n"
    "ds_read_b128 v[44:47], v6 offset:2688\n"
    "ds_read_b128 v[48:51], v20 offset:0\n"
    "ds_read_b128 v[52:55], v20 offset:896\n"
    "ds_read_b128 v[56:59], v20 offset:1792\n"
    "ds_read_b128 v[60:63], v20 offset:2688\n"
    "s_waitcnt lgkmcnt(4)\n"
    "v_lshlrev_b32 v88, 16, v32\n"
    "v_and_b32 v89, 0xffff0000, v32\n"
    "v_lshlrev_b32 v90, 16, v36\n"
    "v_and_b32 v91, 0xffff0000, v36\n"
    "v_lshlrev_b32 v92, 16, v40\n"
    "v_and_b32 v93, 0xffff0000, v40\n"
    "v_lshlrev_b32 v94, 16, v44\n"
    "v_and_b32 v95, 0xffff0000, v44\n"
    "v_pk_fma_f32 v[88:89], v[88:89], v[80:81], v[80:81] op_sel:[0,0,1] op_sel_hi:[1,0,1]\n"
    "v_pk_fma_f32 v[90:91], v[90:91], v[82:83], v[82:83] op_sel:[0,0,1] op_sel_hi:[1,0,1]\n"
    "v_pk_fma_f32 v[92:93], v[92:93], v[84:85], v[84:85] op_sel:[0,0,1] op_sel_hi:[1,0,1]\n"
    "v_pk_fma_f32 v[94:95], v[94:95], v[86:87], v[86:87] op_sel:[0,0,1] op_sel_hi:[1,0,1]\n"
    "v_cvt_pk_bf16_f32 v64, v88, v89\n"
    "v_cvt_pk_bf16_f32 v65, v90, v91\n"
    "v_cvt_pk_bf16_f32 v66, v92, v93\n"
    "v_cvt_pk_bf16_f32 v67, v94, v95\n"
    "buffer_store_dwordx4 v[64:67], v5, s[28:31], s77 offen sc1\n"
    "s_add_u32 s77, s77, 8192\n"
    "v_lshlrev_b32 v88, 16, v33\n"
    "v_and_b32 v89, 0xffff0000, v33\n"
    "v_lshlrev_b32 v90, 16, v37\n"
    "v_and_b32 v91, 0xffff0000, v37\n"
    "v_lshlrev_b32 v92, 16, v41\n"
    "v_and_b32 v93, 0xffff0000, v41\n"
    "v_lshlrev_b32 v94, 16, v45\n"
    "v_and_b32 v95, 0xffff0000, v45\n"
    "v_pk_fma_f32 v[88:89], v[88:89], v[80:81], v[80:81] op_sel:[0,0,1] op_sel_hi:[1,0,1]\n"
    "v_pk_fma_f32 v[90:91], v[90:91], v[82:83], v[82:83] op_sel:[0,0,1] op_sel_hi:[1,0,1]\n"
    "v_pk_fma_f32 v[92:93], v[92:93], v[84:85], v[84:85] op_sel:[0,0,1] op_sel_hi:[1,0,1]\n"
    "v_pk_fma_f32 v[94:95], v[94:95], v[86:87], v[86:87] op_sel:[0,0,1] op_sel_hi:[1,0,1]\n"
    "v_cvt_pk_bf16_f32 v68, v88, v89\n"
    "v_cvt_pk_bf16_f32 v69, v90, v91\n"
    "v_cvt_pk_bf16_f32 v70, v92, v93\n"
    "v_cvt_pk_bf16_f32 v71, v94, v95\n"
    "buffer_store_dwordx4 v[68:71], v5, s[28:31], s77 offen sc1\n"
    "s_add_u32 s77, s77, 8192\n"
    "v_lshlrev_b32 v88, 16, v34\n"
    "v_and_b32 v89, 0xffff0000, v34\n"
    "v_lshlrev_b32 v90, 16, v38\n"
    "v_and_b32 v91, 0xffff0000, v38\n"
    "v_lshlrev_b32 v92, 16, v42\n"
    "v_and_b32 v93, 0xffff0000, v42\n"
    "v_lshlrev_b32 v94, 16, v46\n"
    "v_and_b32 v95, 0xffff0000, v46\n"
    "v_pk_fma_f32 v[88:89], v[88:89], v[80:81], v[80:81] op_sel:[0,0,1] op_sel_hi:[1,0,1]\n"
    "v_pk_fma_f32 v[90:91], v[90:91], v[82:83], v[82:83] op_sel:[0,0,1] op_sel_hi:[1,0,1]\n"
    "v_pk_fma_f32 v[92:93], v[92:93], v[84:85], v[84:85] op_sel:[0,0,1] op_sel_hi:[1,0,1]\n"
    "v_pk_fma_f32 v[94:95], v[94:95], v[86:87], v[86:87] op_sel:[0,0,1] op_sel_hi:[1,0,1]\n"
    "v_cvt_pk_bf16_f32 v72, v88, v89\n"
    "v_cvt_pk_bf16_f32 v73, v90, v91\n"
    "v_cvt_pk_bf16_f32 v74, v92, v93\n"
    "v_cvt_pk_bf16_f32 v75, v94, v95\n"
    "buffer_store_dwordx4 v[72:75], v5, s[28:31], s77 offen sc1\n"
    "s_add_u32 s77, s77, 8192\n"
    "v_lshlrev_b32 v88, 16, v35\n"
    "v_and_b32 v89, 0xffff0000, v35\n"
    "v_lshlrev_b32 v90, 16, v39\n"
    "v_and_b32 v91, 0xffff0000, v39\n"
    "v_lshlrev_b32 v92, 16, v43\n"
    "v_and_b32 v93, 0xffff0000, v43\n"
    "v_lshlrev_b32 v94, 16, v47\n"
    "v_and_b32 v95, 0xffff0000, v47\n"
    "v_pk_fma_f32 v[88:89], v[88:89], v[80:81], v[80:81] op_sel:[0,0,1] op_sel_hi:[1,0,1]\n"
    "v_pk_fma_f32 v[90:91], v[90:91], v[82:83], v[82:83] op_sel:[0,0,1] op_sel_hi:[1,0,1]\n"
    "v_pk_fma_f32 v[92:93], v[92:93], v[84:85], v[84:85] op_sel:[0,0,1] op_sel_hi:[1,0,1]\n"
    "v_pk_fma_f32 v[94:95], v[94:95], v[86:87], v[86:87] op_sel:[0,0,1] op_sel_hi:[1,0,1]\n"
    "v_cvt_pk_bf16_f32 v76, v88, v89\n"
    "v_cvt_pk_bf16_f32 v77, v90, v91\n"
    "v_cvt_pk_bf16_f32 v78, v92, v93\n"
    "v_cvt_pk_bf16_f32 v79, v94, v95\n"
    "buffer_store_dwordx4 v[76:79], v5, s[28:31], s77 offen sc1\n"
    "s_add_u32 s77, s77, 106496\n"
    "s_waitcnt lgkmcnt(0)\n"
    "v_lshlrev_b32 v88, 16, v48\n"
    "v_and_b32 v89, 0xffff0000, v48\n"
    "v_lshlrev_b32 v90, 16, v52\n"
    "v_and_b32 v91, 0xffff0000, v52\n"
    "v_lshlrev_b32 v92, 16, v56\n"
    "v_and_b32 v93, 0xffff0000, v56\n"
    "v_lshlrev_b32 v94, 16, v60\n"
    "v_and_b32 v95, 0xffff0000, v60\n"
    "v_pk_fma_f32 v[88:89], v[88:89], v[80:81], v[80:81] op_sel:[0,0,1] op_sel_hi:[1,0,1]\n"
    "v_pk_fma_f32 v[90:91], v[90:91], v[82:83], v[82:83] op_sel:[0,0,1] op_sel_hi:[1,0,1]\n"
    "v_pk_fma_f32 v[92:93], v[92:93], v[84:85], v[84:85] op_sel:[0,0,1] op_sel_hi:[1,0,1]\n"
    "v_pk_fma_f32 v[94:95], v[94:95], v[86:87], v[86:87] op_sel:[0,0,1] op_sel_hi:[1,0,1]\n"
    "v_cvt_pk_bf16_f32 v64, v88, v89\n"
    "v_cvt_pk_bf16_f32 v65, v90, v91\n"
    "v_cvt_pk_bf16_f32 v66, v92, v93\n"
    "v_cvt_pk_bf16_f32 v67, v94, v95\n"
    "buffer_store_dwordx4 v[64:67], v5, s[28:31], s77 offen sc1\n"
    "s_add_u32 s77, s77, 8192\n"
    "v_lshlrev_b32 v88, 16, v49\n"
    "v_and_b32 v89, 0xffff0000, v49\n"
    "v_lshlrev_b32 v90, 16, v53\n"
    "v_and_b32 v91, 0xffff0000, v53\n"
    "v_lshlrev_b32 v92, 16, v57\n"
    "v_and_b32 v93, 0xffff0000, v57\n"
    "v_lshlrev_b32 v94, 16, v61\n"
    "v_and_b32 v95, 0xffff0000, v61\n"
    "v_pk_fma_f32 v[88:89], v[88:89], v[80:81], v[80:81] op_sel:[0,0,1] op_sel_hi:[1,0,1]\n"
    "v_pk_fma_f32 v[90:91], v[90:91], v[82:83], v[82:83] op_sel:[0,0,1] op_sel_hi:[1,0,1]\n"
    "v_pk_fma_f32 v[92:93], v[92:93], v[84:85], v[84:85] op_sel:[0,0,1] op_sel_hi:[1,0,1]\n"
    "v_pk_fma_f32 v[94:95], v[94:95], v[86:87], v[86:87] op_sel:[0,0,1] op_sel_hi:[1,0,1]\n"
    "v_cvt_pk_bf16_f32 v68, v88, v89\n"
    "v_cvt_pk_bf16_f32 v69, v90, v91\n"
    "v_cvt_pk_bf16_f32 v70, v92, v93\n"
    "v_cvt_pk_bf16_f32 v71, v94, v95\n"
    "buffer_store_dwordx4 v[68:71], v5, s[28:31], s77 offen sc1\n"
    "s_add_u32 s77, s77, 8192\n"
    "v_lshlrev_b32 v88, 16, v50\n"
    "v_and_b32 v89, 0xffff0000, v50\n"
    "v_lshlrev_b32 v90, 16, v54\n"
    "v_and_b32 v91, 0xffff0000, v54\n"
    "v_lshlrev_b32 v92, 16, v58\n"
    "v_and_b32 v93, 0xffff0000, v58\n"
    "v_lshlrev_b32 v94, 16, v62\n"
    "v_and_b32 v95, 0xffff0000, v62\n"
    "v_pk_fma_f32 v[88:89], v[88:89], v[80:81], v[80:81] op_sel:[0,0,1] op_sel_hi:[1,0,1]\n"
    "v_pk_fma_f32 v[90:91], v[90:91], v[82:83], v[82:83] op_sel:[0,0,1] op_sel_hi:[1,0,1]\n"
    "v_pk_fma_f32 v[92:93], v[92:93], v[84:85], v[84:85] op_sel:[0,0,1] op_sel_hi:[1,0,1]\n"
    "v_pk_fma_f32 v[94:95], v[94:95], v[86:87], v[86:87] op_sel:[0,0,1] op_sel_hi:[1,0,1]\n"
    "v_cvt_pk_bf16_f32 v72, v88, v89\n"
    "v_cvt_pk_bf16_f32 v73, v90, v91\n"
    "v_cvt_pk_bf16_f32 v74, v92, v93\n"
    "v_cvt_pk_bf16_f32 v75, v94, v95\n"
    "buffer_store_dwordx4 v[72:75], v5, s[28:31], s77 offen sc1\n"
    "s_add_u32 s77, s77, 8192\n"
    "v_lshlrev_b32 v88, 16, v51\n"
    "v_and_b32 v89, 0xffff0000, v51\n"
    "v_lshlrev_b32 v90, 16, v55\n"
    "v_and_b32 v91, 0xffff0000, v55\n"
    "v_lshlrev_b32 v92, 16, v59\n"
    "v_and_b32 v93, 0xffff0000, v59\n"
    "v_lshlrev_b32 v94, 16, v63\n"
    "v_and_b32 v95, 0xffff0000, v63\n"
    "v_pk_fma_f32 v[88:89], v[88:89], v[80:81], v[80:81] op_sel:[0,0,1] op_sel_hi:[1,0,1]\n"
    "v_pk_fma_f32 v[90:91], v[90:91], v[82:83], v[82:83] op_sel:[0,0,1] op_sel_hi:[1,0,1]\n"
    "v_pk_fma_f32 v[92:93], v[92:93], v[84:85], v[84:85] op_sel:[0,0,1] op_sel_hi:[1,0,1]\n"
    "v_pk_fma_f32 v[94:95], v[94:95], v[86:87], v[86:87] op_sel:[0,0,1] op_sel_hi:[1,0,1]\n"
    "v_cvt_pk_bf16_f32 v76, v88, v89\n"
    "v_cvt_pk_bf16_f32 v77, v90, v91\n"
    "v_cvt_pk_bf16_f32 v78, v92, v93\n"
    "v_cvt_pk_bf16_f32 v79, v94, v95\n"
    "buffer_store_dwordx4 v[76:79], v5, s[28:31], s77 offen sc1\n"
    "s_add_u32 s77, s77, 106496\n"
    ".La52_%=:\n"
    "v_mov_b32 v8, s32\n"
    "v_mov_b32 v9, s33\n"
    "v_mov_b32 v10, s33\n"
    "v_mov_b32 v11, s32\n"
    "v_lshrrev_b32 v4, 3, %2\n"
    "v_min_u32 v4, 1, v4\n"
    "v_lshlrev_b32 v5, 4, %2\n"
    "v_lshl_add_u32 v4, v4, 28, v5\n"
    "s_lshl_b32 s76, s71, 7\n"
    "s_add_u32 s76, s76, 4096\n"
    "s_waitcnt vmcnt(0)\n"
    "s_barrier\n"
    "buffer_store_dwordx4 v[8:11], v4, s[28:31], s76 offen sc1\n"
    "s_lshl3_add_u32 s76, s72, s4\n"
    "v_lshl_add_u32 v16, s76, 6, v254\n"
    "s_mov_b32 s26, 1605632\n"
    "s_mov_b32 s27, 0x20000\n"
    "s_mov_b32 s77, 9\n"
    "v_add_u32 v16, 3136, v16\n"
    "s_barrier\n"
    "s_waitcnt lgkmcnt(0)\n"
    ".La53_%=:\n"
    "v_mul_u32_u24 v17, 18725, v16\n"
    "v_lshrrev_b32 v17, 22, v17\n"
    "v_mul_u32_u24 v18, 224, v17\n"
    "v_sub_u32 v18, v16, v18\n"
    "v_mul_u32_u24 v19, 4682, v17\n"
    "v_lshrrev_b32 v19, 16, v19\n"
    "v_mul_u32_u24 v20, 14, v19\n"
    "v_sub_u32 v20, v17, v20\n"
    "v_lshl_add_u32 v20, v20, 2, v19\n"
    "v_mul_u32_u24 v20, 224, v20\n"
    "v_add_lshl_u32 v20, v20, v18, 7\n"
    "v_add_u32 v16, 2048, v16\n"
    "v_min_u32 v16, 12543, v16\n"
    "s_sub_u32 s77, s77, 2\n"
    "buffer_load_dword v21, v20, s[24:27], 0 offen\n"
    "s_cbranch_scc0 .La53_%=\n"
    "s_cmp_eq_u32 s71, 0\n"
    "s_cbranch_scc0 .La50_%=\n"
    "v_min_u32 v4, 255, %2\n"
    "v_lshlrev_b32 v4, 7, v4\n"
    "s_mov_b32 s76, 491520\n"
    ".La54_%=:\n"
    "buffer_load_dwordx4 v[8:11], v4, s[28:31], s76 offen sc1\n"
    "s_sub_u32 s65, s65, 16\n"
    "s_waitcnt vmcnt(0)\n"
    "v_cmp_ne_u32 vcc, s32, v8\n"
    "v_cmp_ne_u32 s[78:79], s33, v10\n"
    "s_or_b64 vcc, vcc, s[78:79]\n"
    "s_cbranch_scc0 .La55_%=\n"
    "s_sleep 24\n"
    "s_cmp_lt_i32 s65, 0\n"
    "s_cbranch_scc0 .La54_%=\n"
    ".La55_%=:\n"
    "v_min_u32 v4, 447, %2\n"
    "v_lshlrev_b32 v4, 7, v4\n"
    "s_mov_b32 s76, 524288\n"
    ".La56_%=:\n"
    "buffer_load_dwordx4 v[8:11], v4, s[28:31], s76 offen sc1\n"
    "s_sub_u32 s65, s65, 16\n"
    "s_waitcnt vmcnt(0)\n"
    "v_cmp_ne_u32 vcc, s32, v8\n"
    "v_cmp_ne_u32 s[78:79], s33, v10\n"
    "s_or_b64 vcc, vcc, s[78:79]\n"
    "s_cbranch_scc0 .La57_%=\n"
    "s_sleep 24\n"
    "s_cmp_lt_i32 s65, 0\n"
    "s_cbranch_scc0 .La56_%=\n"
    ".La57_%=:\n"
    "v_min_u32 v4, 447, %2\n"
    "v_lshlrev_b32 v4, 7, v4\n"
    "s_mov_b32 s76, 581632\n"
    ".La58_%=:\n"
    "buffer_load_dwordx4 v[8:11], v4, s[28:31], s76 offen sc1\n"
    "s_sub_u32 s65, s65, 16\n"
    "s_waitcnt vmcnt(0)\n"
    "v_cmp_ne_u32 vcc, s32, v8\n"
    "v_cmp_ne_u32 s[78:79], s33, v10\n"
    "s_or_b64 vcc, vcc, s[78:79]\n"
    "s_cbranch_scc0 .La59_%=\n"
    "s_sleep 24\n"
    "s_cmp_lt_i32 s65, 0\n"
    "s_cbranch_scc0 .La58_%=\n"
    ".La59_%=:\n"
    "v_min_u32 v4, 223, %2\n"
    "v_lshlrev_b32 v4, 7, v4\n"
    "s_mov_b32 s76, 8192\n"
    ".La60_%=:\n"
    "buffer_load_dwordx4 v[8:11], v4, s[28:31], s76 offen sc1\n"
    "s_sub_u32 s65, s65, 16\n"
    "s_waitcnt vmcnt(0)\n"
    "v_cmp_ne_u32 vcc, s32, v8\n"
    "v_cmp_ne_u32 s[78:79], s33, v10\n"
    "s_or_b64 vcc, vcc, s[78:79]\n"
    "s_cbranch_scc0 .La61_%=\n"
    "s_sleep 24\n"
    "s_cmp_lt_i32 s65, 0\n"
    "s_cbranch_scc0 .La60_%=\n"
    ".La61_%=:\n"
    "s_barrier\n"
    "s_cmp_eq_u32 s4, 0\n"
    "s_cbranch_scc0 .La50_%=\n"
    "v_mov_b32 v8, s32\n"
    "v_mov_b32 v9, s33\n"
    "v_mov_b32 v10, s32\n"
    "v_mov_b32 v11, s33\n"
    "v_lshlrev_b32 v4, 4, v254\n"
    "s_mov_b64 exec, 0xff\n"
    "buffer_store_dwordx4 v[8:11], v4, s[28:31], 0 offen sc1\n"
    "s_mov_b64 exec, -1\n"
    ".La50_%=:\n"
    "s_endpgm\n"
    "; RETRY_BEGIN\n"
    "; RETRY_END\n"
    ".Lend_%=:\n"
    :: "s"(__builtin_amdgcn_kernarg_segment_ptr()), "s"(blockIdx.x), "v"(threadIdx.x)
    : "v1", "v2", "v3", "v4", "v5", "v6", "v7", "v8", "v9", "v10", "v11", "v12", "v13", "v14", "v15", "v16", "v17", "v18", "v19", "v20", "v21", "v22", "v23", "v24", "v25", "v26", "v27", "v28", "v29", "v30", "v31", "v32", "v33", "v34", "v35", "v36", "v37", "v38", "v39", "v40", "v41", "v42", "v43", "v44", "v45", "v46", "v47", "v48", "v49", "v50", "v51", "v52", "v53", "v54", "v55", "v56", "v57", "v58", "v59", "v60", "v61", "v62", "v63", "v64", "v65", "v66", "v67", "v68", "v69", "v70", "v71", "v72", "v73", "v74", "v75", "v76", "v77", "v78", "v79", "v80", "v81", "v82", "v83", "v84", "v85", "v86", "v87", "v88", "v89", "v90", "v91", "v92", "v93", "v94", "v95", "v96", "v97", "v98", "v99", "v100", "v101", "v102", "v103", "v104", "v105", "v106", "v107", "v108", "v109", "v110", "v111", "v112", "v113", "v114", "v115", "v116", "v117", "v118", "v119", "v120", "v121", "v122", "v123", "v124", "v125", "v126", "v127", "v128", "v129", "v130", "v131", "v132", "v133", "v134", "v135", "v136", "v137", "v138", "v139", "v140", "v141", "v142", "v143", "v144", "v145", "v146", "v147", "v148", "v149", "v150", "v151", "v152", "v153", "v154", "v155", "v156", "v157", "v158", "v159", "v160", "v161", "v162", "v163", "v164", "v165", "v166", "v167", "v168", "v169", "v170", "v171", "v172", "v173", "v174", "v175", "v176", "v177", "v178", "v179", "v180", "v181", "v182", "v183", "v184", "v185", "v186", "v187", "v188", "v189", "v190", "v191", "v192", "v193", "v194", "v195", "v196", "v197", "v198", "v199", "v200", "v201", "v202", "v203", "v204", "v205", "v206", "v207", "v208", "v209", "v210", "v211", "v212", "v213", "v214", "v215", "v216", "v217", "v218", "v219", "v220", "v221", "v222", "v223", "v224", "v225", "v226", "v227", "v228", "v229", "v230", "v231", "v232", "v233", "v234", "v235", "v236", "v237", "v238", "v239", "v240", "v241", "v242", "v243", "v244", "v245", "v246", "v247", "v248", "v249", "v250", "v251", "v252", "v253", "v254", "v255", "s4", "s5", "s6", "s7", "s8", "s9", "s10", "s11", "s12", "s13", "s14", "s15", "s16", "s17", "s18", "s19", "s20", "s21", "s22", "s23", "s24", "s25", "s26", "s27", "s28", "s29", "s30", "s31", "s32", "s33", "s34", "s35", "s36", "s37", "s38", "s39", "s40", "s41", "s42", "s43", "s44", "s45", "s46", "s47", "s48", "s49", "s50", "s51", "s52", "s53", "s54", "s55", "s56", "s57", "s58", "s59", "s60", "s61", "s62", "s63", "s64", "s65", "s66", "s67", "s68", "s69", "s70", "s71", "s72", "s73", "s74", "s75", "s76", "s77", "s78", "s79", "s80", "s81", "s82", "s83", "s84", "s85", "s86", "s87", "s88", "s89", "s90", "s91", "s92", "s93", "s94", "s95", "s96", "s97", "s98", "s99", "vcc", "scc", "m0", "memory");
#endif
}

extern "C" void kernel_launch(void* const* d_in, const int* in_sizes, int n_in, void* d_out, int out_size, void* d_ws, size_t ws_size, hipStream_t stream) {
  Params P;
  P.x = (const char*)d_in[0]; P.logA = (const char*)d_in[1]; P.Aim = (const char*)d_in[2]; P.Cr = (const char*)d_in[3]; P.Ci = (const char*)d_in[4];
  P.logdt = (const char*)d_in[5]; P.Dskip = (const char*)d_in[6]; P.w1 = (const char*)d_in[8]; P.b1 = (const char*)d_in[9];
  P.w2 = (const char*)d_in[10]; P.b2 = (const char*)d_in[11]; P.out = (char*)d_out; P.ws = (char*)d_ws;
  fused_kernel<<<256, 512, 0, stream>>>(P);
}
```
